```python
import torch

def _softmax(x, axis=-1):
    return torch.softmax(x, dim=axis)

def _gelu(x):
    return torch.nn.functional.gelu(x, approximate='tanh')

def _silu(x):
    return torch.nn.functional.silu(x)

def _sigmoid(x):
    return torch.sigmoid(x)

def _layernorm(x, axis=-1, eps=1e-05):
    m = x.mean(dim=axis, keepdim=True)
    v = x.var(dim=axis, keepdim=True, unbiased=False)
    return (x - m) / torch.sqrt(v + eps)

def _scatter_add(tgt, idx, src, axis=0):
    return tgt.clone().index_add_(axis, idx, src)

def _segment_sum(src, seg, n):
    out = torch.zeros((n,) + src.shape[1:], dtype=src.dtype)
    return out.index_add_(0, seg, src)

def _pad(x, pw):
    flat = tuple((v for pair in reversed(pw) for v in pair))
    return torch.nn.functional.pad(x, flat)
kernel_name = 'standalone__molecular_angular_painn__graph'

def setup_inputs():

    _w = lambda a: (lambda t: t.to(torch.bfloat16) if t.is_floating_point() else t)(torch.as_tensor(a))
    N, deg, D = (12500, 32, 128)
    n_rbf = max(4, 4)
    E = N * deg
    cutoff = 5.0
    freqs = (torch.arange(1, n_rbf + 1) * torch.pi / cutoff).to(float)
    return {'sv': _w((lambda *s: torch.randn(*s, dtype=torch.bfloat16) * 0.4)(N, 4, D) * 0.3), 'pos': _w((lambda *s: torch.randn(*s, dtype=torch.bfloat16) * 3.2)(N, 3) * 1.2), 'src': _w((lambda lo, hi, *s: torch.randint(lo, hi, s))(0, N, E).to(torch.int64)), 'dst': _w((lambda lo, hi, *s: torch.randint(lo, hi, s))(0, N, E).to(torch.int64)), 'freqs': _w(freqs), 'w_phi1': _w((lambda *s: torch.randn(*s, dtype=torch.bfloat16) * 3.2)(D, D) / D ** 0.5), 'w_phi2': _w((lambda *s: torch.randn(*s, dtype=torch.bfloat16) * 3.2)(D, 3 * D) / D ** 0.5), 'w_rbf': _w((lambda *s: torch.randn(*s, dtype=torch.bfloat16) * 3.2)(n_rbf, 3 * D) / n_rbf ** 0.5)}

def reference(sv, pos, src, dst, freqs, w_phi1, w_phi2, w_rbf):
    N, _, D = sv.shape
    E = src.shape[0]
    PI, cutoff = (3.141592653589793, 5.0)
    s, v = (sv[:, 0, :].clone(), sv[:, 1:, :].clone())
    r_vec = pos[src] - pos[dst]
    d = torch.sqrt((r_vec * r_vec).sum(-1) + 1e-08)
    r_hat = r_vec / d[:, None]
    rbf = torch.sin(d[:, None] * freqs[None, :]) / d[:, None]
    fcut = 0.5 * (torch.cos(PI * d / cutoff) + 1.0)
    fcut = torch.where(d < cutoff, fcut, 0.0 * d)
    Wf = rbf @ w_rbf * fcut[:, None]
    phi = (_silu(s @ w_phi1) @ w_phi2)[src]
    m = phi * Wf
    m_s, m_vv, m_vr = (m[:, :D].clone(), m[:, D:2 * D].clone(), m[:, 2 * D:].clone())
    ds = _segment_sum(m_s, dst, N)
    dv_e = m_vv[:, None, :] * v[src] + m_vr[:, None, :] * r_hat[:, :, None]
    dv = _segment_sum(dv_e.reshape(E, 3 * D), dst, N).reshape(N, 3, D)
    return sv + torch.concatenate([ds[:, None, :], dv], axis=1)
```

```cpp
#include <hip/hip_runtime.h>
#include <hip/hip_bf16.h>
#include <stdint.h>

typedef __attribute__((ext_vector_type(8))) short bf16x8;
typedef __attribute__((ext_vector_type(4))) float f32x4;
typedef __attribute__((ext_vector_type(4))) unsigned u32x4;
typedef __attribute__((ext_vector_type(3))) unsigned u32x3;
typedef __attribute__((ext_vector_type(2))) unsigned u32x2;
typedef __attribute__((ext_vector_type(4))) int i32x4;

constexpr int NN   = 12500;
constexpr int NE   = 400000;
constexpr int DD   = 128;
constexpr int T1   = 512;
constexpr int NB_E = 60;
constexpr int EPB  = 6668;
constexpr int GT   = 64;
constexpr int NB_G = (NN + GT - 1) / GT;
constexpr int RN   = 25;
constexpr int NR   = (NN + RN - 1) / RN;
constexpr int OFS  = 512;
constexpr int T2   = 512;
constexpr int CAP  = 1024;
static_assert(NB_E * EPB >= NE, "");
static_assert(NB_E <= 64, "");
static_assert(OFS >= NR + 1, "");

constexpr size_t WS_PHI  = 0;
constexpr size_t WS_RECF = 9600000;
constexpr size_t WS_RECM = WS_RECF + (size_t)NB_E * EPB * 16;
constexpr size_t WS_OFFS = WS_RECM + (size_t)NB_E * EPB * 4;
constexpr size_t WS_ACC  = WS_OFFS + (size_t)NB_E * OFS * 2;
constexpr size_t WS_END  = WS_ACC + (size_t)NN * 512 * 4;

__device__ __forceinline__ float bf2f(unsigned h) { return __uint_as_float(h << 16); }
__device__ __forceinline__ float bflo(unsigned u) { return __uint_as_float(u << 16); }
__device__ __forceinline__ float bfhi(unsigned u) { return __uint_as_float(u & 0xffff0000u); }
__device__ __forceinline__ unsigned f2bf(float x) { __bf16 b = (__bf16)x; return (unsigned)__builtin_bit_cast(unsigned short, b); }
__device__ __forceinline__ float rbf(float x) { return bf2f(f2bf(x)); }
__device__ __forceinline__ unsigned pack2(float lo, float hi) { return f2bf(lo) | (f2bf(hi) << 16); }

struct Pos3 { float x, y, z; };
__device__ __forceinline__ Pos3 lds_pos(const unsigned* posw, int j) {
    const int w = (6 * j) >> 2;
    const unsigned d0 = posw[w], d1 = posw[w + 1];
    const bool odd = j & 1;
    Pos3 p;
    p.x = odd ? bfhi(d0) : bflo(d0);
    p.y = odd ? bflo(d1) : bfhi(d0);
    p.z = odd ? bfhi(d1) : bflo(d1);
    return p;
}

__device__ __forceinline__ void edge_block(int b, const unsigned short* __restrict__ pos, const int* __restrict__ src, const int* __restrict__ dst,
                                           const unsigned short* __restrict__ freqs, char* __restrict__ ws, char* smem) {
    unsigned* posw = (unsigned*)smem;
    unsigned* hist = (unsigned*)(smem + 75008);
    unsigned* ncand = hist + OFS;
    u32x2* list = (u32x2*)(smem + 75008 + OFS * 4 + 16);
    unsigned short* carr = (unsigned short*)((char*)list + EPB * 8);
    const int tid = threadIdx.x, lane = tid & 63;
    const int e0 = b * EPB;
    const int ne = min(EPB, NE - e0);
    {
        const u32x4* g = (const u32x4*)pos;
        for (int i = tid; i < 4687; i += T1) ((u32x4*)posw)[i] = g[i];
        if (tid < 2) posw[18748 + tid] = ((const unsigned*)pos)[18748 + tid];
        if (tid == 2) posw[18750] = 0;
        for (int i = tid; i < OFS; i += T1) hist[i] = 0;
        if (tid == 0) *ncand = 0;
    }
    constexpr int NIT = (EPB / 4 + T1 - 1) / T1;
    i32x4 sv4[NIT], dv4[NIT];
#pragma unroll
    for (int it = 0; it < NIT; ++it) {
        const int q = tid + it * T1;
        if (4 * q < ne) { sv4[it] = *(const i32x4*)(src + e0 + 4 * q); dv4[it] = *(const i32x4*)(dst + e0 + 4 * q); }
        else { sv4[it] = (i32x4){0, 0, 0, 0}; dv4[it] = (i32x4){0, 0, 0, 0}; }
    }
    __syncthreads();
#pragma unroll
    for (int it = 0; it < NIT; ++it) {
        const int q = tid + it * T1;
#pragma unroll
        for (int j = 0; j < 4; ++j) {
            const int s = sv4[it][j], d = dv4[it][j];
            const Pos3 ps = lds_pos(posw, s), pd = lds_pos(posw, d);
            const float rx = rbf(ps.x - pd.x), ry = rbf(ps.y - pd.y), rz = rbf(ps.z - pd.z);
            const float ss = rbf(rbf(rx * rx) + rbf(ry * ry) + rbf(rz * rz));
            const bool act = (4 * q + j < ne) && (!(ss >= 24.0f) || ss > 3.3e38f);
            const unsigned long long m = __ballot(act);
            if (m) {
                unsigned base = 0;
                const int leader = __ffsll((long long)m) - 1;
                if (lane == leader) base = atomicAdd(ncand, (unsigned)__popcll(m));
                base = __shfl(base, leader);
                if (act) {
                    const unsigned slot = atomicAdd(&hist[d / RN], 1u);
                    const unsigned idx = base + __popcll(m & ((1ull << lane) - 1));
                    list[idx] = (u32x2){(unsigned)s | ((unsigned)d << 16), (unsigned)(4 * q + j) | (slot << 16)};
                }
            }
        }
    }
    __syncthreads();
    if (tid < 64) {
        constexpr int PER = OFS / 64;
        unsigned c[PER], sum = 0;
#pragma unroll
        for (int k = 0; k < PER; ++k) { c[k] = hist[lane * PER + k]; sum += c[k]; }
        unsigned inc = sum;
#pragma unroll
        for (int o = 1; o < 64; o <<= 1) { const unsigned t = __shfl_up(inc, o); if (lane >= o) inc += t; }
        unsigned run = inc - sum;
        unsigned short* orow = (unsigned short*)(ws + WS_OFFS) + (size_t)b * OFS;
        unsigned o16[PER];
#pragma unroll
        for (int k = 0; k < PER; ++k) { hist[lane * PER + k] = run; o16[k] = run; run += c[k]; }
        u32x4 pk = {o16[0] | (o16[1] << 16), o16[2] | (o16[3] << 16), o16[4] | (o16[5] << 16), o16[6] | (o16[7] << 16)};
        *(u32x4*)(orow + lane * PER) = pk;
    }
    __syncthreads();
    const int n = *ncand;
    for (int i = tid; i < n; i += T1) {
        const u32x2 en = list[i];
        const int d = en.x >> 16;
        carr[hist[d / RN] + (en.y >> 16)] = (unsigned short)(en.y & 0xffff);
    }
    __syncthreads();
    const float f0 = bf2f(freqs[0]), f1 = bf2f(freqs[1]), f2 = bf2f(freqs[2]), f3 = bf2f(freqs[3]);
    u32x4* recf = (u32x4*)(ws + WS_RECF) + e0;
    unsigned* recm = (unsigned*)(ws + WS_RECM) + e0;
    for (int i = tid; i < n; i += T1) {
        const u32x2 en = list[i];
        const int s = en.x & 0xffff, d = en.x >> 16;
        const unsigned el = en.y & 0xffff;
        const int r = d / RN;
        const unsigned c0 = hist[r], c1 = hist[r + 1];
        unsigned rank = 0;
        for (unsigned k = c0; k < c1; ++k) rank += (carr[k] < el);
        const Pos3 ps = lds_pos(posw, s), pd = lds_pos(posw, d);
        const float rx = rbf(ps.x - pd.x), ry = rbf(ps.y - pd.y), rz = rbf(ps.z - pd.z);
        const float ss = rbf(rbf(rx * rx) + rbf(ry * ry) + rbf(rz * rz));
        const float dd = rbf(sqrtf(rbf(ss + 1e-8f)));
        const float hx = rx / dd, hy = ry / dd, hz = rz / dd;
        const float b0 = rbf(sinf(rbf(dd * f0))) / dd, b1 = rbf(sinf(rbf(dd * f1))) / dd;
        const float b2 = rbf(sinf(rbf(dd * f2))) / dd, b3 = rbf(sinf(rbf(dd * f3))) / dd;
        float fc = rbf(0.5f * rbf(rbf(cosf(rbf(rbf(3.14159265358979f * dd) / 5.0f))) + 1.0f));
        fc = (dd < 5.0f) ? fc : 0.0f * dd;
        u32x4 rec = {pack2(b0, b1), pack2(b2, b3), pack2(fc, hx), pack2(hy, hz)};
        recf[c0 + rank] = rec;
        recm[c0 + rank] = (unsigned)s | ((unsigned)(d - r * RN) << 16);
    }
}

__device__ __forceinline__ unsigned permlo(unsigned a, unsigned b) { return __builtin_amdgcn_perm(b, a, 0x05040100u); }
__device__ __forceinline__ unsigned permhi(unsigned a, unsigned b) { return __builtin_amdgcn_perm(b, a, 0x07060302u); }

__device__ __forceinline__ void gemm_block(int g, const unsigned short* __restrict__ sv, const unsigned short* __restrict__ w1,
                                           const unsigned short* __restrict__ w2, char* __restrict__ ws, char* smem) {
    const int tid = threadIdx.x;
    if (tid >= 256) return;
    const int lane = tid & 63, w = tid >> 6, slot = lane & 15, kg = lane >> 4;
    const int n0 = g * GT;
    u32x4* xl = (u32x4*)smem;
    u32x4* hl = (u32x4*)(smem + 16384);
    {
        const int node = min(n0 + 16 * w + slot, NN - 1);
#pragma unroll
        for (int ks = 0; ks < 4; ++ks)
            xl[(w * 4 + ks) * 64 + lane] = *(const u32x4*)(sv + (size_t)node * 512 + 32 * ks + 8 * kg);
    }
    unsigned w1r[4][8];
    u32x3 w2r[4][8];
#pragma unroll
    for (int ks = 0; ks < 4; ++ks)
#pragma unroll
        for (int i = 0; i < 8; ++i) w1r[ks][i] = *(const unsigned*)(w1 + (32 * ks + 8 * kg + i) * 128 + 32 * w + 2 * slot);
#pragma unroll
    for (int ks = 0; ks < 4; ++ks)
#pragma unroll
        for (int i = 0; i < 8; ++i) w2r[ks][i] = *(const u32x3*)(w2 + (32 * ks + 8 * kg + i) * 384 + 96 * w + 6 * slot);
    bf16x8 a1[2][4];
#pragma unroll
    for (int ks = 0; ks < 4; ++ks) {
        u32x4 lo, hi;
#pragma unroll
        for (int p = 0; p < 4; ++p) { lo[p] = permlo(w1r[ks][2 * p], w1r[ks][2 * p + 1]); hi[p] = permhi(w1r[ks][2 * p], w1r[ks][2 * p + 1]); }
        a1[0][ks] = __builtin_bit_cast(bf16x8, lo); a1[1][ks] = __builtin_bit_cast(bf16x8, hi);
    }
    __syncthreads();
#pragma unroll
    for (int nt = 0; nt < 4; ++nt) {
        f32x4 acc[2] = {{0, 0, 0, 0}, {0, 0, 0, 0}};
#pragma unroll
        for (int ks = 0; ks < 4; ++ks) {
            const bf16x8 bx = __builtin_bit_cast(bf16x8, xl[(nt * 4 + ks) * 64 + lane]);
            acc[0] = __builtin_amdgcn_mfma_f32_16x16x32_bf16(a1[0][ks], bx, acc[0], 0, 0, 0);
            acc[1] = __builtin_amdgcn_mfma_f32_16x16x32_bf16(a1[1][ks], bx, acc[1], 0, 0, 0);
        }
        u32x4 h;
#pragma unroll
        for (int r = 0; r < 4; ++r) {
            float v[2];
#pragma unroll
            for (int t = 0; t < 2; ++t) { const float x = rbf(acc[t][r]); v[t] = x / (1.0f + __expf(-x)); }
            h[r] = pack2(v[0], v[1]);
        }
        hl[(nt * 4 + w) * 64 + lane] = h;
    }
    bf16x8 a2[6][4];
#pragma unroll
    for (int ks = 0; ks < 4; ++ks) {
        u32x4 f[6];
#pragma unroll
        for (int p = 0; p < 4; ++p)
#pragma unroll
            for (int c = 0; c < 3; ++c) { f[2 * c][p] = permlo(w2r[ks][2 * p][c], w2r[ks][2 * p + 1][c]); f[2 * c + 1][p] = permhi(w2r[ks][2 * p][c], w2r[ks][2 * p + 1][c]); }
#pragma unroll
        for (int t = 0; t < 6; ++t) a2[t][ks] = __builtin_bit_cast(bf16x8, f[t]);
    }
    __syncthreads();
    unsigned short* phi = (unsigned short*)(ws + WS_PHI);
#pragma unroll
    for (int nt = 0; nt < 4; ++nt) {
        f32x4 acc[6];
#pragma unroll
        for (int t = 0; t < 6; ++t) acc[t] = (f32x4){0, 0, 0, 0};
#pragma unroll
        for (int ks = 0; ks < 4; ++ks) {
            const bf16x8 bh = __builtin_bit_cast(bf16x8, hl[(nt * 4 + ks) * 64 + lane]);
#pragma unroll
            for (int t = 0; t < 6; ++t) acc[t] = __builtin_amdgcn_mfma_f32_16x16x32_bf16(a2[t][ks], bh, acc[t], 0, 0, 0);
        }
        const int node = n0 + 16 * nt + slot;
        unsigned o[12];
#pragma unroll
        for (int r = 0; r < 4; ++r)
#pragma unroll
            for (int c = 0; c < 3; ++c) o[3 * r + c] = pack2(acc[2 * c][r], acc[2 * c + 1][r]);
        if (node < NN) {
            u32x4* dstp = (u32x4*)(phi + (size_t)node * 384 + 96 * w + 24 * kg);
            dstp[0] = (u32x4){o[0], o[1], o[2], o[3]};
            dstp[1] = (u32x4){o[4], o[5], o[6], o[7]};
            dstp[2] = (u32x4){o[8], o[9], o[10], o[11]};
        }
    }
}

__global__ __launch_bounds__(T1) void k1_prepare(const unsigned short* __restrict__ sv, const unsigned short* __restrict__ pos,
                                                 const int* __restrict__ src, const int* __restrict__ dst,
                                                 const unsigned short* __restrict__ freqs, const unsigned short* __restrict__ w1,
                                                 const unsigned short* __restrict__ w2, char* __restrict__ ws) {
    extern __shared__ __attribute__((aligned(16))) char smem[];
    const int b = blockIdx.x;
    if (b < NB_E) edge_block(b, pos, src, dst, freqs, ws, smem);
    else gemm_block(b - NB_E, sv, w1, w2, ws, smem);
}
constexpr int K1_LDS = 75008 + OFS * 4 + 16 + EPB * 8 + EPB * 2;

__global__ __launch_bounds__(T2) void k2_gather(const unsigned short* __restrict__ sv, const unsigned short* __restrict__ wr,
                                                char* __restrict__ ws, unsigned short* __restrict__ out) {
    extern __shared__ __attribute__((aligned(16))) char smem[];
    f32x4* uf = (f32x4*)smem;
    unsigned* bm = (unsigned*)(smem + CAP * 32);
    unsigned* ctr = bm + RN * 32;
    const int tid = threadIdx.x, lane = tid & 63, wv = __builtin_amdgcn_readfirstlane(tid >> 6);
    const int r = blockIdx.x;
    const int n0 = r * RN, nn = min(RN, NN - n0);
    const unsigned short* offs = (const unsigned short*)(ws + WS_OFFS);
    unsigned cs = 0, cc = 0;
    if (lane < NB_E) { cs = offs[lane * OFS + r]; cc = offs[lane * OFS + r + 1] - cs; }
    unsigned inc = cc;
#pragma unroll
    for (int o = 1; o < 64; o <<= 1) { const unsigned t = __shfl_up(inc, o); if (lane >= o) inc += t; }
    const unsigned cb = inc - cc;
    const int total = __builtin_amdgcn_readlane(inc, 63);
    float wf[3][4][2];
#pragma unroll
    for (int p = 0; p < 3; ++p)
#pragma unroll
        for (int k = 0; k < 4; ++k) { const unsigned u = *(const unsigned*)(wr + k * 384 + p * 128 + 2 * lane); wf[p][k][0] = bflo(u); wf[p][k][1] = bfhi(u); }
    const u32x4* recf = (const u32x4*)(ws + WS_RECF);
    const unsigned* recm = (const unsigned*)(ws + WS_RECM);
    const char* phi = ws + WS_PHI;
    float* accw = (float*)(ws + WS_ACC);
    for (int w0 = 0; w0 == 0 || w0 < total; w0 += CAP) {
        const bool firstw = (w0 == 0), lastw = (w0 + CAP >= total);
        if (!firstw) __syncthreads();
        for (int i = tid; i < RN * 32; i += T2) bm[i] = 0;
        if (tid == 0) *ctr = 0;
        __syncthreads();
        {
            const int b = 8 * wv + (lane >> 3);
            const unsigned s_ = __shfl(cs, b), c_ = __shfl(cc, b), p_ = __shfl(cb, b);
            for (unsigned k = lane & 7; k < c_; k += 8) {
                const int p = (int)(p_ + k) - w0;
                if (p >= 0 && p < CAP) {
                    const size_t gi = (size_t)b * EPB + s_ + k;
                    const u32x4 f = recf[gi];
                    const unsigned m = recm[gi];
                    const float fc = bflo(f.z);
                    uf[2 * p] = (f32x4){bflo(f.x) * fc, bfhi(f.x) * fc, bflo(f.y) * fc, bfhi(f.y) * fc};
                    uf[2 * p + 1] = (f32x4){bfhi(f.z), bflo(f.w), bfhi(f.w), __uint_as_float(m & 0xffff)};
                    atomicOr(&bm[(m >> 16) * 32 + (p >> 5)], 1u << (p & 31));
                }
            }
        }
        __syncthreads();
        for (;;) {
            int nl = 0;
            if (lane == 0) nl = atomicAdd(ctr, 1);
            nl = __builtin_amdgcn_readfirstlane(nl);
            if (nl >= nn) break;
            const int node = n0 + nl;
            const unsigned word = bm[nl * 32 + (lane & 31)];
            unsigned long long nz = __ballot(word != 0) & 0xffffffffull;
            float acc[4][2];
            u32x4 res = {0, 0, 0, 0};
            if (firstw) {
#pragma unroll
                for (int a = 0; a < 4; ++a) acc[a][0] = acc[a][1] = 0.f;
            } else {
#pragma unroll
                for (int a = 0; a < 4; ++a) { acc[a][0] = accw[(size_t)node * 512 + a * 128 + 2 * lane]; acc[a][1] = accw[(size_t)node * 512 + a * 128 + 2 * lane + 1]; }
            }
            if (lastw) {
#pragma unroll
                for (int a = 0; a < 4; ++a) res[a] = *(const unsigned*)(sv + (size_t)node * 512 + a * 128 + 2 * lane);
            }
            unsigned cur = 0; int curw = 0;
            while (nz | cur) {
                int p[4]; int cnt = 0;
#pragma unroll
                for (int j = 0; j < 4; ++j) {
                    p[j] = 0;
                    if (nz | cur) {
                        if (!cur) { curw = __builtin_ctzll(nz); nz &= nz - 1; cur = __builtin_amdgcn_readlane(word, curw); }
                        p[j] = curw * 32 + __builtin_ctz(cur); cur &= cur - 1; cnt = j + 1;
                    }
                }
                f32x4 ga[4], gb[4];
                unsigned ld[4][6];
#pragma unroll
                for (int j = 0; j < 4; ++j) {
                    if (j < cnt) {
                        ga[j] = uf[2 * p[j]]; gb[j] = uf[2 * p[j] + 1];
                        const int s = __builtin_amdgcn_readfirstlane(__float_as_uint(gb[j][3]));
                        const unsigned* pp = (const unsigned*)(phi + (size_t)s * 768) + lane;
                        const unsigned* vp = (const unsigned*)(sv + (size_t)s * 512 + 128) + lane;
                        ld[j][0] = pp[0]; ld[j][1] = pp[64]; ld[j][2] = pp[128];
                        ld[j][3] = vp[0]; ld[j][4] = vp[64]; ld[j][5] = vp[128];
                    }
                }
#pragma unroll
                for (int j = 0; j < 4; ++j) {
                    if (j < cnt) {
#pragma unroll
                        for (int c = 0; c < 2; ++c) {
                            float W[3];
#pragma unroll
                            for (int q = 0; q < 3; ++q) W[q] = ga[j][0] * wf[q][0][c] + ga[j][1] * wf[q][1][c] + ga[j][2] * wf[q][2][c] + ga[j][3] * wf[q][3][c];
                            const float ps = c ? bfhi(ld[j][0]) : bflo(ld[j][0]);
                            const float pv = c ? bfhi(ld[j][1]) : bflo(ld[j][1]);
                            const float pr = c ? bfhi(ld[j][2]) : bflo(ld[j][2]);
                            const float mv = pv * W[1], mr = pr * W[2];
                            acc[0][c] += ps * W[0];
#pragma unroll
                            for (int a = 0; a < 3; ++a) {
                                const float v = c ? bfhi(ld[j][3 + a]) : bflo(ld[j][3 + a]);
                                acc[1 + a][c] += mv * v + mr * gb[j][a];
                            }
                        }
                    }
                }
            }
            if (lastw) {
#pragma unroll
                for (int a = 0; a < 4; ++a)
                    *(unsigned*)(out + (size_t)node * 512 + a * 128 + 2 * lane) = pack2(bflo(res[a]) + rbf(acc[a][0]), bfhi(res[a]) + rbf(acc[a][1]));
            } else {
#pragma unroll
                for (int a = 0; a < 4; ++a) { accw[(size_t)node * 512 + a * 128 + 2 * lane] = acc[a][0]; accw[(size_t)node * 512 + a * 128 + 2 * lane + 1] = acc[a][1]; }
            }
        }
    }
}
constexpr int K2_LDS = CAP * 32 + RN * 32 * 4 + 16;

extern "C" void kernel_launch(void* const* d_in, const int* in_sizes, int n_in, void* d_out, int out_size, void* d_ws, size_t ws_size,
                              hipStream_t stream) {
    const unsigned short* sv = (const unsigned short*)d_in[0];
    const unsigned short* pos = (const unsigned short*)d_in[1];
    const int* src = (const int*)d_in[2];
    const int* dst = (const int*)d_in[3];
    const unsigned short* freqs = (const unsigned short*)d_in[4];
    const unsigned short* w1 = (const unsigned short*)d_in[5];
    const unsigned short* w2 = (const unsigned short*)d_in[6];
    const unsigned short* wr = (const unsigned short*)d_in[7];
    (void)hipFuncSetAttribute((const void*)k1_prepare, hipFuncAttributeMaxDynamicSharedMemorySize, K1_LDS);
    (void)hipFuncSetAttribute((const void*)k2_gather, hipFuncAttributeMaxDynamicSharedMemorySize, K2_LDS);
    k1_prepare<<<NB_E + NB_G, T1, K1_LDS, stream>>>(sv, pos, src, dst, freqs, w1, w2, (char*)d_ws);
    k2_gather<<<NR, T2, K2_LDS, stream>>>(sv, wr, (char*)d_ws, (unsigned short*)d_out);
}
```

```cpp
#include <hip/hip_runtime.h>
#include <hip/hip_bf16.h>
#include <stdint.h>

typedef __attribute__((ext_vector_type(8))) short bf16x8;
typedef __attribute__((ext_vector_type(4))) short s16x4;
typedef __attribute__((ext_vector_type(4))) float f32x4;
typedef __attribute__((ext_vector_type(4))) unsigned u32x4;
typedef __attribute__((ext_vector_type(4), aligned(4))) unsigned u32x4a4;
typedef __attribute__((ext_vector_type(2))) unsigned u32x2;
typedef __attribute__((ext_vector_type(4))) int i32x4;
typedef __attribute__((address_space(3))) void* lds_ptr_t;
typedef const __attribute__((address_space(1))) void* glb_ptr_t;
typedef __attribute__((address_space(3))) s16x4* lds_s16x4_t;

constexpr int NN   = 12500;
constexpr int NE   = 400000;
constexpr int T1   = 1024;
constexpr int EPB  = 4096;
constexpr int NB_E = (NE + EPB - 1) / EPB;
constexpr int GTL  = 5;
constexpr int GT   = 16 * GTL;
constexpr int NB_G = (NN + GT - 1) / GT;
constexpr int RN   = 25;
constexpr int NR   = (NN + RN - 1) / RN;
constexpr int NRQ  = NR / 4;
constexpr int OFS  = 512;
constexpr int T2   = 256;
constexpr int W2   = T2 / 64;
constexpr int CAP  = 512;
static_assert(NB_E <= 128 && T2 == 256 && NR % 4 == 0 && EPB == 4 * T1, "");
static_assert(OFS >= NR + 1 && RN <= 62 && OFS <= T1, "");

constexpr size_t WS_PHI  = 0;
constexpr size_t WS_REC  = 12800000;
constexpr size_t WS_CELL = WS_REC + (size_t)NB_E * EPB * 20;
constexpr size_t WS_ACC  = WS_CELL + (size_t)NR * 1024;
constexpr size_t WS_END  = WS_ACC + (size_t)NN * 512 * 4;
static_assert(WS_CELL % 128 == 0 && WS_ACC % 128 == 0, "");
#define STAMP(kid, slot) do {} while (0)

__device__ __forceinline__ float bf2f(unsigned h) { return __uint_as_float(h << 16); }
__device__ __forceinline__ float bflo(unsigned u) { return __uint_as_float(u << 16); }
__device__ __forceinline__ float bfhi(unsigned u) { return __uint_as_float(u & 0xffff0000u); }
typedef __attribute__((ext_vector_type(2))) __bf16 bf16x2_t;
typedef __attribute__((ext_vector_type(2))) float f32x2_t;
__device__ __forceinline__ unsigned pack2(float lo, float hi) { f32x2_t f = {lo, hi}; bf16x2_t b = __builtin_convertvector(f, bf16x2_t); return __builtin_bit_cast(unsigned, b); }
__device__ __forceinline__ float rbf(float x) { return bflo(pack2(x, 0.f)); }

__device__ __forceinline__ unsigned wave_incl_scan(unsigned x) {
    x += __builtin_amdgcn_update_dpp(0u, x, 0x111, 0xf, 0xf, false);
    x += __builtin_amdgcn_update_dpp(0u, x, 0x112, 0xf, 0xf, false);
    x += __builtin_amdgcn_update_dpp(0u, x, 0x114, 0xf, 0xf, false);
    x += __builtin_amdgcn_update_dpp(0u, x, 0x118, 0xf, 0xf, false);
    x += __builtin_amdgcn_update_dpp(0u, x, 0x142, 0xa, 0xf, false);
    x += __builtin_amdgcn_update_dpp(0u, x, 0x143, 0xc, 0xf, false);
    return x;
}
__device__ __forceinline__ unsigned lanes_below(unsigned long long m) {
    return __builtin_amdgcn_mbcnt_hi((unsigned)(m >> 32), __builtin_amdgcn_mbcnt_lo((unsigned)m, 0u));
}
__device__ __forceinline__ void dma16(const void* g, void* l) { __builtin_amdgcn_global_load_lds((glb_ptr_t)g, (lds_ptr_t)l, 16, 0, 0); }
__device__ __forceinline__ void dma4(const void* g, void* l) { __builtin_amdgcn_global_load_lds((glb_ptr_t)g, (lds_ptr_t)l, 4, 0, 0); }
__device__ __forceinline__ void dma16_asm(const void* g, unsigned lds_addr) {
    unsigned keep;
    asm volatile("s_mov_b32 %0, m0\n\ts_mov_b32 m0, %2\n\ts_nop 0\n\tglobal_load_lds_dwordx4 %1, off\n\ts_mov_b32 m0, %0" : "=&s"(keep) : "v"(g), "s"(lds_addr) : "memory");
}
__device__ __forceinline__ void raw_barrier() { asm volatile("s_waitcnt lgkmcnt(0)\n\ts_barrier" ::: "memory"); }
__device__ __forceinline__ void store16_wt(void* p, u32x4 v) { asm volatile("global_store_dwordx4 %0, %1, off sc1\n\ts_nop 1" :: "v"(p), "v"(v) : "memory"); }

__device__ __forceinline__ float rev_frac(float x) {
    const float C_HI = 0.15915494f, C_LO = (float)(0.15915494309189535 - (double)0.15915494f);
    const float r = x * C_HI;
    const float e = __builtin_fmaf(x, C_HI, -r) + x * C_LO;
    return __builtin_amdgcn_fractf(r) + e;
}
__device__ __forceinline__ float fsin(float x) { return __builtin_amdgcn_sinf(rev_frac(x)); }
__device__ __forceinline__ float fcos(float x) { return __builtin_amdgcn_cosf(rev_frac(x)); }

struct Pos3 { float x, y, z; };
__device__ __forceinline__ Pos3 unpack_pos(unsigned d0, unsigned d1, unsigned j) {
    const unsigned sh = (j & 1) << 4;
    const unsigned xy = __builtin_amdgcn_alignbit(d1, d0, sh);
    Pos3 p;
    p.x = bflo(xy); p.y = bfhi(xy); p.z = __uint_as_float((d1 >> sh) << 16);
    return p;
}
__device__ __forceinline__ Pos3 lds_pos(const unsigned* posw, unsigned j) { const unsigned w = (3 * j) >> 1; return unpack_pos(posw[w], posw[w + 1], j); }

struct Rec { u32x4 f; bool act; };
__device__ __forceinline__ Rec edge_record(const unsigned* posw, unsigned s, unsigned d, float f0, float f1, float f2, float f3) {
    const Pos3 ps = lds_pos(posw, s), pd = lds_pos(posw, d);
    const float rx = rbf(ps.x - pd.x), ry = rbf(ps.y - pd.y), rz = rbf(ps.z - pd.z);
    const float ss = rbf(rbf(rx * rx) + rbf(ry * ry) + rbf(rz * rz));
    const float dd = rbf(__builtin_amdgcn_sqrtf(rbf(ss + 1e-8f)));
    float fc = rbf(0.5f * rbf(rbf(fcos(rbf(rbf(3.14159265358979f * dd) * 0.2f))) + 1.0f));
    fc = (dd < 5.0f) ? fc : 0.0f * dd;
    const float inv = __builtin_amdgcn_rcpf(dd);
    const float b0 = rbf(fsin(rbf(dd * f0))) * inv, b1 = rbf(fsin(rbf(dd * f1))) * inv;
    const float b2 = rbf(fsin(rbf(dd * f2))) * inv, b3 = rbf(fsin(rbf(dd * f3))) * inv;
    Rec r;
    r.f = (u32x4){pack2(b0, b1), pack2(b2, b3), pack2(fc, rx * inv), pack2(ry * inv, rz * inv)};
    r.act = !(fc == 0.0f);
    return r;
}
__device__ __forceinline__ void store_record(char* ws, unsigned gi, u32x4 f, unsigned m) {
    char* p = ws + WS_REC + (size_t)gi * 20;
    *(u32x4a4*)p = f;
    *(unsigned*)(p + 16) = m;
}

constexpr int L1_HIST = 75008;
constexpr int L1_LIST = L1_HIST + OFS * 4 + 16;
constexpr int L1_CARR = L1_LIST + EPB * 8;
constexpr int L1_EDGE_END = L1_CARR + EPB * 2;

__device__ __forceinline__ void edge_block(int b, const unsigned short* __restrict__ pos, const int* __restrict__ src, const int* __restrict__ dst,
                                           const unsigned short* __restrict__ freqs, char* __restrict__ ws, char* smem) {
    unsigned* posw = (unsigned*)smem;
    unsigned* hist = (unsigned*)(smem + L1_HIST);
    unsigned* ncand = hist + OFS;
    u32x2* list = (u32x2*)(smem + L1_LIST);
    unsigned short* carr = (unsigned short*)(smem + L1_CARR);
    const int tid = threadIdx.x, lane = tid & 63, wv = __builtin_amdgcn_readfirstlane(tid >> 6);
    const int e0 = b * EPB;
    const int ne = min(EPB, NE - e0);
    STAMP(0, 0);
    const int q4 = min(tid, ne / 4 - 1);
    const i32x4 sv4 = __builtin_nontemporal_load((const i32x4*)(src + e0) + q4);
    const i32x4 dv4 = __builtin_nontemporal_load((const i32x4*)(dst + e0) + q4);
#pragma unroll
    for (int i = 0; i < 5; ++i) { const int p = wv + 16 * i; if (p < 73) dma16((const char*)pos + p * 1024 + lane * 16, smem + p * 1024); }
    if (wv == 15 && lane < 62) dma4((const char*)pos + 73 * 1024 + lane * 4, smem + 73 * 1024);
    const unsigned fq0 = *(const unsigned*)freqs, fq1 = *(const unsigned*)(freqs + 2);
    if (tid < OFS) hist[tid] = 0;
    if (tid == 0) { *ncand = 0; posw[18750] = 0; }
    __syncthreads();
    STAMP(0, 1);
    {
        unsigned pw[4][4];
#pragma unroll
        for (int j = 0; j < 4; ++j) {
            const unsigned ws_ = (3 * (unsigned)sv4[j]) >> 1, wd_ = (3 * (unsigned)dv4[j]) >> 1;
            pw[j][0] = posw[ws_]; pw[j][1] = posw[ws_ + 1]; pw[j][2] = posw[wd_]; pw[j][3] = posw[wd_ + 1];
        }
        unsigned long long m[4];
        unsigned total = 0;
#pragma unroll
        for (int j = 0; j < 4; ++j) {
            const Pos3 ps = unpack_pos(pw[j][0], pw[j][1], sv4[j]), pd = unpack_pos(pw[j][2], pw[j][3], dv4[j]);
            const float rx = ps.x - pd.x, ry = ps.y - pd.y, rz = ps.z - pd.z;
            const float ss = rx * rx + ry * ry + rz * rz;
            const bool act = (4 * tid < ne) & (!(ss >= 26.0f) | (ss > 1e37f));
            m[j] = __ballot(act);
            total += __popcll(m[j]);
        }
        unsigned run = 0;
        if (lane == 0) run = atomicAdd(ncand, total);
        run = __builtin_amdgcn_readfirstlane(run);
#pragma unroll
        for (int j = 0; j < 4; ++j) {
            if ((m[j] >> lane) & 1) list[run + lanes_below(m[j])] = (u32x2){(unsigned)sv4[j] | ((unsigned)dv4[j] << 16), (unsigned)(4 * tid + j)};
            run += __popcll(m[j]);
        }
    }
    __syncthreads();
    STAMP(0, 2);
    const int n = *ncand;
    const float f0 = bflo(fq0), f1 = bfhi(fq0), f2 = bflo(fq1), f3 = bfhi(fq1);
    u32x2 en = list[tid];
    const bool valid = tid < n;
    en.x = valid ? en.x : 0u;
    Rec rc = edge_record(posw, en.x & 0xffff, en.x >> 16, f0, f1, f2, f3);
    rc.act &= valid;
    const unsigned rg = (en.x >> 16) / RN;
    unsigned slot = 0;
    if (rc.act) slot = atomicAdd(&hist[rg], 1u);
    for (int i = tid + T1; i < n; i += T1) {
        const u32x2 e = list[i];
        const Rec r = edge_record(posw, e.x & 0xffff, e.x >> 16, f0, f1, f2, f3);
        list[i].y = e.y | ((r.act ? atomicAdd(&hist[(e.x >> 16) / RN], 1u) : 0xffffu) << 16);
    }
    __syncthreads();
    STAMP(0, 3);
    if (wv == 0) {
        constexpr int PER = OFS / 64;
        const u32x4 ca = *(const u32x4*)(hist + lane * PER), cb = *(const u32x4*)(hist + lane * PER + 4);
        unsigned c[PER] = {ca[0], ca[1], ca[2], ca[3], cb[0], cb[1], cb[2], cb[3]}, sum = 0;
#pragma unroll
        for (int k = 0; k < PER; ++k) sum += c[k];
        unsigned run = wave_incl_scan(sum) - sum;
        unsigned o[PER];
#pragma unroll
        for (int k = 0; k < PER; ++k) { o[k] = run; run += c[k]; }
        *(u32x4*)(hist + lane * PER) = (u32x4){o[0], o[1], o[2], o[3]};
        *(u32x4*)(hist + lane * PER + 4) = (u32x4){o[4], o[5], o[6], o[7]};
    }
    __syncthreads();
    const unsigned c0 = hist[rg], c1 = hist[rg + 1];
    if (rc.act) carr[c0 + slot] = (unsigned short)en.y;
    for (int i = tid + T1; i < n; i += T1) {
        const u32x2 e = list[i];
        if ((e.y >> 16) != 0xffff) carr[hist[(e.x >> 16) / RN] + (e.y >> 16)] = (unsigned short)(e.y & 0xffff);
    }
    if (tid < NR) {
        const unsigned a0 = hist[tid], a1 = hist[tid + 1];
        ((unsigned*)(ws + WS_CELL))[tid * 256 + (b & 7) * 32 + (b >> 3)] = a0 | ((a1 - a0) << 16);
    }
    __syncthreads();
    STAMP(0, 4);
    {
        unsigned rank = 0;
        unsigned cv[4];
#pragma unroll
        for (int q = 0; q < 4; ++q) cv[q] = carr[min(c0 + q, (unsigned)EPB - 1)];
#pragma unroll
        for (int q = 0; q < 4; ++q) rank += (c0 + q < c1) & (cv[q] < en.y);
        if (rc.act) {
            for (unsigned q = c0 + 4; q < c1; ++q) rank += (carr[q] < en.y);
            const unsigned d = en.x >> 16;
            store_record(ws, e0 + c0 + rank, rc.f, (en.x & 0xffff) | ((d - rg * RN) << 16));
        }
    }
    for (int i = tid + T1; i < n; i += T1) {
        const u32x2 e = list[i];
        if ((e.y >> 16) == 0xffff) continue;
        const unsigned s = e.x & 0xffff, d = e.x >> 16, el = e.y & 0xffff, r = d / RN;
        const unsigned a0 = hist[r], a1 = hist[r + 1];
        unsigned rank = 0;
        for (unsigned q = a0; q < a1; ++q) rank += (carr[q] < el);
        const Rec rr = edge_record(posw, s, d, f0, f1, f2, f3);
        store_record(ws, e0 + a0 + rank, rr.f, s | ((d - r * RN) << 16));
    }
    STAMP(0, 5);
}

constexpr int L1_W1 = 0;
constexpr int L1_W2 = 32768;
constexpr int L1_X  = L1_W2 + 98304;
constexpr int L1_H  = 0;
constexpr int L1_O  = L1_W2;
constexpr int OPITCH = 784;
constexpr int L1_GEMM_END = L1_X + GTL * 4096;
constexpr int K1_LDS = L1_GEMM_END > L1_EDGE_END ? L1_GEMM_END : L1_EDGE_END;
static_assert(GT * OPITCH <= 98304, "");

__device__ __forceinline__ s16x4 tr_read(const char* p) { return __builtin_amdgcn_ds_read_tr16_b64_v4i16((lds_s16x4_t)p); }
__device__ __forceinline__ bf16x8 cat8(s16x4 a, s16x4 b) { return __builtin_shufflevector(a, b, 0, 1, 2, 3, 4, 5, 6, 7); }

__device__ __forceinline__ void gemm_block(int g, const unsigned short* __restrict__ sv, const unsigned short* __restrict__ w1,
                                           const unsigned short* __restrict__ w2, char* __restrict__ ws, char* smem) {
    const int tid = threadIdx.x;
    const int lane = tid & 63, wv = __builtin_amdgcn_readfirstlane(tid >> 6), slot = lane & 15, q = lane >> 4;
    const int w = wv & 7, ng = wv >> 3;
    const int n0 = g * GT;
    STAMP(0, 0);
    const unsigned lbase = (unsigned)(size_t)(lds_ptr_t)smem;
#pragma unroll
    for (int i = 0; i < 2; ++i) {
        const int p = wv + 16 * i;
        if (p < GTL * 4) { const int node = min(n0 + 16 * (p >> 2) + slot, NN - 1); dma16_asm(sv + (size_t)node * 512 + 32 * (p & 3) + 8 * q, lbase + L1_X + p * 1024); }
    }
#pragma unroll
    for (int i = 0; i < 2; ++i) {
        const int p = wv + 16 * i, row = 4 * p + q;
        const int sw = (row & 3) | (((row >> 3) & 1) << 2);
        const int ch = (((slot >> 1) ^ sw) << 1) | (slot & 1);
        dma16_asm((const char*)w1 + row * 256 + ch * 16, lbase + L1_W1 + p * 1024);
    }
#pragma unroll
    for (int i = 0; i < 6; ++i) {
        const int p = wv + 16 * i, c = 64 * p + lane, row = c / 48, cp = c - 48 * row, P = cp >> 1;
        const int ch = (((P & ~7) | ((P & 7) ^ (row & 7))) << 1) | (cp & 1);
        dma16_asm((const char*)w2 + row * 768 + ch * 16, lbase + L1_W2 + p * 1024);
    }
    const int qq = slot >> 2, p4 = slot & 3;
    asm volatile("s_waitcnt vmcnt(6)\n\ts_barrier" ::: "memory");
    STAMP(0, 1);
    constexpr int NT = (GTL + 1) / 2;
    bf16x8 a1[4];
    {
        const char* base = smem + L1_W1 + (8 * q + qq) * 256 + (((w ^ (qq | ((q & 1) << 2))) << 1) | (p4 >> 1)) * 16 + 8 * (p4 & 1);
#pragma unroll
        for (int ks = 0; ks < 4; ++ks) a1[ks] = cat8(tr_read(base + ks * 8192), tr_read(base + ks * 8192 + 1024));
    }
    u32x2 hv[NT];
    {
        u32x4 xb[NT][4];
#pragma unroll
        for (int i = 0; i < NT; ++i) {
            const int nt = min(ng + 2 * i, GTL - 1);
#pragma unroll
            for (int ks = 0; ks < 4; ++ks) xb[i][ks] = *(const u32x4*)(smem + L1_X + (nt * 4 + ks) * 1024 + lane * 16);
        }
        f32x4 acc[NT];
#pragma unroll
        for (int i = 0; i < NT; ++i) acc[i] = (f32x4){0, 0, 0, 0};
#pragma unroll
        for (int ks = 0; ks < 4; ++ks)
#pragma unroll
            for (int i = 0; i < NT; ++i) acc[i] = __builtin_amdgcn_mfma_f32_16x16x32_bf16(a1[ks], __builtin_bit_cast(bf16x8, xb[i][ks]), acc[i], 0, 0, 0);
#pragma unroll
        for (int i = 0; i < NT; ++i) {
            float v[4];
            const unsigned x01 = pack2(acc[i][0], acc[i][1]), x23 = pack2(acc[i][2], acc[i][3]);
            const float x[4] = {bflo(x01), bfhi(x01), bflo(x23), bfhi(x23)};
#pragma unroll
            for (int r = 0; r < 4; ++r) v[r] = x[r] * __builtin_amdgcn_rcpf(1.0f + __builtin_amdgcn_exp2f(-1.44269504f * x[r]));
            hv[i] = (u32x2){pack2(v[0], v[1]), pack2(v[2], v[3])};
        }
    }
    raw_barrier();
#pragma unroll
    for (int i = 0; i < NT; ++i) { const int nt = ng + 2 * i; if (nt < GTL) *(u32x2*)(smem + L1_H + (nt * 4 + (w >> 1)) * 1024 + lane * 16 + (w & 1) * 8) = hv[i]; }
    asm volatile("s_waitcnt vmcnt(0) lgkmcnt(0)\n\ts_barrier" ::: "memory");
    STAMP(0, 2);
    bf16x8 a2[3][4];
#pragma unroll
    for (int t = 0; t < 3; ++t) {
        const int P = 3 * w + t, row0 = 4 * q + qq;
        const char* base = smem + L1_W2 + row0 * 768 + ((P & ~7) | ((P & 7) ^ (row0 & 7))) * 32 + (p4 >> 1) * 16 + 8 * (p4 & 1);
#pragma unroll
        for (int ks = 0; ks < 4; ++ks) a2[t][ks] = cat8(tr_read(base + ks * 24576), tr_read(base + ks * 24576 + 12288));
    }
    u32x4 hb[4];
#pragma unroll
    for (int ks = 0; ks < 4; ++ks) hb[ks] = *(const u32x4*)(smem + L1_H + (ng * 4 + ks) * 1024 + lane * 16);
    raw_barrier();
    char* phi = ws + WS_PHI;
#pragma unroll
    for (int i = 0; i < NT; ++i) {
        const int nt = ng + 2 * i;
        f32x4 acc[3] = {{0, 0, 0, 0}, {0, 0, 0, 0}, {0, 0, 0, 0}};
#pragma unroll
        for (int ks = 0; ks < 4; ++ks)
#pragma unroll
            for (int t = 0; t < 3; ++t) acc[t] = __builtin_amdgcn_mfma_f32_16x16x32_bf16(a2[t][ks], __builtin_bit_cast(bf16x8, hb[ks]), acc[t], 0, 0, 0);
        if (i + 1 < NT) {
            const int nn_ = min(nt + 2, GTL - 1);
#pragma unroll
            for (int ks = 0; ks < 4; ++ks) hb[ks] = *(const u32x4*)(smem + L1_H + (nn_ * 4 + ks) * 1024 + lane * 16);
        }
        if (nt < GTL) {
#pragma unroll
            for (int t = 0; t < 3; ++t)
                *(u32x2*)(smem + L1_O + (16 * nt + slot) * OPITCH + (48 * w + 16 * t + 4 * q) * 2) = (u32x2){pack2(acc[t][0], acc[t][1]), pack2(acc[t][2], acc[t][3])};
        }
        raw_barrier();
#pragma unroll
        for (int k = 0; k < 2; ++k) {
            const int p = wv + 16 * k;
            const int c = 64 * p + lane, row = c / 48, cp = c - 48 * row;
            const int nl = 32 * i + row;
            if (p < 24 && nl < GT && n0 + nl < NN) store16_wt(phi + (size_t)(n0 + nl) * 1024 + cp * 16, *(const u32x4*)(smem + L1_O + nl * OPITCH + cp * 16));
        }
    }
    STAMP(0, 5);
}

__global__ __launch_bounds__(T1) void k1_prepare(const unsigned short* __restrict__ sv, const unsigned short* __restrict__ pos,
                                                 const int* __restrict__ src, const int* __restrict__ dst,
                                                 const unsigned short* __restrict__ freqs, const unsigned short* __restrict__ w1,
                                                 const unsigned short* __restrict__ w2, char* __restrict__ ws) {
    extern __shared__ __attribute__((aligned(16))) char smem[];
    const int b = blockIdx.x;
    if (b < NB_E) edge_block(b, pos, src, dst, freqs, ws, smem);
    else gemm_block(b - NB_E, sv, w1, w2, ws, smem);
}

constexpr int NCH     = CAP / 64;
constexpr int NITMAX  = CAP / 2 + RN;
constexpr int L2_U    = 0;
constexpr int L2_RES  = L2_U + (CAP + 1) * 32;
constexpr int L2_CNT  = L2_RES + (RN + 1) * 512;
constexpr int L2_SLOT = L2_CNT + 32 * NCH * 2;
constexpr int L2_MISC = L2_SLOT + (NITMAX + 1) * 16;
constexpr int K2_LDS  = L2_MISC + 16;
static_assert(L2_U == 0 && L2_RES % 16 == 0 && L2_CNT % 16 == 0 && L2_SLOT % 16 == 0 && NCH == 8 && RN <= 31 && CAP * 32 <= 0x4000, "");
constexpr unsigned PAD_A = CAP * 32, PAD_B = 0x3fffu << 10;
typedef __attribute__((ext_vector_type(2))) float f32x2;
typedef __attribute__((address_space(3))) const u32x2* lds_u32x2_t;
typedef __attribute__((address_space(3))) const f32x4* lds_f32x4_t;
typedef __attribute__((address_space(3))) const unsigned* lds_u32_t;
__device__ __forceinline__ u32x2 lds_rd64(unsigned a) { return *(lds_u32x2_t)(size_t)a; }
__device__ __forceinline__ f32x4 lds_rd128(unsigned a) { return *(lds_f32x4_t)(size_t)a; }
__device__ __forceinline__ unsigned lds_rd32(unsigned a) { return *(lds_u32_t)(size_t)a; }

struct K2Ctx {
    char* smem; char* ws;
    __amdgpu_buffer_rsrc_t rs_phi, rs_sv, rs_out;
    int tid, lane, wv, h, n0, nn, hh, c2;
    unsigned lo;
};

__device__ __forceinline__ void put_record(char* smem, int p, u32x4 f, unsigned mm) {
    const float fc = bflo(f.z);
    *(f32x4*)(smem + L2_U + p * 32) = (f32x4){bfhi(f.z), bflo(f.w), bfhi(f.w), __uint_as_float(mm)};
    *(u32x2*)(smem + L2_U + p * 32 + 16) = (u32x2){pack2(bflo(f.x) * fc, bfhi(f.x) * fc), pack2(bflo(f.y) * fc, bfhi(f.y) * fc)};
}
__device__ __forceinline__ void put_record_from(char* smem, const char* ws, int p, unsigned gi) {
    const char* rp = ws + WS_REC + (size_t)gi * 20;
    put_record(smem, p, *(const u32x4a4*)rp, *(const unsigned*)(rp + 16));
}
__device__ __forceinline__ unsigned sum2(unsigned x, unsigned c) { return (x & 0xffff) + (x >> 16) + c; }

__device__ __forceinline__ void build_slots(const K2Ctx& c, int m, int& ta, int& tb) {
    unsigned short* cnt16 = (unsigned short*)(c.smem + L2_CNT);
    u32x2* slots = (u32x2*)(c.smem + L2_SLOT);
    unsigned mm[NCH / W2], rk[NCH / W2];
#pragma unroll
    for (int i = 0; i < NCH / W2; ++i) {
        const int p = 64 * (c.wv + W2 * i) + c.lane;
        if (64 * (c.wv + W2 * i) < m) {
            const bool valid = p < m;
            mm[i] = lds_rd32(L2_U + min(p, CAP) * 32 + 12);
            const unsigned nl = mm[i] >> 16;
            unsigned long long mk = __ballot(valid);
#pragma unroll
            for (int bit = 0; bit < 5; ++bit) { const unsigned long long bb = __ballot((nl >> bit) & 1); mk &= ((nl >> bit) & 1) ? bb : ~bb; }
            rk[i] = lanes_below(mk);
            if (valid && rk[i] == 0) cnt16[nl * NCH + c.wv + W2 * i] = __popcll(mk);
        }
    }
    raw_barrier();
    const u32x4 cv = *(const u32x4*)(cnt16 + (c.lane & 31) * NCH);
    const unsigned cnt = sum2(cv[0], sum2(cv[1], sum2(cv[2], sum2(cv[3], 0))));
    const unsigned its = c.lane < c.nn ? max(1u, (cnt + 1) >> 1) : 0u;
    const unsigned inc = wave_incl_scan(its), st = inc - its;
    const unsigned I = __builtin_amdgcn_readlane(inc, 63);
    if (c.wv == 0) {
        if (c.lane < c.nn) {
            const u32x2 pe = {PAD_A | (c.lane + 1), PAD_B};
            if ((cnt & 1) | (cnt == 0)) slots[2 * inc - 1] = pe;
            if (cnt == 0) slots[2 * st] = pe;
        } else if (c.lane >= 62) slots[2 * NITMAX + c.lane - 62] = (u32x2){PAD_A, PAD_B};
    }
#pragma unroll
    for (int i = 0; i < NCH / W2; ++i) {
        const int ch = c.wv + W2 * i;
        if (64 * ch < m) {
            unsigned below = 0;
#pragma unroll
            for (int d = 0; d < 4; ++d) {
                const unsigned msk = ch >= 2 * d + 2 ? 0xffffffffu : (ch == 2 * d + 1 ? 0x0000ffffu : 0u);
                below = sum2(cv[d] & msk, below);
            }
            const unsigned nl = mm[i] >> 16;
            const unsigned a = __shfl((2 * st + below) | (below << 12) | (its << 22), nl);
            const unsigned rank = ((a >> 12) & 1023) + rk[i];
            const unsigned e = ((rank >> 1) + 1 == (a >> 22)) ? nl + 1 : 0u;
            const int p = 64 * ch + c.lane;
            if (p < m) slots[(a & 4095) + rk[i]] = (u32x2){(unsigned)p * 32 | e, (mm[i] & 0xffff) << 10};
        }
    }
    const unsigned lo = c.wv * I / W2, hi = (c.wv + 1) * I / W2;
    const unsigned long long ma = __ballot(st >= lo && c.lane < c.nn), mb = __ballot(st >= hi && c.lane < c.nn);
    ta = ma ? __builtin_amdgcn_readlane(st, __builtin_ctzll(ma)) : I;
    tb = mb ? __builtin_amdgcn_readlane(st, __builtin_ctzll(mb)) : I;
}

__device__ __forceinline__ float vmul(float a, float b) { float d = a * b; asm("" : "+v"(d)); return d; }
__device__ __forceinline__ float vfma(float a, float b, float c) { float d = __builtin_fmaf(a, b, c); asm("" : "+v"(d)); return d; }
__device__ __forceinline__ f32x2 unpack2(unsigned u) { return (f32x2){bflo(u), bfhi(u)}; }
__device__ __forceinline__ void edge_fma(f32x2 (&acc)[4], const float (&W)[3][2], const unsigned (&ld)[6], f32x4 hat) {
    const f32x2 mv = {vmul(bflo(ld[1]), W[1][0]), vmul(bfhi(ld[1]), W[1][1])};
    const f32x2 mr = {vmul(bflo(ld[2]), W[2][0]), vmul(bfhi(ld[2]), W[2][1])};
    acc[0] = (f32x2){vfma(bflo(ld[0]), W[0][0], acc[0][0]), vfma(bfhi(ld[0]), W[0][1], acc[0][1])};
#pragma unroll
    for (int a = 0; a < 3; ++a) {
        acc[1 + a] = __builtin_elementwise_fma(mv, unpack2(ld[3 + a]), acc[1 + a]);
        acc[1 + a] = __builtin_elementwise_fma(mr, (f32x2){hat[a], hat[a]}, acc[1 + a]);
    }
}
__device__ __forceinline__ f32x4 filter4(u32x2 g, u32x2 wb) {
    return __builtin_amdgcn_mfma_f32_4x4x4bf16_1k(__builtin_bit_cast(s16x4, g), __builtin_bit_cast(s16x4, wb), (f32x4){0.f, 0.f, 0.f, 0.f}, 0, 0, 0);
}
__device__ __forceinline__ void half_reduce(const f32x2 (&acc)[4], f32x2 (&tot)[2]) {
#pragma unroll
    for (int q = 0; q < 2; ++q) {
        const auto s0 = __builtin_amdgcn_permlane32_swap(__float_as_uint(acc[q][0]), __float_as_uint(acc[q + 2][0]), false, false);
        const auto s1 = __builtin_amdgcn_permlane32_swap(__float_as_uint(acc[q][1]), __float_as_uint(acc[q + 2][1]), false, false);
        tot[q] = (f32x2){__uint_as_float(s0[0]), __uint_as_float(s1[0])} + (f32x2){__uint_as_float(s0[1]), __uint_as_float(s1[1])};
    }
}
__device__ __forceinline__ void store_rows(const K2Ctx& c, int nl, const f32x2 (&tot)[2]) {
    const unsigned ra = L2_RES + nl * 512 + c.hh * 256 + c.c2 * 4;
#pragma unroll
    for (int q = 0; q < 2; ++q) {
        const f32x2 o = unpack2(lds_rd32(ra + q * 128)) + tot[q];
        __builtin_amdgcn_raw_buffer_store_b32(pack2(o[0], o[1]), c.rs_out, c.hh * 512 + c.lo + q * 256, (c.n0 + nl) * 1024,   2);
    }
}

struct Batch { unsigned ld[4][6]; unsigned sa[4]; unsigned ga; };
__device__ __forceinline__ void batch_load(Batch& B, const K2Ctx& c, int t4, int tb) {
    unsigned off[4];
#pragma unroll
    for (int j = 0; j < 4; ++j) {
        const int t = (t4 + j < tb) ? t4 + j : NITMAX;
        const u32x2 x = lds_rd64(L2_SLOT + t * 16 + c.hh * 8);
        B.sa[j] = x[0]; off[j] = x[1] | c.lo;
    }
    { const int t = t4 + (c.lane & 3); B.ga = lds_rd32(L2_SLOT + (t < tb ? t : NITMAX) * 16 + c.hh * 8); }
#pragma unroll
    for (int j = 0; j < 4; ++j) {
        B.ld[j][0] = __builtin_amdgcn_raw_buffer_load_b32(c.rs_phi, off[j], 0, 0);
        B.ld[j][1] = __builtin_amdgcn_raw_buffer_load_b32(c.rs_phi, off[j] + 256, 0, 0);
        B.ld[j][2] = __builtin_amdgcn_raw_buffer_load_b32(c.rs_phi, off[j] + 512, 0, 0);
        B.ld[j][3] = __builtin_amdgcn_raw_buffer_load_b32(c.rs_sv, off[j] + 256, 0, 0);
        B.ld[j][4] = __builtin_amdgcn_raw_buffer_load_b32(c.rs_sv, off[j] + 512, 0, 0);
        B.ld[j][5] = __builtin_amdgcn_raw_buffer_load_b32(c.rs_sv, off[j] + 768, 0, 0);
    }
}
__device__ __forceinline__ void batch_compute(const Batch& B, const K2Ctx& c, f32x2 (&acc)[4], const u32x2 (&wb)[3][2]) {
    const u32x2 g = lds_rd64((B.ga & 0x7fe0) + 16);
    f32x4 hat[4];
#pragma unroll
    for (int j = 0; j < 4; ++j) hat[j] = lds_rd128(B.sa[j] & 0x7fe0);
    f32x4 W4[3][2];
#pragma unroll
    for (int q = 0; q < 3; ++q) { W4[q][0] = filter4(g, wb[q][0]); W4[q][1] = filter4(g, wb[q][1]); }
#pragma unroll
    for (int j = 0; j < 4; ++j) {
        const float W[3][2] = {{W4[0][0][j], W4[0][1][j]}, {W4[1][0][j], W4[1][1][j]}, {W4[2][0][j], W4[2][1][j]}};
        edge_fma(acc, W, B.ld[j], hat[j]);
        const int en = __builtin_amdgcn_readfirstlane(B.sa[j]) & 31;
        if (en) {
            f32x2 tot[2];
            half_reduce(acc, tot);
            store_rows(c, en - 1, tot);
#pragma unroll
            for (int a = 0; a < 4; ++a) acc[a] = (f32x2){0.f, 0.f};
        }
    }
}

__global__ __launch_bounds__(T2, 4) void k2_gather(const unsigned short* __restrict__ sv, const unsigned short* __restrict__ wr,
                                                   char* __restrict__ ws, char* __restrict__ out) {
    extern __shared__ __attribute__((aligned(16))) char smem[];
    K2Ctx c;
    c.smem = smem; c.ws = ws;
    c.tid = threadIdx.x; c.lane = c.tid & 63; c.wv = __builtin_amdgcn_readfirstlane(c.tid >> 6);
    c.h = blockIdx.x & 1; const int r = ((blockIdx.x >> 1) & 3) * NRQ + (blockIdx.x >> 3);
    c.n0 = r * RN; c.nn = min(RN, NN - c.n0);
    c.hh = c.lane >> 5; c.c2 = c.lane & 31;
    c.lo = c.h * 128 + c.c2 * 4;
    c.rs_phi = __builtin_amdgcn_make_buffer_rsrc((void*)(ws + WS_PHI), 0, NN * 1024, 0x00020000);
    c.rs_sv = __builtin_amdgcn_make_buffer_rsrc((void*)sv, 0, NN * 1024, 0x00020000);
    c.rs_out = __builtin_amdgcn_make_buffer_rsrc((void*)out, 0, NN * 1024, 0x00020000);
    const int lane = c.lane, tid = c.tid, wv = c.wv, h = c.h, hh = c.hh, c2 = c.c2;
    STAMP(1, 0);
    int* tot_lds = (int*)(smem + L2_MISC);
    unsigned myce = 0, mycb = 0;
    const unsigned* cellp = (const unsigned*)(ws + WS_CELL) + r * 256 + (lane >> 4) * 32 + (lane & 15);
    unsigned ce0 = cellp[0], ce1 = cellp[128];
    u32x2 wb[3][2];
    {
        unsigned wraw[3][4];
#pragma unroll
        for (int p = 0; p < 3; ++p)
#pragma unroll
            for (int k = 0; k < 4; ++k) wraw[p][k] = *(const unsigned*)(wr + k * 384 + p * 128 + h * 64 + 2 * c2);
        if (wv >= 2) {
#pragma unroll
            for (int i4 = 0; i4 < (RN + 3) / 4; ++i4) {
                const int i = min(wv - 2 + 2 * i4, (RN + 1) / 2 - 1);
                const int node = min(c.n0 + 2 * i + hh, NN - 1);
                dma16_asm((const char*)sv + (size_t)node * 1024 + ((lane >> 3) & 3) * 256 + h * 128 + (lane & 7) * 16, L2_RES + i * 1024);
            }
            ((unsigned*)(smem + L2_CNT))[tid - 128] = 0;
            if (tid < 130) ((f32x4*)(smem + L2_U))[2 * CAP + tid - 128] = (f32x4){0.f, 0.f, 0.f, 0.f};
            static_assert(32 * NCH * 2 == 128 * 4, "");
        }
#pragma unroll
        for (int p = 0; p < 3; ++p)
#pragma unroll
            for (int k = 0; k < 2; ++k) { wb[p][0][k] = __builtin_amdgcn_perm(wraw[p][2 * k + 1], wraw[p][2 * k], 0x05040100u); wb[p][1][k] = __builtin_amdgcn_perm(wraw[p][2 * k + 1], wraw[p][2 * k], 0x07060302u); }
        asm volatile("" : "+v"(wb[0][0]), "+v"(wb[0][1]), "+v"(wb[1][0]), "+v"(wb[1][1]), "+v"(wb[2][0]), "+v"(wb[2][1]));
    }
    if (wv < 2) {
        if (8 * (lane & 15) + (lane >> 4) >= NB_E) ce0 = 0;
        if (8 * (lane & 15) + (lane >> 4) + 4 >= NB_E) ce1 = 0;
        const unsigned cc0 = ce0 >> 16, cc1 = ce1 >> 16;
        const unsigned in0 = wave_incl_scan(cc0), t0 = __builtin_amdgcn_readlane(in0, 63);
        const unsigned in1 = wave_incl_scan(cc1) + t0;
        myce = wv == 0 ? ce0 : ce1;
        mycb = wv == 0 ? in0 - cc0 : in1 - cc1;
        if (tid == 63) *tot_lds = in1;
    }
    const unsigned mycc = myce >> 16;
    const unsigned gi0 = (8 * (tid & 15) + (tid >> 4)) * EPB + (myce & 0xffff);
    if (wv < 2) {
        const __amdgpu_buffer_rsrc_t rs_rec = __builtin_amdgcn_make_buffer_rsrc((void*)(ws + WS_REC), 0, NB_E * EPB * 20, 0x00020000);
        u32x4 rf[6]; unsigned rm[6];
#pragma unroll
        for (int k = 0; k < 6; ++k) {
            const unsigned go = ((unsigned)k < mycc && mycb + k < CAP) ? (gi0 + k) * 20 : 0x80000000u;
            rf[k] = __builtin_amdgcn_raw_buffer_load_b128(rs_rec, go, 0, 0);
            rm[k] = __builtin_amdgcn_raw_buffer_load_b32(rs_rec, go + 16, 0, 0);
        }
#pragma unroll
        for (int k = 0; k < 6; ++k) if ((unsigned)k < mycc && mycb + k < CAP) put_record(smem, mycb + k, rf[k], rm[k]);
        for (unsigned q = 6; q < mycc && mycb + q < CAP; ++q) put_record_from(smem, ws, mycb + q, gi0 + q);
    }
    asm volatile("s_waitcnt vmcnt(0) lgkmcnt(0)\n\ts_barrier" ::: "memory");
    STAMP(1, 2);
    const int total = __builtin_amdgcn_readfirstlane(*tot_lds);
    int ta, tb;
    build_slots(c, min(total, CAP), ta, tb);
    raw_barrier();
    STAMP(1, 3);
    f32x2 acc[4];
#pragma unroll
    for (int a = 0; a < 4; ++a) acc[a] = (f32x2){0.f, 0.f};

    if (total <= CAP) {
        Batch A, B;
        batch_load(A, c, ta, tb);
        STAMP(1, 4);
        for (int t4 = ta; t4 < tb; t4 += 8) {
            batch_load(B, c, t4 + 4, tb);
            batch_compute(A, c, acc, wb);
            if (t4 + 4 >= tb) break;
            batch_load(A, c, t4 + 8, tb);
            batch_compute(B, c, acc, wb);
        }
        STAMP(1, 5);
    } else {
        float* accw = (float*)(ws + WS_ACC);
        for (int w0 = 0;;) {
            const bool firstw = (w0 == 0), lastw = (w0 + CAP >= total);
#pragma unroll 1
            for (int t = ta; t < tb; ++t) {
                const u32x2 sl = lds_rd64(L2_SLOT + t * 16 + hh * 8);
                const unsigned off = sl[1] | c.lo;
                unsigned ld[6];
#pragma unroll
                for (int a = 0; a < 3; ++a) { ld[a] = __builtin_amdgcn_raw_buffer_load_b32(c.rs_phi, off + a * 256, 0, 0); ld[3 + a] = __builtin_amdgcn_raw_buffer_load_b32(c.rs_sv, off + 256 + a * 256, 0, 0); }
                const u32x2 g = lds_rd64((sl[0] & 0x7fe0) + 16);
                float W[3][2];
#pragma unroll
                for (int q = 0; q < 3; ++q) { W[q][0] = filter4(g, wb[q][0])[0]; W[q][1] = filter4(g, wb[q][1])[0]; }
                edge_fma(acc, W, ld, lds_rd128(sl[0] & 0x7fe0));
                const int en = __builtin_amdgcn_readfirstlane(sl[0]) & 31;
                if (en) {
                    f32x2 tot[2];
                    half_reduce(acc, tot);
                    float* aw = accw + (((size_t)(c.n0 + en - 1) * 2 + h) * 4 + 2 * hh) * 64 + 2 * c2;
                    if (!firstw) {
#pragma unroll
                        for (int q = 0; q < 2; ++q) { tot[q][0] += aw[q * 64]; tot[q][1] += aw[q * 64 + 1]; }
                    }
                    if (lastw) store_rows(c, en - 1, tot);
                    else {
#pragma unroll
                        for (int q = 0; q < 2; ++q) { aw[q * 64] = tot[q][0]; aw[q * 64 + 1] = tot[q][1]; }
                    }
#pragma unroll
                    for (int a = 0; a < 4; ++a) acc[a] = (f32x2){0.f, 0.f};
                }
            }
            w0 += CAP;
            if (w0 >= total) break;
            __syncthreads();
            if (tid < 128) ((unsigned*)(smem + L2_CNT))[tid] = 0;
            for (unsigned q = 0; q < mycc; ++q) {
                const int p = (int)(mycb + q) - w0;
                if (p >= 0 && p < CAP) put_record_from(smem, ws, p, gi0 + q);
            }
            __syncthreads();
            build_slots(c, min(CAP, total - w0), ta, tb);
            __syncthreads();
        }
    }
}

extern "C" void kernel_launch(void* const* d_in, const int* in_sizes, int n_in, void* d_out, int out_size, void* d_ws, size_t ws_size,
                              hipStream_t stream) {
    const unsigned short* sv = (const unsigned short*)d_in[0];
    const unsigned short* pos = (const unsigned short*)d_in[1];
    const int* src = (const int*)d_in[2];
    const int* dst = (const int*)d_in[3];
    const unsigned short* freqs = (const unsigned short*)d_in[4];
    const unsigned short* w1 = (const unsigned short*)d_in[5];
    const unsigned short* w2 = (const unsigned short*)d_in[6];
    const unsigned short* wr = (const unsigned short*)d_in[7];
    (void)hipFuncSetAttribute((const void*)k1_prepare, hipFuncAttributeMaxDynamicSharedMemorySize, K1_LDS);
    (void)hipFuncSetAttribute((const void*)k2_gather, hipFuncAttributeMaxDynamicSharedMemorySize, K2_LDS);
    k1_prepare<<<NB_E + NB_G, T1, K1_LDS, stream>>>(sv, pos, src, dst, freqs, w1, w2, (char*)d_ws);
    k2_gather<<<NR * 2, T2, K2_LDS, stream>>>(sv, wr, (char*)d_ws, (char*)d_out);
}
```

```cpp
#include <hip/hip_runtime.h>
#include <hip/hip_bf16.h>
#include <stdint.h>

typedef __attribute__((ext_vector_type(8))) short bf16x8;
typedef __attribute__((ext_vector_type(4))) short s16x4;
typedef __attribute__((ext_vector_type(4))) float f32x4;
typedef __attribute__((ext_vector_type(4))) unsigned u32x4;
typedef __attribute__((ext_vector_type(4), aligned(4))) unsigned u32x4a4;
typedef __attribute__((ext_vector_type(2))) unsigned u32x2;
typedef __attribute__((ext_vector_type(4))) int i32x4;
typedef __attribute__((address_space(3))) void* lds_ptr_t;
typedef const __attribute__((address_space(1))) void* glb_ptr_t;
typedef __attribute__((address_space(3))) s16x4* lds_s16x4_t;

constexpr int NN   = 12500;
constexpr int NE   = 400000;
constexpr int T1   = 1024;
constexpr int EPB  = 4096;
constexpr int NB_E = (NE + EPB - 1) / EPB;
constexpr int GTL  = 5;
constexpr int GT   = 16 * GTL;
constexpr int NB_G = (NN + GT - 1) / GT;
constexpr int RN   = 25;
constexpr int NR   = (NN + RN - 1) / RN;
constexpr int NRQ  = NR / 4;
constexpr int OFS  = 512;
constexpr int T2   = 256;
constexpr int W2   = T2 / 64;
constexpr int CAP  = 512;
static_assert(NB_E <= 128 && T2 == 256 && NR % 4 == 0 && EPB == 4 * T1, "");
static_assert(OFS >= NR + 1 && RN <= 62 && OFS <= T1, "");

constexpr size_t WS_PHI  = 0;
constexpr size_t WS_REC  = 12800000;
constexpr size_t WS_CELL = WS_REC + (size_t)NB_E * EPB * 20;
constexpr size_t WS_ACC  = WS_CELL + (size_t)NR * 1024;
constexpr size_t WS_END  = WS_ACC + (size_t)NN * 512 * 4;
static_assert(WS_CELL % 128 == 0 && WS_ACC % 128 == 0, "");
#define STAMP(kid, slot) do {} while (0)

__device__ __forceinline__ float bf2f(unsigned h) { return __uint_as_float(h << 16); }
__device__ __forceinline__ float bflo(unsigned u) { return __uint_as_float(u << 16); }
__device__ __forceinline__ float bfhi(unsigned u) { return __uint_as_float(u & 0xffff0000u); }
typedef __attribute__((ext_vector_type(2))) __bf16 bf16x2_t;
typedef __attribute__((ext_vector_type(2))) float f32x2_t;
__device__ __forceinline__ unsigned pack2(float lo, float hi) { f32x2_t f = {lo, hi}; bf16x2_t b = __builtin_convertvector(f, bf16x2_t); return __builtin_bit_cast(unsigned, b); }
__device__ __forceinline__ float rbf(float x) { return bflo(pack2(x, 0.f)); }

__device__ __forceinline__ unsigned wave_incl_scan(unsigned x) {
    x += __builtin_amdgcn_update_dpp(0u, x, 0x111, 0xf, 0xf, false);
    x += __builtin_amdgcn_update_dpp(0u, x, 0x112, 0xf, 0xf, false);
    x += __builtin_amdgcn_update_dpp(0u, x, 0x114, 0xf, 0xf, false);
    x += __builtin_amdgcn_update_dpp(0u, x, 0x118, 0xf, 0xf, false);
    x += __builtin_amdgcn_update_dpp(0u, x, 0x142, 0xa, 0xf, false);
    x += __builtin_amdgcn_update_dpp(0u, x, 0x143, 0xc, 0xf, false);
    return x;
}
__device__ __forceinline__ unsigned lanes_below(unsigned long long m) {
    return __builtin_amdgcn_mbcnt_hi((unsigned)(m >> 32), __builtin_amdgcn_mbcnt_lo((unsigned)m, 0u));
}
__device__ __forceinline__ void dma16(const void* g, void* l) { __builtin_amdgcn_global_load_lds((glb_ptr_t)g, (lds_ptr_t)l, 16, 0, 0); }
__device__ __forceinline__ void dma4(const void* g, void* l) { __builtin_amdgcn_global_load_lds((glb_ptr_t)g, (lds_ptr_t)l, 4, 0, 0); }
__device__ __forceinline__ void dma16_asm(const void* g, unsigned lds_addr) {
    unsigned keep;
    asm volatile("s_mov_b32 %0, m0\n\ts_mov_b32 m0, %2\n\ts_nop 0\n\tglobal_load_lds_dwordx4 %1, off\n\ts_mov_b32 m0, %0" : "=&s"(keep) : "v"(g), "s"(lds_addr) : "memory");
}
__device__ __forceinline__ void raw_barrier() { asm volatile("s_waitcnt lgkmcnt(0)\n\ts_barrier" ::: "memory"); }
__device__ __forceinline__ void store16_wt(void* p, u32x4 v) { asm volatile("global_store_dwordx4 %0, %1, off sc1\n\ts_nop 1" :: "v"(p), "v"(v) : "memory"); }

__device__ __forceinline__ float rev_frac(float x) {
    const float C_HI = 0.15915494f, C_LO = (float)(0.15915494309189535 - (double)0.15915494f);
    const float r = x * C_HI;
    const float e = __builtin_fmaf(x, C_HI, -r) + x * C_LO;
    return __builtin_amdgcn_fractf(r) + e;
}
__device__ __forceinline__ float fsin(float x) { return __builtin_amdgcn_sinf(rev_frac(x)); }
__device__ __forceinline__ float fcos(float x) { return __builtin_amdgcn_cosf(rev_frac(x)); }

struct Pos3 { float x, y, z; };
__device__ __forceinline__ Pos3 unpack_pos(unsigned d0, unsigned d1, unsigned j) {
    const unsigned sh = (j & 1) << 4;
    const unsigned xy = __builtin_amdgcn_alignbit(d1, d0, sh);
    Pos3 p;
    p.x = bflo(xy); p.y = bfhi(xy); p.z = __uint_as_float((d1 >> sh) << 16);
    return p;
}
__device__ __forceinline__ Pos3 lds_pos(const unsigned* posw, unsigned j) { const unsigned w = (3 * j) >> 1; return unpack_pos(posw[w], posw[w + 1], j); }

struct Rec { u32x4 f; bool act; };
__device__ __forceinline__ Rec edge_record(const unsigned* posw, unsigned s, unsigned d, float f0, float f1, float f2, float f3) {
    const Pos3 ps = lds_pos(posw, s), pd = lds_pos(posw, d);
    const float rx = rbf(ps.x - pd.x), ry = rbf(ps.y - pd.y), rz = rbf(ps.z - pd.z);
    const float ss = rbf(rbf(rx * rx) + rbf(ry * ry) + rbf(rz * rz));
    const float dd = rbf(__builtin_amdgcn_sqrtf(rbf(ss + 1e-8f)));
    float fc = rbf(0.5f * rbf(rbf(fcos(rbf(rbf(3.14159265358979f * dd) * 0.2f))) + 1.0f));
    fc = (dd < 5.0f) ? fc : 0.0f * dd;
    const float inv = __builtin_amdgcn_rcpf(dd);
    const float b0 = rbf(fsin(rbf(dd * f0))) * inv, b1 = rbf(fsin(rbf(dd * f1))) * inv;
    const float b2 = rbf(fsin(rbf(dd * f2))) * inv, b3 = rbf(fsin(rbf(dd * f3))) * inv;
    Rec r;
    r.f = (u32x4){pack2(b0, b1), pack2(b2, b3), pack2(fc, rx * inv), pack2(ry * inv, rz * inv)};
    r.act = !(fc == 0.0f);
    return r;
}
__device__ __forceinline__ void store_record(char* ws, unsigned gi, u32x4 f, unsigned m) {
    char* p = ws + WS_REC + (size_t)gi * 20;
    *(u32x4a4*)p = f;
    *(unsigned*)(p + 16) = m;
}

constexpr int L1_HIST = 75008;
constexpr int L1_LIST = L1_HIST + OFS * 4 + 16;
constexpr int L1_CARR = L1_LIST + EPB * 8;
constexpr int L1_EDGE_END = L1_CARR + EPB * 2;

__device__ __forceinline__ void edge_block(int b, const unsigned short* __restrict__ pos, const int* __restrict__ src, const int* __restrict__ dst,
                                           const unsigned short* __restrict__ freqs, char* __restrict__ ws, char* smem) {
    unsigned* posw = (unsigned*)smem;
    unsigned* hist = (unsigned*)(smem + L1_HIST);
    unsigned* ncand = hist + OFS;
    u32x2* list = (u32x2*)(smem + L1_LIST);
    unsigned short* carr = (unsigned short*)(smem + L1_CARR);
    const int tid = threadIdx.x, lane = tid & 63, wv = __builtin_amdgcn_readfirstlane(tid >> 6);
    const int e0 = b * EPB;
    const int ne = min(EPB, NE - e0);
    STAMP(0, 0);
    const int q4 = min(tid, ne / 4 - 1);
    const i32x4 sv4 = __builtin_nontemporal_load((const i32x4*)(src + e0) + q4);
    const i32x4 dv4 = __builtin_nontemporal_load((const i32x4*)(dst + e0) + q4);
#pragma unroll
    for (int i = 0; i < 5; ++i) { const int p = wv + 16 * i; if (p < 73) dma16((const char*)pos + p * 1024 + lane * 16, smem + p * 1024); }
    if (wv == 15 && lane < 62) dma4((const char*)pos + 73 * 1024 + lane * 4, smem + 73 * 1024);
    const unsigned fq0 = *(const unsigned*)freqs, fq1 = *(const unsigned*)(freqs + 2);
    if (tid < OFS) hist[tid] = 0;
    if (tid == 0) { *ncand = 0; posw[18750] = 0; }
    __syncthreads();
    STAMP(0, 1);
    {
        unsigned pw[4][4];
#pragma unroll
        for (int j = 0; j < 4; ++j) {
            const unsigned ws_ = (3 * (unsigned)sv4[j]) >> 1, wd_ = (3 * (unsigned)dv4[j]) >> 1;
            pw[j][0] = posw[ws_]; pw[j][1] = posw[ws_ + 1]; pw[j][2] = posw[wd_]; pw[j][3] = posw[wd_ + 1];
        }
        unsigned long long m[4];
        unsigned total = 0;
#pragma unroll
        for (int j = 0; j < 4; ++j) {
            const Pos3 ps = unpack_pos(pw[j][0], pw[j][1], sv4[j]), pd = unpack_pos(pw[j][2], pw[j][3], dv4[j]);
            const float rx = ps.x - pd.x, ry = ps.y - pd.y, rz = ps.z - pd.z;
            const float ss = rx * rx + ry * ry + rz * rz;
            const bool act = (4 * tid < ne) & (!(ss >= 26.0f) | (ss > 1e37f));
            m[j] = __ballot(act);
            total += __popcll(m[j]);
        }
        unsigned run = 0;
        if (lane == 0) run = atomicAdd(ncand, total);
        run = __builtin_amdgcn_readfirstlane(run);
#pragma unroll
        for (int j = 0; j < 4; ++j) {
            if ((m[j] >> lane) & 1) list[run + lanes_below(m[j])] = (u32x2){(unsigned)sv4[j] | ((unsigned)dv4[j] << 16), (unsigned)(4 * tid + j)};
            run += __popcll(m[j]);
        }
    }
    __syncthreads();
    STAMP(0, 2);
    const int n = *ncand;
    const float f0 = bflo(fq0), f1 = bfhi(fq0), f2 = bflo(fq1), f3 = bfhi(fq1);
    u32x2 en = list[tid];
    const bool valid = tid < n;
    en.x = valid ? en.x : 0u;
    Rec rc = edge_record(posw, en.x & 0xffff, en.x >> 16, f0, f1, f2, f3);
    rc.act &= valid;
    const unsigned rg = (en.x >> 16) / RN;
    unsigned slot = 0;
    if (rc.act) slot = atomicAdd(&hist[rg], 1u);
    for (int i = tid + T1; i < n; i += T1) {
        const u32x2 e = list[i];
        const Rec r = edge_record(posw, e.x & 0xffff, e.x >> 16, f0, f1, f2, f3);
        list[i].y = e.y | ((r.act ? atomicAdd(&hist[(e.x >> 16) / RN], 1u) : 0xffffu) << 16);
    }
    __syncthreads();
    STAMP(0, 3);
    if (wv == 0) {
        constexpr int PER = OFS / 64;
        const u32x4 ca = *(const u32x4*)(hist + lane * PER), cb = *(const u32x4*)(hist + lane * PER + 4);
        unsigned c[PER] = {ca[0], ca[1], ca[2], ca[3], cb[0], cb[1], cb[2], cb[3]}, sum = 0;
#pragma unroll
        for (int k = 0; k < PER; ++k) sum += c[k];
        unsigned run = wave_incl_scan(sum) - sum;
        unsigned o[PER];
#pragma unroll
        for (int k = 0; k < PER; ++k) { o[k] = run; run += c[k]; }
        *(u32x4*)(hist + lane * PER) = (u32x4){o[0], o[1], o[2], o[3]};
        *(u32x4*)(hist + lane * PER + 4) = (u32x4){o[4], o[5], o[6], o[7]};
    }
    __syncthreads();
    const unsigned c0 = hist[rg], c1 = hist[rg + 1];
    if (rc.act) carr[c0 + slot] = (unsigned short)en.y;
    for (int i = tid + T1; i < n; i += T1) {
        const u32x2 e = list[i];
        if ((e.y >> 16) != 0xffff) carr[hist[(e.x >> 16) / RN] + (e.y >> 16)] = (unsigned short)(e.y & 0xffff);
    }
    if (tid < NR) {
        const unsigned a0 = hist[tid], a1 = hist[tid + 1];
        ((unsigned*)(ws + WS_CELL))[tid * 256 + (b & 7) * 32 + (b >> 3)] = a0 | ((a1 - a0) << 16);
    }
    __syncthreads();
    STAMP(0, 4);
    {
        unsigned rank = 0;
        unsigned cv[4];
#pragma unroll
        for (int q = 0; q < 4; ++q) cv[q] = carr[min(c0 + q, (unsigned)EPB - 1)];
#pragma unroll
        for (int q = 0; q < 4; ++q) rank += (c0 + q < c1) & (cv[q] < en.y);
        if (rc.act) {
            for (unsigned q = c0 + 4; q < c1; ++q) rank += (carr[q] < en.y);
            const unsigned d = en.x >> 16;
            store_record(ws, e0 + c0 + rank, rc.f, (en.x & 0xffff) | ((d - rg * RN) << 16));
        }
    }
    for (int i = tid + T1; i < n; i += T1) {
        const u32x2 e = list[i];
        if ((e.y >> 16) == 0xffff) continue;
        const unsigned s = e.x & 0xffff, d = e.x >> 16, el = e.y & 0xffff, r = d / RN;
        const unsigned a0 = hist[r], a1 = hist[r + 1];
        unsigned rank = 0;
        for (unsigned q = a0; q < a1; ++q) rank += (carr[q] < el);
        const Rec rr = edge_record(posw, s, d, f0, f1, f2, f3);
        store_record(ws, e0 + a0 + rank, rr.f, s | ((d - r * RN) << 16));
    }
    STAMP(0, 5);
}

constexpr int L1_W1 = 0;
constexpr int L1_W2 = 32768;
constexpr int L1_X  = L1_W2 + 98304;
constexpr int L1_H  = 0;
constexpr int L1_O  = L1_W2;
constexpr int OPITCH = 784;
constexpr int L1_GEMM_END = L1_X + GTL * 4096;
constexpr int K1_LDS = L1_GEMM_END > L1_EDGE_END ? L1_GEMM_END : L1_EDGE_END;
static_assert(GT * OPITCH <= 98304, "");

__device__ __forceinline__ s16x4 tr_read(const char* p) { return __builtin_amdgcn_ds_read_tr16_b64_v4i16((lds_s16x4_t)p); }
__device__ __forceinline__ bf16x8 cat8(s16x4 a, s16x4 b) { return __builtin_shufflevector(a, b, 0, 1, 2, 3, 4, 5, 6, 7); }

__device__ __forceinline__ void gemm_block(int g, const unsigned short* __restrict__ sv, const unsigned short* __restrict__ w1,
                                           const unsigned short* __restrict__ w2, char* __restrict__ ws, char* smem) {
    const int tid = threadIdx.x;
    const int lane = tid & 63, wv = __builtin_amdgcn_readfirstlane(tid >> 6), slot = lane & 15, q = lane >> 4;
    const int w = wv & 7, ng = wv >> 3;
    const int n0 = g * GT;
    STAMP(0, 0);
    const unsigned lbase = (unsigned)(size_t)(lds_ptr_t)smem;
#pragma unroll
    for (int i = 0; i < 2; ++i) {
        const int p = wv + 16 * i;
        if (p < GTL * 4) { const int node = min(n0 + 16 * (p >> 2) + slot, NN - 1); dma16_asm(sv + (size_t)node * 512 + 32 * (p & 3) + 8 * q, lbase + L1_X + p * 1024); }
    }
#pragma unroll
    for (int i = 0; i < 2; ++i) {
        const int p = wv + 16 * i, row = 4 * p + q;
        const int sw = (row & 3) | (((row >> 3) & 1) << 2);
        const int ch = (((slot >> 1) ^ sw) << 1) | (slot & 1);
        dma16_asm((const char*)w1 + row * 256 + ch * 16, lbase + L1_W1 + p * 1024);
    }
#pragma unroll
    for (int i = 0; i < 6; ++i) {
        const int p = wv + 16 * i, c = 64 * p + lane, row = c / 48, cp = c - 48 * row, P = cp >> 1;
        const int ch = (((P & ~7) | ((P & 7) ^ (row & 7))) << 1) | (cp & 1);
        dma16_asm((const char*)w2 + row * 768 + ch * 16, lbase + L1_W2 + p * 1024);
    }
    const int qq = slot >> 2, p4 = slot & 3;
    asm volatile("s_waitcnt vmcnt(6)\n\ts_barrier" ::: "memory");
    STAMP(0, 1);
    constexpr int NT = (GTL + 1) / 2;
    bf16x8 a1[4];
    {
        const char* base = smem + L1_W1 + (8 * q + qq) * 256 + (((w ^ (qq | ((q & 1) << 2))) << 1) | (p4 >> 1)) * 16 + 8 * (p4 & 1);
#pragma unroll
        for (int ks = 0; ks < 4; ++ks) a1[ks] = cat8(tr_read(base + ks * 8192), tr_read(base + ks * 8192 + 1024));
    }
    u32x2 hv[NT];
    {
        u32x4 xb[NT][4];
#pragma unroll
        for (int i = 0; i < NT; ++i) {
            const int nt = min(ng + 2 * i, GTL - 1);
#pragma unroll
            for (int ks = 0; ks < 4; ++ks) xb[i][ks] = *(const u32x4*)(smem + L1_X + (nt * 4 + ks) * 1024 + lane * 16);
        }
        f32x4 acc[NT];
#pragma unroll
        for (int i = 0; i < NT; ++i) acc[i] = (f32x4){0, 0, 0, 0};
#pragma unroll
        for (int ks = 0; ks < 4; ++ks)
#pragma unroll
            for (int i = 0; i < NT; ++i) acc[i] = __builtin_amdgcn_mfma_f32_16x16x32_bf16(a1[ks], __builtin_bit_cast(bf16x8, xb[i][ks]), acc[i], 0, 0, 0);
#pragma unroll
        for (int i = 0; i < NT; ++i) {
            float v[4];
            const unsigned x01 = pack2(acc[i][0], acc[i][1]), x23 = pack2(acc[i][2], acc[i][3]);
            const float x[4] = {bflo(x01), bfhi(x01), bflo(x23), bfhi(x23)};
#pragma unroll
            for (int r = 0; r < 4; ++r) v[r] = x[r] * __builtin_amdgcn_rcpf(1.0f + __builtin_amdgcn_exp2f(-1.44269504f * x[r]));
            hv[i] = (u32x2){pack2(v[0], v[1]), pack2(v[2], v[3])};
        }
    }
    raw_barrier();
#pragma unroll
    for (int i = 0; i < NT; ++i) { const int nt = ng + 2 * i; if (nt < GTL) *(u32x2*)(smem + L1_H + (nt * 4 + (w >> 1)) * 1024 + lane * 16 + (w & 1) * 8) = hv[i]; }
    asm volatile("s_waitcnt vmcnt(0) lgkmcnt(0)\n\ts_barrier" ::: "memory");
    STAMP(0, 2);
    bf16x8 a2[3][4];
#pragma unroll
    for (int t = 0; t < 3; ++t) {
        const int P = 3 * w + t, row0 = 4 * q + qq;
        const char* base = smem + L1_W2 + row0 * 768 + ((P & ~7) | ((P & 7) ^ (row0 & 7))) * 32 + (p4 >> 1) * 16 + 8 * (p4 & 1);
#pragma unroll
        for (int ks = 0; ks < 4; ++ks) a2[t][ks] = cat8(tr_read(base + ks * 24576), tr_read(base + ks * 24576 + 12288));
    }
    u32x4 hb[4];
#pragma unroll
    for (int ks = 0; ks < 4; ++ks) hb[ks] = *(const u32x4*)(smem + L1_H + (ng * 4 + ks) * 1024 + lane * 16);
    raw_barrier();
    char* phi = ws + WS_PHI;
#pragma unroll
    for (int i = 0; i < NT; ++i) {
        const int nt = ng + 2 * i;
        f32x4 acc[3] = {{0, 0, 0, 0}, {0, 0, 0, 0}, {0, 0, 0, 0}};
#pragma unroll
        for (int ks = 0; ks < 4; ++ks)
#pragma unroll
            for (int t = 0; t < 3; ++t) acc[t] = __builtin_amdgcn_mfma_f32_16x16x32_bf16(a2[t][ks], __builtin_bit_cast(bf16x8, hb[ks]), acc[t], 0, 0, 0);
        if (i + 1 < NT) {
            const int nn_ = min(nt + 2, GTL - 1);
#pragma unroll
            for (int ks = 0; ks < 4; ++ks) hb[ks] = *(const u32x4*)(smem + L1_H + (nn_ * 4 + ks) * 1024 + lane * 16);
        }
        if (nt < GTL) {
#pragma unroll
            for (int t = 0; t < 3; ++t)
                *(u32x2*)(smem + L1_O + (16 * nt + slot) * OPITCH + (48 * w + 16 * t + 4 * q) * 2) = (u32x2){pack2(acc[t][0], acc[t][1]), pack2(acc[t][2], acc[t][3])};
        }
        raw_barrier();
#pragma unroll
        for (int k = 0; k < 2; ++k) {
            const int p = wv + 16 * k;
            const int c = 64 * p + lane, row = c / 48, cp = c - 48 * row;
            const int nl = 32 * i + row;
            if (p < 24 && nl < GT && n0 + nl < NN) store16_wt(phi + (size_t)(n0 + nl) * 1024 + cp * 16, *(const u32x4*)(smem + L1_O + nl * OPITCH + cp * 16));
        }
    }
    STAMP(0, 5);
}

__global__ __launch_bounds__(T1) void k1_prepare(const unsigned short* __restrict__ sv, const unsigned short* __restrict__ pos,
                                                 const int* __restrict__ src, const int* __restrict__ dst,
                                                 const unsigned short* __restrict__ freqs, const unsigned short* __restrict__ w1,
                                                 const unsigned short* __restrict__ w2, char* __restrict__ ws) {
    extern __shared__ __attribute__((aligned(16))) char smem[];
    const int b = blockIdx.x;
    if (b < NB_E) edge_block(b, pos, src, dst, freqs, ws, smem);
    else gemm_block(b - NB_E, sv, w1, w2, ws, smem);
}

constexpr int NCH     = CAP / 64;
constexpr int NITMAX  = CAP / 2 + RN;
constexpr int L2_U    = 0;
constexpr int L2_RES  = L2_U + (CAP + 1) * 32;
constexpr int L2_CNT  = L2_RES + (RN + 1) * 512;
constexpr int L2_SLOT = L2_CNT + 32 * NCH * 2;
constexpr int L2_MISC = L2_SLOT + (NITMAX + 1) * 16;
constexpr int K2_LDS  = L2_MISC + 16;
static_assert(L2_U == 0 && L2_RES % 16 == 0 && L2_CNT % 16 == 0 && L2_SLOT % 16 == 0 && NCH == 8 && RN <= 31 && CAP * 32 <= 0x4000, "");
constexpr unsigned PAD_A = CAP * 32, PAD_B = 0x3fffu << 10;
typedef __attribute__((ext_vector_type(2))) float f32x2;
typedef __attribute__((address_space(3))) const u32x2* lds_u32x2_t;
typedef __attribute__((address_space(3))) const f32x4* lds_f32x4_t;
typedef __attribute__((address_space(3))) const unsigned* lds_u32_t;
__device__ __forceinline__ u32x2 lds_rd64(unsigned a) { return *(lds_u32x2_t)(size_t)a; }
__device__ __forceinline__ f32x4 lds_rd128(unsigned a) { return *(lds_f32x4_t)(size_t)a; }
__device__ __forceinline__ unsigned lds_rd32(unsigned a) { return *(lds_u32_t)(size_t)a; }

struct K2Ctx {
    char* smem; char* ws;
    __amdgpu_buffer_rsrc_t rs_phi, rs_sv, rs_out;
    int tid, lane, wv, h, n0, nn, hh, c2;
    unsigned lo;
};

__device__ __forceinline__ void put_record(char* smem, int p, u32x4 f, unsigned mm) {
    const float fc = bflo(f.z);
    *(f32x4*)(smem + L2_U + p * 32) = (f32x4){bfhi(f.z), bflo(f.w), bfhi(f.w), __uint_as_float(mm)};
    *(u32x2*)(smem + L2_U + p * 32 + 16) = (u32x2){pack2(bflo(f.x) * fc, bfhi(f.x) * fc), pack2(bflo(f.y) * fc, bfhi(f.y) * fc)};
}
__device__ __forceinline__ void put_record_from(char* smem, const char* ws, int p, unsigned gi) {
    const char* rp = ws + WS_REC + (size_t)gi * 20;
    put_record(smem, p, *(const u32x4a4*)rp, *(const unsigned*)(rp + 16));
}
__device__ __forceinline__ unsigned sum2(unsigned x, unsigned c) { return (x & 0xffff) + (x >> 16) + c; }

__device__ __forceinline__ void build_slots(const K2Ctx& c, int m, int& ta, int& tb) {
    unsigned short* cnt16 = (unsigned short*)(c.smem + L2_CNT);
    u32x2* slots = (u32x2*)(c.smem + L2_SLOT);
    unsigned mm[NCH / W2], rk[NCH / W2];
#pragma unroll
    for (int i = 0; i < NCH / W2; ++i) {
        const int p = 64 * (c.wv + W2 * i) + c.lane;
        if (64 * (c.wv + W2 * i) < m) {
            const bool valid = p < m;
            mm[i] = lds_rd32(L2_U + min(p, CAP) * 32 + 12);
            const unsigned nl = mm[i] >> 16;
            unsigned long long mk = __ballot(valid);
#pragma unroll
            for (int bit = 0; bit < 5; ++bit) { const unsigned long long bb = __ballot((nl >> bit) & 1); mk &= ((nl >> bit) & 1) ? bb : ~bb; }
            rk[i] = lanes_below(mk);
            if (valid && rk[i] == 0) cnt16[nl * NCH + c.wv + W2 * i] = __popcll(mk);
        }
    }
    raw_barrier();
    const u32x4 cv = *(const u32x4*)(cnt16 + (c.lane & 31) * NCH);
    const unsigned cnt = sum2(cv[0], sum2(cv[1], sum2(cv[2], sum2(cv[3], 0))));
    const unsigned its = c.lane < c.nn ? max(1u, (cnt + 1) >> 1) : 0u;
    const unsigned inc = wave_incl_scan(its), st = inc - its;
    const unsigned I = __builtin_amdgcn_readlane(inc, 63);
    if (c.wv == 0) {
        if (c.lane < c.nn) {
            const u32x2 pe = {PAD_A | (c.lane + 1), PAD_B};
            if ((cnt & 1) | (cnt == 0)) slots[2 * inc - 1] = pe;
            if (cnt == 0) slots[2 * st] = pe;
        } else if (c.lane >= 62) slots[2 * NITMAX + c.lane - 62] = (u32x2){PAD_A, PAD_B};
    }
#pragma unroll
    for (int i = 0; i < NCH / W2; ++i) {
        const int ch = c.wv + W2 * i;
        if (64 * ch < m) {
            unsigned below = 0;
#pragma unroll
            for (int d = 0; d < 4; ++d) {
                const unsigned msk = ch >= 2 * d + 2 ? 0xffffffffu : (ch == 2 * d + 1 ? 0x0000ffffu : 0u);
                below = sum2(cv[d] & msk, below);
            }
            const unsigned nl = mm[i] >> 16;
            const unsigned a = __shfl((2 * st + below) | (below << 12) | (its << 22), nl);
            const unsigned rank = ((a >> 12) & 1023) + rk[i];
            const unsigned e = ((rank >> 1) + 1 == (a >> 22)) ? nl + 1 : 0u;
            const int p = 64 * ch + c.lane;
            if (p < m) slots[(a & 4095) + rk[i]] = (u32x2){(unsigned)p * 32 | e, (mm[i] & 0xffff) << 10};
        }
    }
    const unsigned lo = c.wv * I / W2, hi = (c.wv + 1) * I / W2;
    const unsigned long long ma = __ballot(st >= lo && c.lane < c.nn), mb = __ballot(st >= hi && c.lane < c.nn);
    ta = ma ? __builtin_amdgcn_readlane(st, __builtin_ctzll(ma)) : I;
    tb = mb ? __builtin_amdgcn_readlane(st, __builtin_ctzll(mb)) : I;
}

__device__ __forceinline__ float vmul(float a, float b) { float d = a * b; asm("" : "+v"(d)); return d; }
__device__ __forceinline__ float vfma(float a, float b, float c) { float d = __builtin_fmaf(a, b, c); asm("" : "+v"(d)); return d; }
__device__ __forceinline__ f32x2 unpack2(unsigned u) { return (f32x2){bflo(u), bfhi(u)}; }
__device__ __forceinline__ void edge_fma(f32x2 (&acc)[4], const float (&W)[3][2], const unsigned (&ld)[6], f32x4 hat) {
    const f32x2 mv = {vmul(bflo(ld[1]), W[1][0]), vmul(bfhi(ld[1]), W[1][1])};
    const f32x2 mr = {vmul(bflo(ld[2]), W[2][0]), vmul(bfhi(ld[2]), W[2][1])};
    acc[0] = (f32x2){vfma(bflo(ld[0]), W[0][0], acc[0][0]), vfma(bfhi(ld[0]), W[0][1], acc[0][1])};
#pragma unroll
    for (int a = 0; a < 3; ++a) {
        acc[1 + a] = __builtin_elementwise_fma(mv, unpack2(ld[3 + a]), acc[1 + a]);
        acc[1 + a] = __builtin_elementwise_fma(mr, (f32x2){hat[a], hat[a]}, acc[1 + a]);
    }
}
__device__ __forceinline__ f32x4 filter4(u32x2 g, u32x2 wb) {
    return __builtin_amdgcn_mfma_f32_4x4x4bf16_1k(__builtin_bit_cast(s16x4, g), __builtin_bit_cast(s16x4, wb), (f32x4){0.f, 0.f, 0.f, 0.f}, 0, 0, 0);
}
__device__ __forceinline__ void half_reduce(const f32x2 (&acc)[4], f32x2 (&tot)[2]) {
#pragma unroll
    for (int q = 0; q < 2; ++q) {
        const auto s0 = __builtin_amdgcn_permlane32_swap(__float_as_uint(acc[q][0]), __float_as_uint(acc[q + 2][0]), false, false);
        const auto s1 = __builtin_amdgcn_permlane32_swap(__float_as_uint(acc[q][1]), __float_as_uint(acc[q + 2][1]), false, false);
        tot[q] = (f32x2){__uint_as_float(s0[0]), __uint_as_float(s1[0])} + (f32x2){__uint_as_float(s0[1]), __uint_as_float(s1[1])};
    }
}
__device__ __forceinline__ void store_rows(const K2Ctx& c, int nl, const f32x2 (&tot)[2]) {
    const unsigned ra = L2_RES + nl * 512 + c.hh * 256 + c.c2 * 4;
#pragma unroll
    for (int q = 0; q < 2; ++q) {
        const f32x2 o = unpack2(lds_rd32(ra + q * 128)) + tot[q];
        __builtin_amdgcn_raw_buffer_store_b32(pack2(o[0], o[1]), c.rs_out, c.hh * 512 + c.lo + q * 256, (c.n0 + nl) * 1024,   2);
    }
}

struct Batch { unsigned ld[4][6]; unsigned sa[4]; unsigned ga; };
__device__ __forceinline__ void batch_load(Batch& B, const K2Ctx& c, int t4, int tb) {
    unsigned off[4];
#pragma unroll
    for (int j = 0; j < 4; ++j) {
        const int t = (t4 + j < tb) ? t4 + j : NITMAX;
        const u32x2 x = lds_rd64(L2_SLOT + t * 16 + c.hh * 8);
        B.sa[j] = x[0]; off[j] = x[1] | c.lo;
    }
    { const int t = t4 + (c.lane & 3); B.ga = lds_rd32(L2_SLOT + (t < tb ? t : NITMAX) * 16 + c.hh * 8); }
#pragma unroll
    for (int j = 0; j < 4; ++j) {
        B.ld[j][0] = __builtin_amdgcn_raw_buffer_load_b32(c.rs_phi, off[j], 0, 0);
        B.ld[j][1] = __builtin_amdgcn_raw_buffer_load_b32(c.rs_phi, off[j] + 256, 0, 0);
        B.ld[j][2] = __builtin_amdgcn_raw_buffer_load_b32(c.rs_phi, off[j] + 512, 0, 0);
        B.ld[j][3] = __builtin_amdgcn_raw_buffer_load_b32(c.rs_sv, off[j] + 256, 0, 0);
        B.ld[j][4] = __builtin_amdgcn_raw_buffer_load_b32(c.rs_sv, off[j] + 512, 0, 0);
        B.ld[j][5] = __builtin_amdgcn_raw_buffer_load_b32(c.rs_sv, off[j] + 768, 0, 0);
    }
}
__device__ __forceinline__ void batch_compute(const Batch& B, const K2Ctx& c, f32x2 (&acc)[4], const u32x2 (&wb)[3][2]) {
    const u32x2 g = lds_rd64((B.ga & 0x7fe0) + 16);
    f32x4 hat[4];
#pragma unroll
    for (int j = 0; j < 4; ++j) hat[j] = lds_rd128(B.sa[j] & 0x7fe0);
    f32x4 W4[3][2];
#pragma unroll
    for (int q = 0; q < 3; ++q) { W4[q][0] = filter4(g, wb[q][0]); W4[q][1] = filter4(g, wb[q][1]); }
#pragma unroll
    for (int j = 0; j < 4; ++j) {
        const float W[3][2] = {{W4[0][0][j], W4[0][1][j]}, {W4[1][0][j], W4[1][1][j]}, {W4[2][0][j], W4[2][1][j]}};
        edge_fma(acc, W, B.ld[j], hat[j]);
        const int en = __builtin_amdgcn_readfirstlane(B.sa[j]) & 31;
        if (en) {
            f32x2 tot[2];
            half_reduce(acc, tot);
            store_rows(c, en - 1, tot);
#pragma unroll
            for (int a = 0; a < 4; ++a) acc[a] = (f32x2){0.f, 0.f};
        }
    }
}

__global__ __launch_bounds__(T2, 4) void k2_gather(const unsigned short* __restrict__ sv, const unsigned short* __restrict__ wr,
                                                   char* __restrict__ ws, char* __restrict__ out) {
    extern __shared__ __attribute__((aligned(16))) char smem[];
    K2Ctx c;
    c.smem = smem; c.ws = ws;
    c.tid = threadIdx.x; c.lane = c.tid & 63; c.wv = __builtin_amdgcn_readfirstlane(c.tid >> 6);
    c.h = blockIdx.x & 1; const int r = ((blockIdx.x >> 1) & 3) * NRQ + (blockIdx.x >> 3);
    c.n0 = r * RN; c.nn = min(RN, NN - c.n0);
    c.hh = c.lane >> 5; c.c2 = c.lane & 31;
    c.lo = c.h * 128 + c.c2 * 4;
    c.rs_phi = __builtin_amdgcn_make_buffer_rsrc((void*)(ws + WS_PHI), 0, NN * 1024, 0x00020000);
    c.rs_sv = __builtin_amdgcn_make_buffer_rsrc((void*)sv, 0, NN * 1024, 0x00020000);
    c.rs_out = __builtin_amdgcn_make_buffer_rsrc((void*)out, 0, NN * 1024, 0x00020000);
    const int lane = c.lane, tid = c.tid, wv = c.wv, h = c.h, hh = c.hh, c2 = c.c2;
    STAMP(1, 0);
    int* tot_lds = (int*)(smem + L2_MISC);
    unsigned myce = 0, mycb = 0;
    const unsigned* cellp = (const unsigned*)(ws + WS_CELL) + r * 256 + (lane >> 4) * 32 + (lane & 15);
    unsigned ce0 = cellp[0], ce1 = cellp[128];
    u32x2 wb[3][2];
    {
        unsigned wraw[3][4];
#pragma unroll
        for (int p = 0; p < 3; ++p)
#pragma unroll
            for (int k = 0; k < 4; ++k) wraw[p][k] = *(const unsigned*)(wr + k * 384 + p * 128 + h * 64 + 2 * c2);
        if (wv >= 2) {
#pragma unroll
            for (int i4 = 0; i4 < (RN + 3) / 4; ++i4) {
                const int i = min(wv - 2 + 2 * i4, (RN + 1) / 2 - 1);
                const int node = min(c.n0 + 2 * i + hh, NN - 1);
                dma16_asm((const char*)sv + (size_t)node * 1024 + ((lane >> 3) & 3) * 256 + h * 128 + (lane & 7) * 16, L2_RES + i * 1024);
            }
            ((unsigned*)(smem + L2_CNT))[tid - 128] = 0;
            if (tid < 130) ((f32x4*)(smem + L2_U))[2 * CAP + tid - 128] = (f32x4){0.f, 0.f, 0.f, 0.f};
            static_assert(32 * NCH * 2 == 128 * 4, "");
        }
#pragma unroll
        for (int p = 0; p < 3; ++p)
#pragma unroll
            for (int k = 0; k < 2; ++k) { wb[p][0][k] = __builtin_amdgcn_perm(wraw[p][2 * k + 1], wraw[p][2 * k], 0x05040100u); wb[p][1][k] = __builtin_amdgcn_perm(wraw[p][2 * k + 1], wraw[p][2 * k], 0x07060302u); }
        asm volatile("" : "+v"(wb[0][0]), "+v"(wb[0][1]), "+v"(wb[1][0]), "+v"(wb[1][1]), "+v"(wb[2][0]), "+v"(wb[2][1]));
    }
    if (wv < 2) {
        if (8 * (lane & 15) + (lane >> 4) >= NB_E) ce0 = 0;
        if (8 * (lane & 15) + (lane >> 4) + 4 >= NB_E) ce1 = 0;
        const unsigned cc0 = ce0 >> 16, cc1 = ce1 >> 16;
        const unsigned in0 = wave_incl_scan(cc0), t0 = __builtin_amdgcn_readlane(in0, 63);
        const unsigned in1 = wave_incl_scan(cc1) + t0;
        myce = wv == 0 ? ce0 : ce1;
        mycb = wv == 0 ? in0 - cc0 : in1 - cc1;
        if (tid == 63) *tot_lds = in1;
    }
    const unsigned mycc = myce >> 16;
    const unsigned gi0 = (8 * (tid & 15) + (tid >> 4)) * EPB + (myce & 0xffff);
    if (wv < 2) {
        const __amdgpu_buffer_rsrc_t rs_rec = __builtin_amdgcn_make_buffer_rsrc((void*)(ws + WS_REC), 0, NB_E * EPB * 20, 0x00020000);
        u32x4 rf[6]; unsigned rm[6];
#pragma unroll
        for (int k = 0; k < 6; ++k) {
            const unsigned go = ((unsigned)k < mycc && mycb + k < CAP) ? (gi0 + k) * 20 : 0x80000000u;
            rf[k] = __builtin_amdgcn_raw_buffer_load_b128(rs_rec, go, 0, 0);
            rm[k] = __builtin_amdgcn_raw_buffer_load_b32(rs_rec, go + 16, 0, 0);
        }
#pragma unroll
        for (int k = 0; k < 6; ++k) if ((unsigned)k < mycc && mycb + k < CAP) put_record(smem, mycb + k, rf[k], rm[k]);
        for (unsigned q = 6; q < mycc && mycb + q < CAP; ++q) put_record_from(smem, ws, mycb + q, gi0 + q);
    }
    asm volatile("s_waitcnt vmcnt(0) lgkmcnt(0)\n\ts_barrier" ::: "memory");
    STAMP(1, 2);
    const int total = __builtin_amdgcn_readfirstlane(*tot_lds);
    int ta, tb;
    build_slots(c, min(total, CAP), ta, tb);
    raw_barrier();
    STAMP(1, 3);
    f32x2 acc[4];
#pragma unroll
    for (int a = 0; a < 4; ++a) acc[a] = (f32x2){0.f, 0.f};

    if (total <= CAP) {
        Batch A, B;
        batch_load(A, c, ta, tb);
        STAMP(1, 4);
        for (int t4 = ta; t4 < tb; t4 += 8) {
            batch_load(B, c, t4 + 4, tb);
            batch_compute(A, c, acc, wb);
            if (t4 + 4 >= tb) break;
            batch_load(A, c, t4 + 8, tb);
            batch_compute(B, c, acc, wb);
        }
        STAMP(1, 5);
    } else {
        float* accw = (float*)(ws + WS_ACC);
        for (int w0 = 0;;) {
            const bool firstw = (w0 == 0), lastw = (w0 + CAP >= total);
#pragma unroll 1
            for (int t = ta; t < tb; ++t) {
                const u32x2 sl = lds_rd64(L2_SLOT + t * 16 + hh * 8);
                const unsigned off = sl[1] | c.lo;
                unsigned ld[6];
#pragma unroll
                for (int a = 0; a < 3; ++a) { ld[a] = __builtin_amdgcn_raw_buffer_load_b32(c.rs_phi, off + a * 256, 0, 0); ld[3 + a] = __builtin_amdgcn_raw_buffer_load_b32(c.rs_sv, off + 256 + a * 256, 0, 0); }
                const u32x2 g = lds_rd64((sl[0] & 0x7fe0) + 16);
                float W[3][2];
#pragma unroll
                for (int q = 0; q < 3; ++q) { W[q][0] = filter4(g, wb[q][0])[0]; W[q][1] = filter4(g, wb[q][1])[0]; }
                edge_fma(acc, W, ld, lds_rd128(sl[0] & 0x7fe0));
                const int en = __builtin_amdgcn_readfirstlane(sl[0]) & 31;
                if (en) {
                    f32x2 tot[2];
                    half_reduce(acc, tot);
                    float* aw = accw + (((size_t)(c.n0 + en - 1) * 2 + h) * 4 + 2 * hh) * 64 + 2 * c2;
                    if (!firstw) {
#pragma unroll
                        for (int q = 0; q < 2; ++q) { tot[q][0] += aw[q * 64]; tot[q][1] += aw[q * 64 + 1]; }
                    }
                    if (lastw) store_rows(c, en - 1, tot);
                    else {
#pragma unroll
                        for (int q = 0; q < 2; ++q) { aw[q * 64] = tot[q][0]; aw[q * 64 + 1] = tot[q][1]; }
                    }
#pragma unroll
                    for (int a = 0; a < 4; ++a) acc[a] = (f32x2){0.f, 0.f};
                }
            }
            w0 += CAP;
            if (w0 >= total) break;
            __syncthreads();
            if (tid < 128) ((unsigned*)(smem + L2_CNT))[tid] = 0;
            for (unsigned q = 0; q < mycc; ++q) {
                const int p = (int)(mycb + q) - w0;
                if (p >= 0 && p < CAP) put_record_from(smem, ws, p, gi0 + q);
            }
            __syncthreads();
            build_slots(c, min(CAP, total - w0), ta, tb);
            __syncthreads();
        }
    }
}

__global__ void k_chase(const char* base, size_t stride, int n, char* ws) {
    size_t off = (size_t)blockIdx.x * 128;
    unsigned acc = 0;
    for (int i = 0; i < n; ++i) { unsigned v = *(const volatile unsigned*)(base + off); unsigned z = v; asm volatile("v_and_b32 %0, 0, %0" : "+v"(z)); off += stride + z; acc += v; }
    if (acc == 0x12345678u && ws == nullptr) ws[0] = 1;
}
template <int N> __device__ __forceinline__ float chain_(float x, float y) { if constexpr (N == 0) return x; else { x = __builtin_fmaf(x, y, (float)N * 0.001f); asm volatile("" : "+v"(x)); return chain_<N - 1>(x, y); } }
__global__ void k_code(char* ws, float y) { float x = threadIdx.x;
#define C5 x = chain_<500>(x, y);
  C5 C5 C5 C5 C5 C5 C5 C5
  if (x == 1234.5f && ws == nullptr) ws[0] = 1; }
extern "C" void kernel_launch(void* const* d_in, const int* in_sizes, int n_in, void* d_out, int out_size, void* d_ws, size_t ws_size,
                              hipStream_t stream) {
    const unsigned short* sv = (const unsigned short*)d_in[0];
    const unsigned short* pos = (const unsigned short*)d_in[1];
    const int* src = (const int*)d_in[2];
    const int* dst = (const int*)d_in[3];
    const unsigned short* freqs = (const unsigned short*)d_in[4];
    const unsigned short* w1 = (const unsigned short*)d_in[5];
    const unsigned short* w2 = (const unsigned short*)d_in[6];
    const unsigned short* wr = (const unsigned short*)d_in[7];
    (void)hipFuncSetAttribute((const void*)k1_prepare, hipFuncAttributeMaxDynamicSharedMemorySize, K1_LDS);
    (void)hipFuncSetAttribute((const void*)k2_gather, hipFuncAttributeMaxDynamicSharedMemorySize, K2_LDS);
    k_chase<<<1, 64, 0, stream>>>((const char*)d_in[0], 4096, 0, (char*)d_ws);
    k1_prepare<<<NB_E + NB_G, T1, K1_LDS, stream>>>(sv, pos, src, dst, freqs, w1, w2, (char*)d_ws);
    k2_gather<<<NR * 2, T2, K2_LDS, stream>>>(sv, wr, (char*)d_ws, (char*)d_out);
}
```

```cpp
#include <hip/hip_runtime.h>
#include <hip/hip_bf16.h>
#include <stdint.h>

typedef __attribute__((ext_vector_type(8))) short bf16x8;
typedef __attribute__((ext_vector_type(4))) short s16x4;
typedef __attribute__((ext_vector_type(4))) float f32x4;
typedef __attribute__((ext_vector_type(4))) unsigned u32x4;
typedef __attribute__((ext_vector_type(4), aligned(4))) unsigned u32x4a4;
typedef __attribute__((ext_vector_type(2))) unsigned u32x2;
typedef __attribute__((ext_vector_type(4))) int i32x4;
typedef __attribute__((address_space(3))) void* lds_ptr_t;
typedef const __attribute__((address_space(1))) void* glb_ptr_t;
typedef __attribute__((address_space(3))) s16x4* lds_s16x4_t;

constexpr int NN   = 12500;
constexpr int NE   = 400000;
constexpr int T1   = 1024;
constexpr int EPB  = 3200;
constexpr int NB_E = NE / EPB;
constexpr int GTL  = 6;
constexpr int GT   = 16 * GTL;
constexpr int NB_G = (NN + GT - 1) / GT;
constexpr int RN   = 25;
constexpr int NR   = (NN + RN - 1) / RN;
constexpr int NRQ  = NR / 4;
constexpr int OFS  = 512;
constexpr int T2   = 256;
constexpr int W2   = T2 / 64;
constexpr int CAP  = 512;
static_assert(NB_E <= 128 && NB_E * EPB == NE && T2 == 256 && NR % 4 == 0 && EPB % 4 == 0 && EPB <= 4 * T1, "");
static_assert(OFS >= NR + 1 && RN <= 62 && OFS <= T1, "");

constexpr size_t WS_PHI  = 0;
constexpr size_t WS_REC  = 12800000;
constexpr size_t WS_CELL = WS_REC + (size_t)NB_E * EPB * 20;
constexpr size_t WS_ACC  = WS_CELL + (size_t)NR * 1024;
constexpr size_t WS_END  = WS_ACC + (size_t)NN * 512 * 4;
static_assert(WS_CELL % 128 == 0 && WS_ACC % 128 == 0, "");
#define STAMP_DECL do {} while (0)
#define STAMP(kid, slot) do {} while (0)
#define STAMP_FLUSH(kid) do {} while (0)

__device__ __forceinline__ float bf2f(unsigned h) { return __uint_as_float(h << 16); }
__device__ __forceinline__ float bflo(unsigned u) { return __uint_as_float(u << 16); }
__device__ __forceinline__ float bfhi(unsigned u) { return __uint_as_float(u & 0xffff0000u); }
typedef __attribute__((ext_vector_type(2))) __bf16 bf16x2_t;
typedef __attribute__((ext_vector_type(2))) float f32x2_t;
__device__ __forceinline__ unsigned pack2(float lo, float hi) { f32x2_t f = {lo, hi}; bf16x2_t b = __builtin_convertvector(f, bf16x2_t); return __builtin_bit_cast(unsigned, b); }
__device__ __forceinline__ float rbf(float x) { return bflo(pack2(x, 0.f)); }

__device__ __forceinline__ unsigned wave_incl_scan(unsigned x) {
    x += __builtin_amdgcn_update_dpp(0u, x, 0x111, 0xf, 0xf, false);
    x += __builtin_amdgcn_update_dpp(0u, x, 0x112, 0xf, 0xf, false);
    x += __builtin_amdgcn_update_dpp(0u, x, 0x114, 0xf, 0xf, false);
    x += __builtin_amdgcn_update_dpp(0u, x, 0x118, 0xf, 0xf, false);
    x += __builtin_amdgcn_update_dpp(0u, x, 0x142, 0xa, 0xf, false);
    x += __builtin_amdgcn_update_dpp(0u, x, 0x143, 0xc, 0xf, false);
    return x;
}
__device__ __forceinline__ unsigned lanes_below(unsigned long long m) {
    return __builtin_amdgcn_mbcnt_hi((unsigned)(m >> 32), __builtin_amdgcn_mbcnt_lo((unsigned)m, 0u));
}
__device__ __forceinline__ unsigned long long ballot(bool p) { return __builtin_amdgcn_ballot_w64(p); }
__device__ __forceinline__ unsigned lds_add_rtn(unsigned lds_addr, unsigned v) {
    unsigned old;
    asm volatile("ds_add_rtn_u32 %0, %1, %2\n\ts_waitcnt lgkmcnt(0)" : "=v"(old) : "v"(lds_addr), "v"(v) : "memory");
    return old;
}
__device__ __forceinline__ void dma16(const void* g, void* l) { __builtin_amdgcn_global_load_lds((glb_ptr_t)g, (lds_ptr_t)l, 16, 0, 0); }
__device__ __forceinline__ void dma4(const void* g, void* l) { __builtin_amdgcn_global_load_lds((glb_ptr_t)g, (lds_ptr_t)l, 4, 0, 0); }
__device__ __forceinline__ void dma16_asm(const void* g, unsigned lds_addr) {
    unsigned keep;
    asm volatile("s_mov_b32 %0, m0\n\ts_mov_b32 m0, %2\n\ts_nop 0\n\tglobal_load_lds_dwordx4 %1, off\n\ts_mov_b32 m0, %0" : "=&s"(keep) : "v"(g), "s"(lds_addr) : "memory");
}
__device__ __forceinline__ void bdma16(__amdgpu_buffer_rsrc_t rs, unsigned voff, unsigned soff, unsigned lds_addr) {
    unsigned keep;
    asm volatile("s_mov_b32 %0, m0\n\ts_mov_b32 m0, %4\n\ts_nop 0\n\tbuffer_load_dwordx4 %1, %2, %3 offen lds\n\ts_mov_b32 m0, %0" : "=&s"(keep) : "v"(voff), "s"(rs), "s"(soff), "s"(lds_addr) : "memory");
}
__device__ __forceinline__ void raw_barrier() { asm volatile("s_waitcnt lgkmcnt(0)\n\ts_barrier" ::: "memory"); }
__device__ __forceinline__ void store16_wt(void* p, u32x4 v) { asm volatile("global_store_dwordx4 %0, %1, off sc1\n\ts_nop 1" :: "v"(p), "v"(v) : "memory"); }

__device__ __forceinline__ float rev_frac(float x) {
    const float C_HI = 0.15915494f, C_LO = (float)(0.15915494309189535 - (double)0.15915494f);
    const float r = x * C_HI;
    const float e = __builtin_fmaf(x, C_HI, -r) + x * C_LO;
    return __builtin_amdgcn_fractf(r) + e;
}
__device__ __forceinline__ float fsin(float x) { return __builtin_amdgcn_sinf(rev_frac(x)); }
__device__ __forceinline__ float fcos(float x) { return __builtin_amdgcn_cosf(rev_frac(x)); }

struct Pos3 { float x, y, z; };
typedef __attribute__((address_space(3))) const unsigned* lds_cu32_t;
typedef __attribute__((address_space(3))) u32x2* lds_u32x2w_t;
__device__ __forceinline__ u32x2 pos_words(unsigned j6) { lds_cu32_t p = (lds_cu32_t)(size_t)(j6 & ~3u); return (u32x2){p[0], p[1]}; }
__device__ __forceinline__ Pos3 unpack_pos(u32x2 d, unsigned j6) {
    const unsigned xy = __builtin_amdgcn_alignbyte(d[1], d[0], j6), zz = __builtin_amdgcn_alignbyte(d[1], d[1], j6);
    Pos3 p;
    p.x = bflo(xy); p.y = bfhi(xy); p.z = bflo(zz);
    return p;
}
__device__ __forceinline__ Pos3 lds_pos(unsigned j) { const unsigned j6 = __umul24(j, 6); return unpack_pos(pos_words(j6), j6); }

struct Rec { u32x4 f; bool act; };
__device__ __forceinline__ Rec edge_record(unsigned s, unsigned d, float f0, float f1, float f2, float f3) {
    const Pos3 ps = lds_pos(s), pd = lds_pos(d);
    const unsigned rxy = pack2(ps.x - pd.x, ps.y - pd.y);
    const float rx = bflo(rxy), ry = bfhi(rxy), rz = rbf(ps.z - pd.z);
    const unsigned sxy = pack2(rx * rx, ry * ry);
    const float ss = rbf(bflo(sxy) + bfhi(sxy) + rbf(rz * rz));
    const float dd = rbf(__builtin_amdgcn_sqrtf(rbf(ss + 1e-8f)));
    float fc = rbf(0.5f * rbf(rbf(fcos(rbf(rbf(3.14159265358979f * dd) * 0.2f))) + 1.0f));
    fc = (dd < 5.0f) ? fc : 0.0f * dd;
    const float inv = __builtin_amdgcn_rcpf(dd);
    const unsigned a01 = pack2(dd * f0, dd * f1), a23 = pack2(dd * f2, dd * f3);
    const unsigned s01 = pack2(fsin(bflo(a01)), fsin(bfhi(a01))), s23 = pack2(fsin(bflo(a23)), fsin(bfhi(a23)));
    const float b0 = bflo(s01) * inv, b1 = bfhi(s01) * inv, b2 = bflo(s23) * inv, b3 = bfhi(s23) * inv;
    Rec r;
    r.f = (u32x4){pack2(b0, b1), pack2(b2, b3), pack2(fc, rx * inv), pack2(ry * inv, rz * inv)};
    r.act = !(fc == 0.0f);
    return r;
}
__device__ __forceinline__ void store_record(char* ws, unsigned gi, u32x4 f, unsigned m) {
    char* p = ws + WS_REC + (size_t)gi * 20;
    *(u32x4a4*)p = f;
    *(unsigned*)(p + 16) = m;
}

constexpr int L1_HIST = 75008;
constexpr int L1_LIST = L1_HIST + OFS * 4 + 16;
constexpr int L1_CARR = L1_LIST + EPB * 8;
constexpr int L1_EDGE_END = L1_CARR + EPB * 2;

__device__ __forceinline__ void edge_block(int b, const unsigned short* __restrict__ pos, const int* __restrict__ src, const int* __restrict__ dst,
                                           const unsigned short* __restrict__ freqs, char* __restrict__ ws, char* smem) {
    unsigned* hist = (unsigned*)(smem + L1_HIST);
    unsigned* ncand = hist + OFS;
    u32x2* list = (u32x2*)(smem + L1_LIST);
    unsigned short* carr = (unsigned short*)(smem + L1_CARR);
    const int tid = threadIdx.x, lane = tid & 63, wv = __builtin_amdgcn_readfirstlane(tid >> 6);
    const int e0 = b * EPB;
    STAMP_DECL;
    STAMP(0, 0);
    const int q4 = min(tid, EPB / 4 - 1);
    const i32x4 sv4 = __builtin_nontemporal_load((const i32x4*)(src + e0) + q4);
    const i32x4 dv4 = __builtin_nontemporal_load((const i32x4*)(dst + e0) + q4);
    u32x2 fq;
    asm volatile("global_load_dwordx2 %0, %1, %2" : "=&v"(fq) : "v"(0), "s"(freqs) : "memory");
    {
        const __amdgpu_buffer_rsrc_t rs_pos = __builtin_amdgcn_make_buffer_rsrc((void*)pos, 0, NN * 6, 0x00020000);
#pragma unroll
        for (int i = 0; i < 5; ++i) { const int p = wv + 16 * i; if (p < 74) bdma16(rs_pos, p * 1024 + lane * 16, 0, p * 1024); }
    }
    if (tid < OFS) hist[tid] = 0;
    if (tid == 0) *ncand = 0;
    asm volatile("s_waitcnt vmcnt(0) lgkmcnt(0)\n\ts_barrier" : "+v"(fq) :: "memory");
    STAMP(0, 1);
    if (4 * tid < EPB) {
        u32x2 pw[4][2]; unsigned j6[4][2];
#pragma unroll
        for (int j = 0; j < 4; ++j) {
            j6[j][0] = __umul24(sv4[j], 6); j6[j][1] = __umul24(dv4[j], 6);
            pw[j][0] = pos_words(j6[j][0]); pw[j][1] = pos_words(j6[j][1]);
        }
        asm volatile("" :: "v"(pw[0][0]), "v"(pw[0][1]), "v"(pw[1][0]), "v"(pw[1][1]), "v"(pw[2][0]), "v"(pw[2][1]), "v"(pw[3][0]), "v"(pw[3][1]));
        bool act[4];
        unsigned below = 0, total = 0;
#pragma unroll
        for (int j = 0; j < 4; ++j) {
            const Pos3 ps = unpack_pos(pw[j][0], j6[j][0]), pd = unpack_pos(pw[j][1], j6[j][1]);
            const float rx = ps.x - pd.x, ry = ps.y - pd.y, rz = ps.z - pd.z;
            const float ss = rx * rx + ry * ry + rz * rz;
            act[j] = __float_as_uint(ss) - 0x41d00000u > 0x7f7fffffu - 0x41d00000u;
            const unsigned long long m = ballot(act[j]);
            below = __builtin_amdgcn_mbcnt_hi((unsigned)(m >> 32), __builtin_amdgcn_mbcnt_lo((unsigned)m, below));
            total += __popcll(m);
        }
        unsigned run = 0;
        if (lane == 0) run = lds_add_rtn(L1_HIST + OFS * 4, total);
        unsigned la = L1_LIST + (__builtin_amdgcn_readfirstlane(run) + below) * 8;
#pragma unroll
        for (int j = 0; j < 4; ++j)
            if (act[j]) { *(lds_u32x2w_t)(size_t)la = (u32x2){(unsigned)sv4[j] | ((unsigned)dv4[j] << 16), (unsigned)(4 * tid + j)}; la += 8; }
    }
    __syncthreads();
    STAMP(0, 2);
    const int n = *ncand;
    const float f0 = bflo(fq[0]), f1 = bfhi(fq[0]), f2 = bflo(fq[1]), f3 = bfhi(fq[1]);
    u32x2 en = {0u, 0u};
    Rec rc; rc.act = false;
    unsigned rg = 0, slot = 0;
    if ((tid & ~63) < n) {
        en = list[tid];
        const bool valid = tid < n;
        en.x = valid ? en.x : 0u;
        rc = edge_record(en.x & 0xffff, en.x >> 16, f0, f1, f2, f3);
        rc.act &= valid;
        rg = (en.x >> 16) / RN;
        if (rc.act) slot = atomicAdd(&hist[rg], 1u);
    }
    for (int i = tid + T1; i < n; i += T1) {
        const u32x2 e = list[i];
        const Rec r = edge_record(e.x & 0xffff, e.x >> 16, f0, f1, f2, f3);
        list[i].y = e.y | ((r.act ? atomicAdd(&hist[(e.x >> 16) / RN], 1u) : 0xffffu) << 16);
    }
    __syncthreads();
    STAMP(0, 3);
    if (wv == 0) {
        constexpr int PER = OFS / 64;
        const u32x4 ca = *(const u32x4*)(hist + lane * PER), cb = *(const u32x4*)(hist + lane * PER + 4);
        unsigned c[PER] = {ca[0], ca[1], ca[2], ca[3], cb[0], cb[1], cb[2], cb[3]}, sum = 0;
#pragma unroll
        for (int k = 0; k < PER; ++k) sum += c[k];
        unsigned run = wave_incl_scan(sum) - sum;
        unsigned o[PER];
#pragma unroll
        for (int k = 0; k < PER; ++k) { o[k] = run; run += c[k]; }
        *(u32x4*)(hist + lane * PER) = (u32x4){o[0], o[1], o[2], o[3]};
        *(u32x4*)(hist + lane * PER + 4) = (u32x4){o[4], o[5], o[6], o[7]};
    }
    __syncthreads();
    unsigned c0 = 0, c1 = 0;
    if ((tid & ~63) < n) {
        c0 = hist[rg]; c1 = hist[rg + 1];
        if (rc.act) carr[c0 + slot] = (unsigned short)en.y;
    }
    for (int i = tid + T1; i < n; i += T1) {
        const u32x2 e = list[i];
        if ((e.y >> 16) != 0xffff) carr[hist[(e.x >> 16) / RN] + (e.y >> 16)] = (unsigned short)(e.y & 0xffff);
    }
    if (tid >= T1 - OFS && tid < T1 - OFS + NR) {
        const int r = tid - (T1 - OFS);
        const unsigned a0 = hist[r], a1 = hist[r + 1];
        ((unsigned*)(ws + WS_CELL))[r * 256 + (b & 7) * 32 + (b >> 3)] = a0 | ((a1 - a0) << 16);
    }
    __syncthreads();
    STAMP(0, 4);
    if ((tid & ~63) < n) {
        unsigned rank = 0;
        unsigned cv[4];
#pragma unroll
        for (int q = 0; q < 4; ++q) cv[q] = carr[min(c0 + q, (unsigned)EPB - 1)];
#pragma unroll
        for (int q = 0; q < 4; ++q) rank += (c0 + q < c1) & (cv[q] < en.y);
        if (rc.act) {
            for (unsigned q = c0 + 4; q < c1; ++q) rank += (carr[q] < en.y);
            const unsigned d = en.x >> 16;
            store_record(ws, e0 + c0 + rank, rc.f, (en.x & 0xffff) | ((d - rg * RN) << 16));
        }
    }
    for (int i = tid + T1; i < n; i += T1) {
        const u32x2 e = list[i];
        if ((e.y >> 16) == 0xffff) continue;
        const unsigned s = e.x & 0xffff, d = e.x >> 16, el = e.y & 0xffff, r = d / RN;
        const unsigned a0 = hist[r], a1 = hist[r + 1];
        unsigned rank = 0;
        for (unsigned q = a0; q < a1; ++q) rank += (carr[q] < el);
        const Rec rr = edge_record(s, d, f0, f1, f2, f3);
        store_record(ws, e0 + a0 + rank, rr.f, s | ((d - r * RN) << 16));
    }
    STAMP(0, 5);
    STAMP_FLUSH(0);
}

constexpr int L1_W1 = 0;
constexpr int L1_W2 = 32768;
constexpr int L1_X  = L1_W2 + 98304;
constexpr int L1_H0 = L1_X + GTL * 4096;
constexpr int OPITCH = 784;
constexpr int L1_GEMM_END = L1_H0 + 8192;
constexpr int K1_LDS = L1_GEMM_END > L1_EDGE_END ? L1_GEMM_END : L1_EDGE_END;
static_assert(32 * OPITCH <= 32768 && GTL == 6 && K1_LDS <= 163840, "");
__host__ __device__ constexpr int l1_h(int s) { return s == 0 ? L1_H0 : L1_X + (s - 1) * 8192; }
__host__ __device__ constexpr int l1_o(int s) { return s == 0 ? L1_W1 : L1_W2 + (s - 1) * 32768; }

__device__ __forceinline__ s16x4 tr_read(unsigned a) { return __builtin_amdgcn_ds_read_tr16_b64_v4i16((lds_s16x4_t)(size_t)a); }
__device__ __forceinline__ bf16x8 cat8(s16x4 a, s16x4 b) { return __builtin_shufflevector(a, b, 0, 1, 2, 3, 4, 5, 6, 7); }
typedef __attribute__((address_space(3))) u32x4* lds_u32x4_t;
__device__ __forceinline__ u32x4 lds_ld16(unsigned a) { return *(lds_u32x4_t)(size_t)a; }

#define PIN8(x) asm volatile("" :: "v"(x[0][0]), "v"(x[0][1]), "v"(x[0][2]), "v"(x[0][3]), "v"(x[1][0]), "v"(x[1][1]), "v"(x[1][2]), "v"(x[1][3]))
__device__ __forceinline__ void gemm_block(int g, const unsigned short* __restrict__ sv, const unsigned short* __restrict__ w1,
                                           const unsigned short* __restrict__ w2, char* __restrict__ ws) {
    const int tid = threadIdx.x;
    const int lane = tid & 63, wv = __builtin_amdgcn_readfirstlane(tid >> 6), slot = lane & 15, q = lane >> 4;
    const int w = wv & 7;
    const int n0 = g * GT;
    STAMP_DECL;
    STAMP(0, 0);
    {
        const __amdgpu_buffer_rsrc_t rs_x = __builtin_amdgcn_make_buffer_rsrc((void*)sv, 0, NN * 1024, 0x00020000);
        const __amdgpu_buffer_rsrc_t rs_w1 = __builtin_amdgcn_make_buffer_rsrc((void*)w1, 0, 128 * 256, 0x00020000);
        const __amdgpu_buffer_rsrc_t rs_w2 = __builtin_amdgcn_make_buffer_rsrc((void*)w2, 0, 128 * 768, 0x00020000);
        const unsigned o1 = q * 256 + ((((slot >> 1) ^ (q | ((wv & 2) << 1))) << 1) | (slot & 1)) * 16;
        bdma16(rs_w1, o1, wv * 1024, L1_W1 + wv * 1024);
        bdma16(rs_w1, o1, (wv + 16) * 1024, L1_W1 + (wv + 16) * 1024);
        const unsigned xo = slot * 1024 + q * 16 + (n0 + 16 * (wv >> 2)) * 1024 + (wv & 3) * 64;
        bdma16(rs_x, xo, 0, L1_X + wv * 1024);
        if (wv < 8) bdma16(rs_x, xo + 64 * 1024, 0, L1_X + (16 + wv) * 1024);
        asm volatile("s_barrier" ::: "memory");
#pragma unroll
        for (int s3 = 0; s3 < 3; ++s3) {
            const int c = 64 * s3 + lane, row = c / 48, cp = c - 48 * row, P = cp >> 1;
            const unsigned o2 = row * 768 + ((((P & ~7) | ((P & 7) ^ ((4 * (wv & 1) + row) & 7))) << 1) | (cp & 1)) * 16;
            bdma16(rs_w2, o2, wv * 3072, L1_W2 + wv * 3072 + s3 * 1024);
            bdma16(rs_w2, o2, (wv + 16) * 3072, L1_W2 + (wv + 16) * 3072 + s3 * 1024);
        }
    }
    const int qq = slot >> 2, p4 = slot & 3;
    asm volatile("s_waitcnt vmcnt(6)\n\ts_barrier" ::: "memory");
    STAMP(0, 1);
    constexpr int NP = GTL / 2;
    const __amdgpu_buffer_rsrc_t rs_phi = __builtin_amdgcn_make_buffer_rsrc((void*)(ws + WS_PHI), 0, NN * 1024, 0x00020000);
    auto ship = [&](int s) {
#pragma unroll
        for (int k = 0; k < 3; ++k) {
            const int c = 192 * w + 64 * k + lane, r = c / 48, cp = c - 48 * r;
            __builtin_amdgcn_raw_buffer_store_b128(lds_ld16(l1_o(s) + r * OPITCH + cp * 16), rs_phi, (n0 + 32 * s + r) * 1024 + cp * 16, 0, 16);
        }
    };
    if (wv < 8) {
        bf16x8 a1[4];
        {
            const unsigned base = L1_W1 + (8 * q + qq) * 256 + (((w ^ (qq | ((q & 1) << 2))) << 1) | (p4 >> 1)) * 16 + 8 * (p4 & 1);
#pragma unroll
            for (int ks = 0; ks < 4; ++ks) a1[ks] = cat8(tr_read(base + ks * 8192), tr_read(base + ks * 8192 + 1024));
        }
        const unsigned hw = (w >> 1) * 1024 + lane * 16 + (w & 1) * 8;
#pragma unroll
        for (int s = 0; s < NP + 1; ++s) {
            if (s >= 2) ship(s - 2);
            if (s < NP) {
                u32x4 xb[2][4];
#pragma unroll
                for (int i = 0; i < 2; ++i)
#pragma unroll
                    for (int ks = 0; ks < 4; ++ks) xb[i][ks] = lds_ld16(L1_X + ((2 * s + i) * 4 + ks) * 1024 + lane * 16);
                PIN8(xb);
                f32x4 acc[2] = {{0, 0, 0, 0}, {0, 0, 0, 0}};
#pragma unroll
                for (int ks = 0; ks < 4; ++ks)
#pragma unroll
                    for (int i = 0; i < 2; ++i) acc[i] = __builtin_amdgcn_mfma_f32_16x16x32_bf16(a1[ks], __builtin_bit_cast(bf16x8, xb[i][ks]), acc[i], 0, 0, 0);
#pragma unroll
                for (int i = 0; i < 2; ++i) {
                    const unsigned x01 = pack2(acc[i][0], acc[i][1]), x23 = pack2(acc[i][2], acc[i][3]);
                    const float x[4] = {bflo(x01), bfhi(x01), bflo(x23), bfhi(x23)};
                    float v[4];
#pragma unroll
                    for (int r = 0; r < 4; ++r) v[r] = x[r] * __builtin_amdgcn_rcpf(1.0f + __builtin_amdgcn_exp2f(-1.44269504f * x[r]));
                    *(lds_u32x2w_t)(size_t)(l1_h(s) + i * 4096 + hw) = (u32x2){pack2(v[0], v[1]), pack2(v[2], v[3])};
                }
            }
            if (s == 0) asm volatile("s_waitcnt vmcnt(0)" ::: "memory");
            raw_barrier();
            if (s == 0) STAMP(0, 2);
        }
        ship(NP - 1);
    } else {
        asm volatile("s_waitcnt vmcnt(0)" ::: "memory");
        raw_barrier();
        STAMP(0, 2);
        bf16x8 a2[3][4];
#pragma unroll
        for (int t = 0; t < 3; ++t) {
            const int P = 3 * w + t, row0 = 4 * q + qq;
            const unsigned base = L1_W2 + row0 * 768 + ((P & ~7) | ((P & 7) ^ (row0 & 7))) * 32 + (p4 >> 1) * 16 + 8 * (p4 & 1);
#pragma unroll
            for (int ks = 0; ks < 4; ++ks) a2[t][ks] = cat8(tr_read(base + ks * 24576), tr_read(base + ks * 24576 + 12288));
        }
        const unsigned obase = slot * OPITCH + (48 * w + 4 * q) * 2;
#pragma unroll
        for (int s = 1; s < NP + 1; ++s) {
            u32x4 hb[2][4];
#pragma unroll
            for (int i = 0; i < 2; ++i)
#pragma unroll
                for (int ks = 0; ks < 4; ++ks) hb[i][ks] = lds_ld16(l1_h(s - 1) + i * 4096 + ks * 1024 + lane * 16);
            PIN8(hb);
            f32x4 acc[2][3];
#pragma unroll
            for (int i = 0; i < 2; ++i)
#pragma unroll
                for (int t = 0; t < 3; ++t) acc[i][t] = (f32x4){0, 0, 0, 0};
#pragma unroll
            for (int i = 0; i < 2; ++i)
#pragma unroll
                for (int ks = 0; ks < 4; ++ks)
#pragma unroll
                    for (int t = 0; t < 3; ++t) acc[i][t] = __builtin_amdgcn_mfma_f32_16x16x32_bf16(a2[t][ks], __builtin_bit_cast(bf16x8, hb[i][ks]), acc[i][t], 0, 0, 0);
#pragma unroll
            for (int i = 0; i < 2; ++i)
#pragma unroll
                for (int t = 0; t < 3; ++t)
                    *(lds_u32x2w_t)(size_t)(l1_o(s - 1) + obase + i * 16 * OPITCH + t * 32) = (u32x2){pack2(acc[i][t][0], acc[i][t][1]), pack2(acc[i][t][2], acc[i][t][3])};
            raw_barrier();
            if (s == 1) STAMP(0, 3);
            if (s == 2) STAMP(0, 4);
        }
    }
    STAMP(0, 5);
    STAMP_FLUSH(0);
}

__global__ __launch_bounds__(T1) void k1_prepare(const unsigned short* __restrict__ sv, const unsigned short* __restrict__ pos,
                                                 const int* __restrict__ src, const int* __restrict__ dst,
                                                 const unsigned short* __restrict__ freqs, const unsigned short* __restrict__ w1,
                                                 const unsigned short* __restrict__ w2, char* __restrict__ ws) {
    extern __shared__ __attribute__((aligned(16))) char smem[];
    const int b = blockIdx.x;
    if (b < NB_E) edge_block(b, pos, src, dst, freqs, ws, smem);
    else gemm_block(b - NB_E, sv, w1, w2, ws);
}

constexpr int NCH     = CAP / 64;
constexpr int NITMAX  = CAP / 2 + RN;
constexpr int L2_U    = 0;
constexpr int L2_RES  = L2_U + (CAP + 1) * 32;
constexpr int L2_CNT  = L2_RES + (RN + 1) * 512;
constexpr int L2_SLOT = L2_CNT + 32 * NCH * 2;
constexpr int L2_MISC = L2_SLOT + (NITMAX + 1) * 16;
constexpr int K2_LDS  = L2_MISC + 16;
static_assert(L2_U == 0 && L2_RES % 16 == 0 && L2_CNT % 16 == 0 && L2_SLOT % 16 == 0 && NCH == 8 && RN <= 31 && CAP * 32 <= 0x4000, "");
constexpr unsigned PAD_A = CAP * 32, PAD_B = 0x3fffu << 10;
typedef __attribute__((ext_vector_type(2))) float f32x2;
typedef __attribute__((address_space(3))) const u32x2* lds_u32x2_t;
typedef __attribute__((address_space(3))) const f32x4* lds_f32x4_t;
typedef __attribute__((address_space(3))) const unsigned* lds_u32_t;
__device__ __forceinline__ u32x2 lds_rd64(unsigned a) { return *(lds_u32x2_t)(size_t)a; }
__device__ __forceinline__ f32x4 lds_rd128(unsigned a) { return *(lds_f32x4_t)(size_t)a; }
__device__ __forceinline__ unsigned lds_rd32(unsigned a) { return *(lds_u32_t)(size_t)a; }

struct K2Ctx {
    char* smem; char* ws;
    __amdgpu_buffer_rsrc_t rs_phi, rs_sv, rs_out;
    int tid, lane, wv, h, n0, nn, hh, c2;
    unsigned lo;
};

__device__ __forceinline__ void put_record(char* smem, int p, u32x4 f, unsigned mm) {
    const float fc = bflo(f.z);
    *(f32x4*)(smem + L2_U + p * 32) = (f32x4){bfhi(f.z), bflo(f.w), bfhi(f.w), __uint_as_float(mm)};
    *(u32x2*)(smem + L2_U + p * 32 + 16) = (u32x2){pack2(bflo(f.x) * fc, bfhi(f.x) * fc), pack2(bflo(f.y) * fc, bfhi(f.y) * fc)};
}
__device__ __forceinline__ void put_record_from(char* smem, const char* ws, int p, unsigned gi) {
    const char* rp = ws + WS_REC + (size_t)gi * 20;
    put_record(smem, p, *(const u32x4a4*)rp, *(const unsigned*)(rp + 16));
}
__device__ __forceinline__ unsigned sum2(unsigned x, unsigned c) { return (x & 0xffff) + (x >> 16) + c; }

__device__ __forceinline__ void build_slots(const K2Ctx& c, int m, int& ta, int& tb) {
    unsigned short* cnt16 = (unsigned short*)(c.smem + L2_CNT);
    u32x2* slots = (u32x2*)(c.smem + L2_SLOT);
    unsigned mm[NCH / W2], rk[NCH / W2];
#pragma unroll
    for (int i = 0; i < NCH / W2; ++i) {
        const int p = 64 * (c.wv + W2 * i) + c.lane;
        if (64 * (c.wv + W2 * i) < m) {
            const bool valid = p < m;
            mm[i] = lds_rd32(L2_U + min(p, CAP) * 32 + 12);
            const unsigned nl = mm[i] >> 16;
            unsigned long long mk = ballot(valid);
#pragma unroll
            for (int bit = 0; bit < 5; ++bit) { const bool on = (nl >> bit) & 1; const unsigned long long bb = ballot(on); mk &= on ? bb : ~bb; }
            rk[i] = lanes_below(mk);
            if (valid && rk[i] == 0) cnt16[nl * NCH + c.wv + W2 * i] = __popcll(mk);
        }
    }
    raw_barrier();
    const u32x4 cv = *(const u32x4*)(cnt16 + (c.lane & 31) * NCH);
    const unsigned cnt = sum2(cv[0], sum2(cv[1], sum2(cv[2], sum2(cv[3], 0))));
    const unsigned its = c.lane < c.nn ? max(1u, (cnt + 1) >> 1) : 0u;
    const unsigned inc = wave_incl_scan(its), st = inc - its;
    const unsigned I = __builtin_amdgcn_readlane(inc, 63);
    if (c.wv == 0) {
        if (c.lane < c.nn) {
            const u32x2 pe = {PAD_A | (c.lane + 1), PAD_B};
            if ((cnt & 1) | (cnt == 0)) slots[2 * inc - 1] = pe;
            if (cnt == 0) slots[2 * st] = pe;
        } else if (c.lane >= 62) slots[2 * NITMAX + c.lane - 62] = (u32x2){PAD_A, PAD_B};
    }
#pragma unroll
    for (int i = 0; i < NCH / W2; ++i) {
        const int ch = c.wv + W2 * i;
        if (64 * ch < m) {
            unsigned below = 0;
#pragma unroll
            for (int d = 0; d < 4; ++d) {
                const unsigned msk = ch >= 2 * d + 2 ? 0xffffffffu : (ch == 2 * d + 1 ? 0x0000ffffu : 0u);
                below = sum2(cv[d] & msk, below);
            }
            const unsigned nl = mm[i] >> 16;
            const unsigned a = __shfl((2 * st + below) | (below << 12) | (its << 22), nl);
            const unsigned rank = ((a >> 12) & 1023) + rk[i];
            const unsigned e = ((rank >> 1) + 1 == (a >> 22)) ? nl + 1 : 0u;
            const int p = 64 * ch + c.lane;
            if (p < m) slots[(a & 4095) + rk[i]] = (u32x2){(unsigned)p * 32 | e, (mm[i] & 0xffff) << 10};
        }
    }
    const unsigned lo = c.wv * I / W2, hi = (c.wv + 1) * I / W2;
    const unsigned long long ma = ballot((st >= lo) & (c.lane < c.nn)), mb = ballot((st >= hi) & (c.lane < c.nn));
    ta = ma ? __builtin_amdgcn_readlane(st, __builtin_ctzll(ma)) : I;
    tb = mb ? __builtin_amdgcn_readlane(st, __builtin_ctzll(mb)) : I;
}

__device__ __forceinline__ float vmul(float a, float b) { float d = a * b; asm("" : "+v"(d)); return d; }
__device__ __forceinline__ float vfma(float a, float b, float c) { float d = __builtin_fmaf(a, b, c); asm("" : "+v"(d)); return d; }
__device__ __forceinline__ f32x2 unpack2(unsigned u) { return (f32x2){bflo(u), bfhi(u)}; }
__device__ __forceinline__ void edge_fma(f32x2 (&acc)[4], const float (&W)[3][2], const unsigned (&ld)[6], f32x4 hat) {
    const f32x2 mv = {vmul(bflo(ld[1]), W[1][0]), vmul(bfhi(ld[1]), W[1][1])};
    const f32x2 mr = {vmul(bflo(ld[2]), W[2][0]), vmul(bfhi(ld[2]), W[2][1])};
    acc[0] = (f32x2){vfma(bflo(ld[0]), W[0][0], acc[0][0]), vfma(bfhi(ld[0]), W[0][1], acc[0][1])};
#pragma unroll
    for (int a = 0; a < 3; ++a) {
        acc[1 + a] = __builtin_elementwise_fma(mv, unpack2(ld[3 + a]), acc[1 + a]);
        acc[1 + a] = __builtin_elementwise_fma(mr, (f32x2){hat[a], hat[a]}, acc[1 + a]);
    }
}
__device__ __forceinline__ f32x4 filter4(u32x2 g, u32x2 wb) {
    return __builtin_amdgcn_mfma_f32_4x4x4bf16_1k(__builtin_bit_cast(s16x4, g), __builtin_bit_cast(s16x4, wb), (f32x4){0.f, 0.f, 0.f, 0.f}, 0, 0, 0);
}
__device__ __forceinline__ void half_reduce(const f32x2 (&acc)[4], f32x2 (&tot)[2]) {
#pragma unroll
    for (int q = 0; q < 2; ++q) {
        const auto s0 = __builtin_amdgcn_permlane32_swap(__float_as_uint(acc[q][0]), __float_as_uint(acc[q + 2][0]), false, false);
        const auto s1 = __builtin_amdgcn_permlane32_swap(__float_as_uint(acc[q][1]), __float_as_uint(acc[q + 2][1]), false, false);
        tot[q] = (f32x2){__uint_as_float(s0[0]), __uint_as_float(s1[0])} + (f32x2){__uint_as_float(s0[1]), __uint_as_float(s1[1])};
    }
}
__device__ __forceinline__ void store_rows(const K2Ctx& c, int nl, const f32x2 (&tot)[2]) {
    const unsigned ra = L2_RES + nl * 512 + c.hh * 256 + c.c2 * 4;
#pragma unroll
    for (int q = 0; q < 2; ++q) {
        const f32x2 o = unpack2(lds_rd32(ra + q * 128)) + tot[q];
        __builtin_amdgcn_raw_buffer_store_b32(pack2(o[0], o[1]), c.rs_out, c.hh * 512 + c.lo + q * 256, (c.n0 + nl) * 1024, 0);
    }
}

struct Batch { unsigned ld[4][6]; unsigned sa[4]; unsigned ga; };
__device__ __forceinline__ void batch_load(Batch& B, const K2Ctx& c, int t4, int tb) {
    unsigned off[4];
#pragma unroll
    for (int j = 0; j < 4; ++j) {
        const int t = (t4 + j < tb) ? t4 + j : NITMAX;
        const u32x2 x = lds_rd64(L2_SLOT + t * 16 + c.hh * 8);
        B.sa[j] = x[0]; off[j] = x[1] | c.lo;
    }
    { const int t = t4 + (c.lane & 3); B.ga = lds_rd32(L2_SLOT + (t < tb ? t : NITMAX) * 16 + c.hh * 8); }
#pragma unroll
    for (int j = 0; j < 4; ++j) {
        B.ld[j][0] = __builtin_amdgcn_raw_buffer_load_b32(c.rs_phi, off[j], 0, 0);
        B.ld[j][1] = __builtin_amdgcn_raw_buffer_load_b32(c.rs_phi, off[j] + 256, 0, 0);
        B.ld[j][2] = __builtin_amdgcn_raw_buffer_load_b32(c.rs_phi, off[j] + 512, 0, 0);
        B.ld[j][3] = __builtin_amdgcn_raw_buffer_load_b32(c.rs_sv, off[j] + 256, 0, 0);
        B.ld[j][4] = __builtin_amdgcn_raw_buffer_load_b32(c.rs_sv, off[j] + 512, 0, 0);
        B.ld[j][5] = __builtin_amdgcn_raw_buffer_load_b32(c.rs_sv, off[j] + 768, 0, 0);
    }
}
__device__ __forceinline__ void batch_compute(const Batch& B, const K2Ctx& c, f32x2 (&acc)[4], const u32x2 (&wb)[3][2], bool first) {
    const u32x2 g = lds_rd64((B.ga & 0x7fe0) + 16);
    f32x4 hat[4];
#pragma unroll
    for (int j = 0; j < 4; ++j) hat[j] = lds_rd128(B.sa[j] & 0x7fe0);
    f32x4 W4[3][2];
#pragma unroll
    for (int q = 0; q < 3; ++q) { W4[q][0] = filter4(g, wb[q][0]); W4[q][1] = filter4(g, wb[q][1]); }
#pragma unroll
    for (int j = 0; j < 4; ++j) {
        const float W[3][2] = {{W4[0][0][j], W4[0][1][j]}, {W4[1][0][j], W4[1][1][j]}, {W4[2][0][j], W4[2][1][j]}};
        edge_fma(acc, W, B.ld[j], hat[j]);
        if (j == 0 && first) asm volatile("s_barrier" : "+v"(acc[0]), "+v"(acc[1]), "+v"(acc[2]), "+v"(acc[3]) :: "memory");
        const int en = __builtin_amdgcn_readfirstlane(B.sa[j]) & 31;
        if (en) {
            f32x2 tot[2];
            half_reduce(acc, tot);
            store_rows(c, en - 1, tot);
#pragma unroll
            for (int a = 0; a < 4; ++a) acc[a] = (f32x2){0.f, 0.f};
        }
    }
}

__global__ __launch_bounds__(T2, 4) void k2_gather(const unsigned short* __restrict__ sv, const unsigned short* __restrict__ wr,
                                                   char* __restrict__ ws, char* __restrict__ out) {
    extern __shared__ __attribute__((aligned(16))) char smem[];
    K2Ctx c;
    c.smem = smem; c.ws = ws;
    c.tid = threadIdx.x; c.lane = c.tid & 63; c.wv = __builtin_amdgcn_readfirstlane(c.tid >> 6);
    c.h = blockIdx.x & 1; const int r = ((blockIdx.x >> 1) & 3) * NRQ + (blockIdx.x >> 3);
    c.n0 = r * RN; c.nn = min(RN, NN - c.n0);
    c.hh = c.lane >> 5; c.c2 = c.lane & 31;
    c.lo = c.h * 128 + c.c2 * 4;
    c.rs_phi = __builtin_amdgcn_make_buffer_rsrc((void*)(ws + WS_PHI), 0, NN * 1024, 0x00020000);
    c.rs_sv = __builtin_amdgcn_make_buffer_rsrc((void*)sv, 0, NN * 1024, 0x00020000);
    c.rs_out = __builtin_amdgcn_make_buffer_rsrc((void*)out, 0, NN * 1024, 0x00020000);
    const int lane = c.lane, tid = c.tid, wv = c.wv, h = c.h, hh = c.hh, c2 = c.c2;
    STAMP_DECL;
    STAMP(1, 0);
    int* tot_lds = (int*)(smem + L2_MISC);
    unsigned myce = 0, mycb = 0;
    const unsigned* cellp = (const unsigned*)(ws + WS_CELL) + r * 256 + (lane >> 4) * 32 + (lane & 15);
    unsigned ce0 = cellp[0], ce1 = cellp[128];
    u32x2 wb[3][2];
    {
        unsigned wraw[3][4];
#pragma unroll
        for (int p = 0; p < 3; ++p)
#pragma unroll
            for (int k = 0; k < 4; ++k) wraw[p][k] = *(const unsigned*)(wr + k * 384 + p * 128 + h * 64 + 2 * c2);
        if (wv >= 2) {
            ((unsigned*)(smem + L2_CNT))[tid - 128] = 0;
            if (tid < 130) ((f32x4*)(smem + L2_U))[2 * CAP + tid - 128] = (f32x4){0.f, 0.f, 0.f, 0.f};
            static_assert(32 * NCH * 2 == 128 * 4, "");
        }
#pragma unroll
        for (int p = 0; p < 3; ++p)
#pragma unroll
            for (int k = 0; k < 2; ++k) { wb[p][0][k] = __builtin_amdgcn_perm(wraw[p][2 * k + 1], wraw[p][2 * k], 0x05040100u); wb[p][1][k] = __builtin_amdgcn_perm(wraw[p][2 * k + 1], wraw[p][2 * k], 0x07060302u); }
        asm volatile("" : "+v"(wb[0][0]), "+v"(wb[0][1]), "+v"(wb[1][0]), "+v"(wb[1][1]), "+v"(wb[2][0]), "+v"(wb[2][1]));
    }
    if (wv < 2) {
        if (8 * (lane & 15) + (lane >> 4) >= NB_E) ce0 = 0;
        if (8 * (lane & 15) + (lane >> 4) + 4 >= NB_E) ce1 = 0;
        const unsigned cc0 = ce0 >> 16, cc1 = ce1 >> 16;
        const unsigned in0 = wave_incl_scan(cc0), t0 = __builtin_amdgcn_readlane(in0, 63);
        const unsigned in1 = wave_incl_scan(cc1) + t0;
        myce = wv == 0 ? ce0 : ce1;
        mycb = wv == 0 ? in0 - cc0 : in1 - cc1;
        if (tid == 63) *tot_lds = in1;
    }
    const unsigned mycc = myce >> 16;
    const unsigned gi0 = (8 * (tid & 15) + (tid >> 4)) * EPB + (myce & 0xffff);
    u32x4 rf[6]; unsigned rm[6];
    if (wv < 2) {
        const __amdgpu_buffer_rsrc_t rs_rec = __builtin_amdgcn_make_buffer_rsrc((void*)(ws + WS_REC), 0, NB_E * EPB * 20, 0x00020000);
#pragma unroll
        for (int k = 0; k < 6; ++k) {
            const unsigned go = ((unsigned)k < mycc && mycb + k < CAP) ? (gi0 + k) * 20 : 0x80000000u;
            asm volatile("buffer_load_dwordx4 %0, %2, %3, 0 offen\n\tbuffer_load_dword %1, %2, %3, 0 offen offset:16" : "=&v"(rf[k]), "=&v"(rm[k]) : "v"(go), "s"(rs_rec) : "memory");
        }
    }
    asm volatile("s_barrier" ::: "memory");
    if (wv >= 2) {
#pragma unroll
        for (int i4 = 0; i4 < (RN + 3) / 4; ++i4) {
            const int i = min(wv - 2 + 2 * i4, (RN + 1) / 2 - 1);
            const int node = min(c.n0 + 2 * i + hh, NN - 1);
            dma16_asm((const char*)sv + (size_t)node * 1024 + ((lane >> 3) & 3) * 256 + h * 128 + (lane & 7) * 16, L2_RES + i * 1024);
        }
    } else {
        asm volatile("s_waitcnt vmcnt(0)" : "+v"(rf[0]), "+v"(rf[1]), "+v"(rf[2]), "+v"(rf[3]), "+v"(rf[4]), "+v"(rf[5]),
                     "+v"(rm[0]), "+v"(rm[1]), "+v"(rm[2]), "+v"(rm[3]), "+v"(rm[4]), "+v"(rm[5]) :: "memory");
#pragma unroll
        for (int k = 0; k < 6; ++k) if ((unsigned)k < mycc && mycb + k < CAP) put_record(smem, mycb + k, rf[k], rm[k]);
        for (unsigned q = 6; q < mycc && mycb + q < CAP; ++q) put_record_from(smem, ws, mycb + q, gi0 + q);
    }
    __builtin_amdgcn_s_waitcnt(0xc07f);
    asm volatile("s_barrier" ::: "memory");
    STAMP(1, 2);
    const int total = __builtin_amdgcn_readfirstlane(*tot_lds);
    int ta, tb;
    build_slots(c, min(total, CAP), ta, tb);
    raw_barrier();
    STAMP(1, 3);
    f32x2 acc[4];
#pragma unroll
    for (int a = 0; a < 4; ++a) acc[a] = (f32x2){0.f, 0.f};

    if (total <= CAP) {
        Batch A, B;
        batch_load(A, c, ta, tb);
        STAMP(1, 4);
        for (int t4 = ta; t4 == ta || t4 < tb; t4 += 8) {
            batch_load(B, c, t4 + 4, tb);
            batch_compute(A, c, acc, wb, t4 == ta);
            if (t4 + 4 >= tb) break;
            batch_load(A, c, t4 + 8, tb);
            batch_compute(B, c, acc, wb, false);
        }
        STAMP(1, 5);
        STAMP_FLUSH(1);
    } else {
        float* accw = (float*)(ws + WS_ACC);
        asm volatile("s_waitcnt vmcnt(0)\n\ts_barrier" ::: "memory");
        for (int w0 = 0;;) {
            const bool firstw = (w0 == 0), lastw = (w0 + CAP >= total);
#pragma unroll 1
            for (int t = ta; t < tb; ++t) {
                const u32x2 sl = lds_rd64(L2_SLOT + t * 16 + hh * 8);
                const unsigned off = sl[1] | c.lo;
                unsigned ld[6];
#pragma unroll
                for (int a = 0; a < 3; ++a) { ld[a] = __builtin_amdgcn_raw_buffer_load_b32(c.rs_phi, off + a * 256, 0, 0); ld[3 + a] = __builtin_amdgcn_raw_buffer_load_b32(c.rs_sv, off + 256 + a * 256, 0, 0); }
                const u32x2 g = lds_rd64((sl[0] & 0x7fe0) + 16);
                float W[3][2];
#pragma unroll
                for (int q = 0; q < 3; ++q) { W[q][0] = filter4(g, wb[q][0])[0]; W[q][1] = filter4(g, wb[q][1])[0]; }
                edge_fma(acc, W, ld, lds_rd128(sl[0] & 0x7fe0));
                const int en = __builtin_amdgcn_readfirstlane(sl[0]) & 31;
                if (en) {
                    f32x2 tot[2];
                    half_reduce(acc, tot);
                    float* aw = accw + (((size_t)(c.n0 + en - 1) * 2 + h) * 4 + 2 * hh) * 64 + 2 * c2;
                    if (!firstw) {
#pragma unroll
                        for (int q = 0; q < 2; ++q) { tot[q][0] += aw[q * 64]; tot[q][1] += aw[q * 64 + 1]; }
                    }
                    if (lastw) store_rows(c, en - 1, tot);
                    else {
#pragma unroll
                        for (int q = 0; q < 2; ++q) { aw[q * 64] = tot[q][0]; aw[q * 64 + 1] = tot[q][1]; }
                    }
#pragma unroll
                    for (int a = 0; a < 4; ++a) acc[a] = (f32x2){0.f, 0.f};
                }
            }
            w0 += CAP;
            if (w0 >= total) break;
            __syncthreads();
            if (tid < 128) ((unsigned*)(smem + L2_CNT))[tid] = 0;
            for (unsigned q = 0; q < mycc; ++q) {
                const int p = (int)(mycb + q) - w0;
                if (p >= 0 && p < CAP) put_record_from(smem, ws, p, gi0 + q);
            }
            __syncthreads();
            build_slots(c, min(CAP, total - w0), ta, tb);
            __syncthreads();
        }
    }
}

extern "C" void kernel_launch(void* const* d_in, const int* in_sizes, int n_in, void* d_out, int out_size, void* d_ws, size_t ws_size,
                              hipStream_t stream) {
    const unsigned short* sv = (const unsigned short*)d_in[0];
    const unsigned short* pos = (const unsigned short*)d_in[1];
    const int* src = (const int*)d_in[2];
    const int* dst = (const int*)d_in[3];
    const unsigned short* freqs = (const unsigned short*)d_in[4];
    const unsigned short* w1 = (const unsigned short*)d_in[5];
    const unsigned short* w2 = (const unsigned short*)d_in[6];
    const unsigned short* wr = (const unsigned short*)d_in[7];
    (void)hipFuncSetAttribute((const void*)k1_prepare, hipFuncAttributeMaxDynamicSharedMemorySize, K1_LDS);
    (void)hipFuncSetAttribute((const void*)k2_gather, hipFuncAttributeMaxDynamicSharedMemorySize, K2_LDS);
    k1_prepare<<<NB_E + NB_G, T1, K1_LDS, stream>>>(sv, pos, src, dst, freqs, w1, w2, (char*)d_ws);
    k2_gather<<<NR * 2, T2, K2_LDS, stream>>>(sv, wr, (char*)d_ws, (char*)d_out);
}
```

```cpp
#include <hip/hip_runtime.h>
#include <hip/hip_bf16.h>
#include <stdint.h>

typedef __attribute__((ext_vector_type(8))) short bf16x8;
typedef __attribute__((ext_vector_type(4))) short s16x4;
typedef __attribute__((ext_vector_type(4))) float f32x4;
typedef __attribute__((ext_vector_type(4))) unsigned u32x4;
typedef __attribute__((ext_vector_type(4), aligned(4))) unsigned u32x4a4;
typedef __attribute__((ext_vector_type(2))) unsigned u32x2;
typedef __attribute__((ext_vector_type(4))) int i32x4;
typedef __attribute__((address_space(3))) void* lds_ptr_t;
typedef const __attribute__((address_space(1))) void* glb_ptr_t;
typedef __attribute__((address_space(3))) s16x4* lds_s16x4_t;

constexpr int NN   = 12500;
constexpr int NE   = 400000;
constexpr int T1   = 1024;
constexpr int EPB  = 3200;
constexpr int NB_E = NE / EPB;
constexpr int GTL  = 6;
constexpr int GT   = 16 * GTL;
constexpr int NB_G = (NN + GT - 1) / GT;
constexpr int RN   = 25;
constexpr int NR   = (NN + RN - 1) / RN;
constexpr int NRQ  = NR / 4;
constexpr int OFS  = 512;
constexpr int T2   = 256;
constexpr int W2   = T2 / 64;
constexpr int CAP  = 512;
static_assert(NB_E <= 128 && NB_E * EPB == NE && T2 == 256 && NR % 4 == 0 && EPB % 4 == 0 && EPB <= 4 * T1, "");
static_assert(OFS >= NR + 1 && RN <= 62 && OFS <= T1, "");

constexpr size_t WS_PHI  = 0;
constexpr size_t WS_REC  = 12800000;
constexpr size_t WS_CELL = WS_REC + (size_t)NB_E * EPB * 20;
constexpr size_t WS_ACC  = WS_CELL + (size_t)NR * 1024;
constexpr size_t WS_END  = WS_ACC + (size_t)NN * 512 * 4;
static_assert(WS_CELL % 128 == 0 && WS_ACC % 128 == 0, "");
#define STAMP_DECL do {} while (0)
#define STAMP(kid, slot) do {} while (0)
#define STAMP_FLUSH(kid) do {} while (0)

__device__ __forceinline__ float bf2f(unsigned h) { return __uint_as_float(h << 16); }
__device__ __forceinline__ float bflo(unsigned u) { return __uint_as_float(u << 16); }
__device__ __forceinline__ float bfhi(unsigned u) { return __uint_as_float(u & 0xffff0000u); }
typedef __attribute__((ext_vector_type(2))) __bf16 bf16x2_t;
typedef __attribute__((ext_vector_type(2))) float f32x2_t;
__device__ __forceinline__ unsigned pack2(float lo, float hi) { f32x2_t f = {lo, hi}; bf16x2_t b = __builtin_convertvector(f, bf16x2_t); return __builtin_bit_cast(unsigned, b); }
__device__ __forceinline__ float rbf(float x) { return bflo(pack2(x, 0.f)); }

__device__ __forceinline__ unsigned wave_incl_scan(unsigned x) {
    x += __builtin_amdgcn_update_dpp(0u, x, 0x111, 0xf, 0xf, false);
    x += __builtin_amdgcn_update_dpp(0u, x, 0x112, 0xf, 0xf, false);
    x += __builtin_amdgcn_update_dpp(0u, x, 0x114, 0xf, 0xf, false);
    x += __builtin_amdgcn_update_dpp(0u, x, 0x118, 0xf, 0xf, false);
    x += __builtin_amdgcn_update_dpp(0u, x, 0x142, 0xa, 0xf, false);
    x += __builtin_amdgcn_update_dpp(0u, x, 0x143, 0xc, 0xf, false);
    return x;
}
__device__ __forceinline__ unsigned lanes_below(unsigned long long m) {
    return __builtin_amdgcn_mbcnt_hi((unsigned)(m >> 32), __builtin_amdgcn_mbcnt_lo((unsigned)m, 0u));
}
__device__ __forceinline__ unsigned long long ballot(bool p) { return __builtin_amdgcn_ballot_w64(p); }
__device__ __forceinline__ unsigned lds_add_rtn(unsigned lds_addr, unsigned v) {
    unsigned old;
    asm volatile("ds_add_rtn_u32 %0, %1, %2\n\ts_waitcnt lgkmcnt(0)" : "=v"(old) : "v"(lds_addr), "v"(v) : "memory");
    return old;
}
__device__ __forceinline__ void dma16(const void* g, void* l) { __builtin_amdgcn_global_load_lds((glb_ptr_t)g, (lds_ptr_t)l, 16, 0, 0); }
__device__ __forceinline__ void dma4(const void* g, void* l) { __builtin_amdgcn_global_load_lds((glb_ptr_t)g, (lds_ptr_t)l, 4, 0, 0); }
__device__ __forceinline__ void dma16_asm(const void* g, unsigned lds_addr) {
    unsigned keep;
    asm volatile("s_mov_b32 %0, m0\n\ts_mov_b32 m0, %2\n\ts_nop 0\n\tglobal_load_lds_dwordx4 %1, off\n\ts_mov_b32 m0, %0" : "=&s"(keep) : "v"(g), "s"(lds_addr) : "memory");
}
__device__ __forceinline__ void bdma16(__amdgpu_buffer_rsrc_t rs, unsigned voff, unsigned soff, unsigned lds_addr) {
    unsigned keep;
    asm volatile("s_mov_b32 %0, m0\n\ts_mov_b32 m0, %4\n\ts_nop 0\n\tbuffer_load_dwordx4 %1, %2, %3 offen lds\n\ts_mov_b32 m0, %0" : "=&s"(keep) : "v"(voff), "s"(rs), "s"(soff), "s"(lds_addr) : "memory");
}
__device__ __forceinline__ void raw_barrier() { asm volatile("s_waitcnt lgkmcnt(0)\n\ts_barrier" ::: "memory"); }
__device__ __forceinline__ void store16_wt(void* p, u32x4 v) { asm volatile("global_store_dwordx4 %0, %1, off sc1\n\ts_nop 1" :: "v"(p), "v"(v) : "memory"); }

__device__ __forceinline__ float rev_frac(float x) {
    const float C_HI = 0.15915494f, C_LO = (float)(0.15915494309189535 - (double)0.15915494f);
    const float r = x * C_HI;
    const float e = __builtin_fmaf(x, C_HI, -r) + x * C_LO;
    return __builtin_amdgcn_fractf(r) + e;
}
__device__ __forceinline__ float fsin(float x) { return __builtin_amdgcn_sinf(rev_frac(x)); }
__device__ __forceinline__ float fcos(float x) { return __builtin_amdgcn_cosf(rev_frac(x)); }

struct Pos3 { float x, y, z; };
typedef __attribute__((address_space(3))) const unsigned* lds_cu32_t;
typedef __attribute__((address_space(3))) u32x2* lds_u32x2w_t;
__device__ __forceinline__ u32x2 pos_words(unsigned j6) { lds_cu32_t p = (lds_cu32_t)(size_t)(j6 & ~3u); return (u32x2){p[0], p[1]}; }
__device__ __forceinline__ Pos3 unpack_pos(u32x2 d, unsigned j6) {
    const unsigned xy = __builtin_amdgcn_alignbyte(d[1], d[0], j6), zz = __builtin_amdgcn_alignbyte(d[1], d[1], j6);
    Pos3 p;
    p.x = bflo(xy); p.y = bfhi(xy); p.z = bflo(zz);
    return p;
}
__device__ __forceinline__ Pos3 lds_pos(unsigned j) { const unsigned j6 = __umul24(j, 6); return unpack_pos(pos_words(j6), j6); }

struct Rec { u32x4 f; bool act; };
__device__ __forceinline__ Rec edge_record(unsigned s, unsigned d, float f0, float f1, float f2, float f3) {
    const Pos3 ps = lds_pos(s), pd = lds_pos(d);
    const unsigned rxy = pack2(ps.x - pd.x, ps.y - pd.y);
    const float rx = bflo(rxy), ry = bfhi(rxy), rz = rbf(ps.z - pd.z);
    const unsigned sxy = pack2(rx * rx, ry * ry);
    const float ss = rbf(bflo(sxy) + bfhi(sxy) + rbf(rz * rz));
    const float dd = rbf(__builtin_amdgcn_sqrtf(rbf(ss + 1e-8f)));
    float fc = rbf(0.5f * rbf(rbf(fcos(rbf(rbf(3.14159265358979f * dd) * 0.2f))) + 1.0f));
    fc = (dd < 5.0f) ? fc : 0.0f * dd;
    const float inv = __builtin_amdgcn_rcpf(dd);
    const unsigned a01 = pack2(dd * f0, dd * f1), a23 = pack2(dd * f2, dd * f3);
    const unsigned s01 = pack2(fsin(bflo(a01)), fsin(bfhi(a01))), s23 = pack2(fsin(bflo(a23)), fsin(bfhi(a23)));
    const float b0 = bflo(s01) * inv, b1 = bfhi(s01) * inv, b2 = bflo(s23) * inv, b3 = bfhi(s23) * inv;
    Rec r;
    r.f = (u32x4){pack2(b0, b1), pack2(b2, b3), pack2(fc, rx * inv), pack2(ry * inv, rz * inv)};
    r.act = !(fc == 0.0f);
    return r;
}
__device__ __forceinline__ void store_record(char* ws, unsigned gi, u32x4 f, unsigned m) {
    char* p = ws + WS_REC + (size_t)gi * 20;
    *(u32x4a4*)p = f;
    *(unsigned*)(p + 16) = m;
}

constexpr int L1_HIST = 75008;
constexpr int L1_LIST = L1_HIST + OFS * 4 + 16;
constexpr int L1_CARR = L1_LIST + EPB * 8;
constexpr int L1_EDGE_END = L1_CARR + EPB * 2;

__device__ __forceinline__ void edge_block(int b, const unsigned short* __restrict__ pos, const int* __restrict__ src, const int* __restrict__ dst,
                                           const unsigned short* __restrict__ freqs, char* __restrict__ ws, char* smem) {
    unsigned* hist = (unsigned*)(smem + L1_HIST);
    unsigned* ncand = hist + OFS;
    u32x2* list = (u32x2*)(smem + L1_LIST);
    unsigned short* carr = (unsigned short*)(smem + L1_CARR);
    const int tid = threadIdx.x, lane = tid & 63, wv = __builtin_amdgcn_readfirstlane(tid >> 6);
    const int e0 = b * EPB;
    STAMP_DECL;
    STAMP(0, 0);
    const int q4 = min(tid, EPB / 4 - 1);
    const i32x4 sv4 = __builtin_nontemporal_load((const i32x4*)(src + e0) + q4);
    const i32x4 dv4 = __builtin_nontemporal_load((const i32x4*)(dst + e0) + q4);
    u32x2 fq;
    asm volatile("global_load_dwordx2 %0, %1, %2" : "=&v"(fq) : "v"(0), "s"(freqs) : "memory");
    {
        const __amdgpu_buffer_rsrc_t rs_pos = __builtin_amdgcn_make_buffer_rsrc((void*)pos, 0, NN * 6, 0x00020000);
#pragma unroll
        for (int i = 0; i < 5; ++i) { const int p = wv + 16 * i; if (p < 74) bdma16(rs_pos, p * 1024 + lane * 16, 0, p * 1024); }
    }
    if (tid < OFS) hist[tid] = 0;
    if (tid == 0) *ncand = 0;
    asm volatile("s_waitcnt vmcnt(0) lgkmcnt(0)\n\ts_barrier" : "+v"(fq) :: "memory");
    STAMP(0, 1);
    if (4 * tid < EPB) {
        u32x2 pw[4][2]; unsigned j6[4][2];
#pragma unroll
        for (int j = 0; j < 4; ++j) {
            j6[j][0] = __umul24(sv4[j], 6); j6[j][1] = __umul24(dv4[j], 6);
            pw[j][0] = pos_words(j6[j][0]); pw[j][1] = pos_words(j6[j][1]);
        }
        asm volatile("" :: "v"(pw[0][0]), "v"(pw[0][1]), "v"(pw[1][0]), "v"(pw[1][1]), "v"(pw[2][0]), "v"(pw[2][1]), "v"(pw[3][0]), "v"(pw[3][1]));
        bool act[4];
        unsigned below = 0, total = 0;
#pragma unroll
        for (int j = 0; j < 4; ++j) {
            const Pos3 ps = unpack_pos(pw[j][0], j6[j][0]), pd = unpack_pos(pw[j][1], j6[j][1]);
            const float rx = ps.x - pd.x, ry = ps.y - pd.y, rz = ps.z - pd.z;
            const float ss = rx * rx + ry * ry + rz * rz;
            act[j] = __float_as_uint(ss) - 0x41d00000u > 0x7f7fffffu - 0x41d00000u;
            const unsigned long long m = ballot(act[j]);
            below = __builtin_amdgcn_mbcnt_hi((unsigned)(m >> 32), __builtin_amdgcn_mbcnt_lo((unsigned)m, below));
            total += __popcll(m);
        }
        unsigned run = 0;
        if (lane == 0) run = lds_add_rtn(L1_HIST + OFS * 4, total);
        unsigned la = L1_LIST + (__builtin_amdgcn_readfirstlane(run) + below) * 8;
#pragma unroll
        for (int j = 0; j < 4; ++j)
            if (act[j]) { *(lds_u32x2w_t)(size_t)la = (u32x2){(unsigned)sv4[j] | ((unsigned)dv4[j] << 16), (unsigned)(4 * tid + j)}; la += 8; }
    }
    __syncthreads();
    STAMP(0, 2);
    const int n = *ncand;
    const float f0 = bflo(fq[0]), f1 = bfhi(fq[0]), f2 = bflo(fq[1]), f3 = bfhi(fq[1]);
    u32x2 en = {0u, 0u};
    Rec rc; rc.act = false;
    unsigned rg = 0, slot = 0;
    if ((tid & ~63) < n) {
        en = list[tid];
        const bool valid = tid < n;
        en.x = valid ? en.x : 0u;
        rc = edge_record(en.x & 0xffff, en.x >> 16, f0, f1, f2, f3);
        rc.act &= valid;
        rg = (en.x >> 16) / RN;
        if (rc.act) slot = atomicAdd(&hist[rg], 1u);
    }
    for (int i = tid + T1; i < n; i += T1) {
        const u32x2 e = list[i];
        const Rec r = edge_record(e.x & 0xffff, e.x >> 16, f0, f1, f2, f3);
        list[i].y = e.y | ((r.act ? atomicAdd(&hist[(e.x >> 16) / RN], 1u) : 0xffffu) << 16);
    }
    __syncthreads();
    STAMP(0, 3);
    if (wv == 0) {
        constexpr int PER = OFS / 64;
        const u32x4 ca = *(const u32x4*)(hist + lane * PER), cb = *(const u32x4*)(hist + lane * PER + 4);
        unsigned c[PER] = {ca[0], ca[1], ca[2], ca[3], cb[0], cb[1], cb[2], cb[3]}, sum = 0;
#pragma unroll
        for (int k = 0; k < PER; ++k) sum += c[k];
        unsigned run = wave_incl_scan(sum) - sum;
        unsigned o[PER];
#pragma unroll
        for (int k = 0; k < PER; ++k) { o[k] = run; run += c[k]; }
        *(u32x4*)(hist + lane * PER) = (u32x4){o[0], o[1], o[2], o[3]};
        *(u32x4*)(hist + lane * PER + 4) = (u32x4){o[4], o[5], o[6], o[7]};
    }
    __syncthreads();
    unsigned c0 = 0, c1 = 0;
    if ((tid & ~63) < n) {
        c0 = hist[rg]; c1 = hist[rg + 1];
        if (rc.act) carr[c0 + slot] = (unsigned short)en.y;
    }
    for (int i = tid + T1; i < n; i += T1) {
        const u32x2 e = list[i];
        if ((e.y >> 16) != 0xffff) carr[hist[(e.x >> 16) / RN] + (e.y >> 16)] = (unsigned short)(e.y & 0xffff);
    }
    if (tid >= T1 - OFS && tid < T1 - OFS + NR) {
        const int r = tid - (T1 - OFS);
        const unsigned a0 = hist[r], a1 = hist[r + 1];
        ((unsigned*)(ws + WS_CELL))[r * 256 + (b & 7) * 32 + (b >> 3)] = a0 | ((a1 - a0) << 16);
    }
    __syncthreads();
    STAMP(0, 4);
    if ((tid & ~63) < n) {
        unsigned rank = 0;
        unsigned cv[4];
#pragma unroll
        for (int q = 0; q < 4; ++q) cv[q] = carr[min(c0 + q, (unsigned)EPB - 1)];
#pragma unroll
        for (int q = 0; q < 4; ++q) rank += (c0 + q < c1) & (cv[q] < en.y);
        if (rc.act) {
            for (unsigned q = c0 + 4; q < c1; ++q) rank += (carr[q] < en.y);
            const unsigned d = en.x >> 16;
            store_record(ws, e0 + c0 + rank, rc.f, (en.x & 0xffff) | ((d - rg * RN) << 16));
        }
    }
    for (int i = tid + T1; i < n; i += T1) {
        const u32x2 e = list[i];
        if ((e.y >> 16) == 0xffff) continue;
        const unsigned s = e.x & 0xffff, d = e.x >> 16, el = e.y & 0xffff, r = d / RN;
        const unsigned a0 = hist[r], a1 = hist[r + 1];
        unsigned rank = 0;
        for (unsigned q = a0; q < a1; ++q) rank += (carr[q] < el);
        const Rec rr = edge_record(s, d, f0, f1, f2, f3);
        store_record(ws, e0 + a0 + rank, rr.f, s | ((d - r * RN) << 16));
    }
    STAMP(0, 5);
    STAMP_FLUSH(0);
}

constexpr int L1_W1 = 0;
constexpr int L1_W2 = 32768;
constexpr int L1_X  = L1_W2 + 98304;
constexpr int L1_H0 = L1_X + GTL * 4096;
constexpr int OPITCH = 784;
constexpr int L1_GEMM_END = L1_H0 + 8192;
constexpr int K1_LDS = L1_GEMM_END > L1_EDGE_END ? L1_GEMM_END : L1_EDGE_END;
static_assert(32 * OPITCH <= 32768 && GTL == 6 && K1_LDS <= 163840, "");
__host__ __device__ constexpr int l1_h(int s) { return s == 0 ? L1_H0 : L1_X + (s - 1) * 8192; }
__host__ __device__ constexpr int l1_o(int s) { return s == 0 ? L1_W1 : L1_W2 + (s - 1) * 32768; }

__device__ __forceinline__ s16x4 tr_read(unsigned a) { return __builtin_amdgcn_ds_read_tr16_b64_v4i16((lds_s16x4_t)(size_t)a); }
__device__ __forceinline__ bf16x8 cat8(s16x4 a, s16x4 b) { return __builtin_shufflevector(a, b, 0, 1, 2, 3, 4, 5, 6, 7); }
typedef __attribute__((address_space(3))) u32x4* lds_u32x4_t;
__device__ __forceinline__ u32x4 lds_ld16(unsigned a) { return *(lds_u32x4_t)(size_t)a; }

#define PIN8(x) asm volatile("" :: "v"(x[0][0]), "v"(x[0][1]), "v"(x[0][2]), "v"(x[0][3]), "v"(x[1][0]), "v"(x[1][1]), "v"(x[1][2]), "v"(x[1][3]))
__device__ __forceinline__ void gemm_block(int g, const unsigned short* __restrict__ sv, const unsigned short* __restrict__ w1,
                                           const unsigned short* __restrict__ w2, char* __restrict__ ws) {
    const int tid = threadIdx.x;
    const int lane = tid & 63, wv = __builtin_amdgcn_readfirstlane(tid >> 6), slot = lane & 15, q = lane >> 4;
    const int w = wv & 7;
    const int n0 = g * GT;
    STAMP_DECL;
    STAMP(0, 0);
    {
        const __amdgpu_buffer_rsrc_t rs_x = __builtin_amdgcn_make_buffer_rsrc((void*)sv, 0, NN * 1024, 0x00020000);
        const __amdgpu_buffer_rsrc_t rs_w1 = __builtin_amdgcn_make_buffer_rsrc((void*)w1, 0, 128 * 256, 0x00020000);
        const __amdgpu_buffer_rsrc_t rs_w2 = __builtin_amdgcn_make_buffer_rsrc((void*)w2, 0, 128 * 768, 0x00020000);
        const unsigned o1 = q * 256 + ((((slot >> 1) ^ (q | ((wv & 2) << 1))) << 1) | (slot & 1)) * 16;
        bdma16(rs_w1, o1, wv * 1024, L1_W1 + wv * 1024);
        bdma16(rs_w1, o1, (wv + 16) * 1024, L1_W1 + (wv + 16) * 1024);
        const unsigned xo = slot * 1024 + q * 16 + (n0 + 16 * (wv >> 2)) * 1024 + (wv & 3) * 64;
        bdma16(rs_x, xo, 0, L1_X + wv * 1024);
        if (wv < 8) bdma16(rs_x, xo + 64 * 1024, 0, L1_X + (16 + wv) * 1024);
        asm volatile("s_barrier" ::: "memory");
#pragma unroll
        for (int s3 = 0; s3 < 3; ++s3) {
            const int c = 64 * s3 + lane, row = c / 48, cp = c - 48 * row, P = cp >> 1;
            const unsigned o2 = row * 768 + ((((P & ~7) | ((P & 7) ^ ((4 * (wv & 1) + row) & 7))) << 1) | (cp & 1)) * 16;
            bdma16(rs_w2, o2, wv * 3072, L1_W2 + wv * 3072 + s3 * 1024);
            bdma16(rs_w2, o2, (wv + 16) * 3072, L1_W2 + (wv + 16) * 3072 + s3 * 1024);
        }
    }
    const int qq = slot >> 2, p4 = slot & 3;
    asm volatile("s_waitcnt vmcnt(6)\n\ts_barrier" ::: "memory");
    STAMP(0, 1);
    constexpr int NP = GTL / 2;
    const __amdgpu_buffer_rsrc_t rs_phi = __builtin_amdgcn_make_buffer_rsrc((void*)(ws + WS_PHI), 0, NN * 1024, 0x00020000);
    auto ship = [&](int s) {
#pragma unroll
        for (int k = 0; k < 3; ++k) {
            const int c = 192 * w + 64 * k + lane, r = c / 48, cp = c - 48 * r;
            __builtin_amdgcn_raw_buffer_store_b128(lds_ld16(l1_o(s) + r * OPITCH + cp * 16), rs_phi, (n0 + 32 * s + r) * 1024 + cp * 16, 0, 16);
        }
    };
    if (wv < 8) {
        bf16x8 a1[4];
        {
            const unsigned base = L1_W1 + (8 * q + qq) * 256 + (((w ^ (qq | ((q & 1) << 2))) << 1) | (p4 >> 1)) * 16 + 8 * (p4 & 1);
#pragma unroll
            for (int ks = 0; ks < 4; ++ks) a1[ks] = cat8(tr_read(base + ks * 8192), tr_read(base + ks * 8192 + 1024));
        }
        const unsigned hw = (w >> 1) * 1024 + lane * 16 + (w & 1) * 8;
#pragma unroll
        for (int s = 0; s < NP + 1; ++s) {
            if (s >= 2) ship(s - 2);
            if (s < NP) {
                u32x4 xb[2][4];
#pragma unroll
                for (int i = 0; i < 2; ++i)
#pragma unroll
                    for (int ks = 0; ks < 4; ++ks) xb[i][ks] = lds_ld16(L1_X + ((2 * s + i) * 4 + ks) * 1024 + lane * 16);
                PIN8(xb);
                f32x4 acc[2] = {{0, 0, 0, 0}, {0, 0, 0, 0}};
#pragma unroll
                for (int ks = 0; ks < 4; ++ks)
#pragma unroll
                    for (int i = 0; i < 2; ++i) acc[i] = __builtin_amdgcn_mfma_f32_16x16x32_bf16(a1[ks], __builtin_bit_cast(bf16x8, xb[i][ks]), acc[i], 0, 0, 0);
#pragma unroll
                for (int i = 0; i < 2; ++i) {
                    const unsigned x01 = pack2(acc[i][0], acc[i][1]), x23 = pack2(acc[i][2], acc[i][3]);
                    const float x[4] = {bflo(x01), bfhi(x01), bflo(x23), bfhi(x23)};
                    float v[4];
#pragma unroll
                    for (int r = 0; r < 4; ++r) v[r] = x[r] * __builtin_amdgcn_rcpf(1.0f + __builtin_amdgcn_exp2f(-1.44269504f * x[r]));
                    *(lds_u32x2w_t)(size_t)(l1_h(s) + i * 4096 + hw) = (u32x2){pack2(v[0], v[1]), pack2(v[2], v[3])};
                }
            }
            if (s == 0) asm volatile("s_waitcnt vmcnt(0)" ::: "memory");
            raw_barrier();
            if (s == 0) STAMP(0, 2);
        }
        ship(NP - 1);
    } else {
        asm volatile("s_waitcnt vmcnt(0)" ::: "memory");
        raw_barrier();
        STAMP(0, 2);
        bf16x8 a2[3][4];
#pragma unroll
        for (int t = 0; t < 3; ++t) {
            const int P = 3 * w + t, row0 = 4 * q + qq;
            const unsigned base = L1_W2 + row0 * 768 + ((P & ~7) | ((P & 7) ^ (row0 & 7))) * 32 + (p4 >> 1) * 16 + 8 * (p4 & 1);
#pragma unroll
            for (int ks = 0; ks < 4; ++ks) a2[t][ks] = cat8(tr_read(base + ks * 24576), tr_read(base + ks * 24576 + 12288));
        }
        const unsigned obase = slot * OPITCH + (48 * w + 4 * q) * 2;
#pragma unroll
        for (int s = 1; s < NP + 1; ++s) {
            u32x4 hb[2][4];
#pragma unroll
            for (int i = 0; i < 2; ++i)
#pragma unroll
                for (int ks = 0; ks < 4; ++ks) hb[i][ks] = lds_ld16(l1_h(s - 1) + i * 4096 + ks * 1024 + lane * 16);
            PIN8(hb);
            f32x4 acc[2][3];
#pragma unroll
            for (int i = 0; i < 2; ++i)
#pragma unroll
                for (int t = 0; t < 3; ++t) acc[i][t] = (f32x4){0, 0, 0, 0};
#pragma unroll
            for (int i = 0; i < 2; ++i)
#pragma unroll
                for (int ks = 0; ks < 4; ++ks)
#pragma unroll
                    for (int t = 0; t < 3; ++t) acc[i][t] = __builtin_amdgcn_mfma_f32_16x16x32_bf16(a2[t][ks], __builtin_bit_cast(bf16x8, hb[i][ks]), acc[i][t], 0, 0, 0);
#pragma unroll
            for (int i = 0; i < 2; ++i)
#pragma unroll
                for (int t = 0; t < 3; ++t)
                    *(lds_u32x2w_t)(size_t)(l1_o(s - 1) + obase + i * 16 * OPITCH + t * 32) = (u32x2){pack2(acc[i][t][0], acc[i][t][1]), pack2(acc[i][t][2], acc[i][t][3])};
            raw_barrier();
            if (s == 1) STAMP(0, 3);
            if (s == 2) STAMP(0, 4);
        }
    }
    STAMP(0, 5);
    STAMP_FLUSH(0);
}

__global__ __launch_bounds__(T1) void k1_prepare(const unsigned short* __restrict__ sv, const unsigned short* __restrict__ pos,
                                                 const int* __restrict__ src, const int* __restrict__ dst,
                                                 const unsigned short* __restrict__ freqs, const unsigned short* __restrict__ w1,
                                                 const unsigned short* __restrict__ w2, char* __restrict__ ws) {
    extern __shared__ __attribute__((aligned(16))) char smem[];
    const int b = blockIdx.x;
    if (b < NB_E) edge_block(b, pos, src, dst, freqs, ws, smem);
    else gemm_block(b - NB_E, sv, w1, w2, ws);
}

constexpr int NCH     = CAP / 64;
constexpr int NITMAX  = CAP / 2 + RN;
constexpr int L2_U    = 0;
constexpr int L2_RES  = L2_U + (CAP + 1) * 32;
constexpr int L2_CNT  = L2_RES + (RN + 1) * 512;
constexpr int L2_SLOT = L2_CNT + 32 * NCH * 2;
constexpr int L2_MISC = L2_SLOT + (NITMAX + 1) * 16;
constexpr int K2_LDS  = L2_MISC + 32;
static_assert(L2_U == 0 && L2_RES % 16 == 0 && L2_CNT % 16 == 0 && L2_SLOT % 16 == 0 && NCH == 8 && RN <= 31 && CAP * 32 <= 0x4000, "");
constexpr unsigned PAD_A = CAP * 32, PAD_B = 0x3fffu << 10;
typedef __attribute__((ext_vector_type(2))) float f32x2;
typedef __attribute__((address_space(3))) const u32x2* lds_u32x2_t;
typedef __attribute__((address_space(3))) const f32x4* lds_f32x4_t;
typedef __attribute__((address_space(3))) const unsigned* lds_u32_t;
__device__ __forceinline__ u32x2 lds_rd64(unsigned a) { return *(lds_u32x2_t)(size_t)a; }
__device__ __forceinline__ f32x4 lds_rd128(unsigned a) { return *(lds_f32x4_t)(size_t)a; }
__device__ __forceinline__ unsigned lds_rd32(unsigned a) { return *(lds_u32_t)(size_t)a; }

struct K2Ctx {
    char* smem; char* ws;
    __amdgpu_buffer_rsrc_t rs_phi, rs_sv, rs_out;
    int tid, lane, wv, h, n0, nn, hh, c2;
    unsigned lo;
};

__device__ __forceinline__ void put_record(char* smem, int p, u32x4 f, unsigned mm) {
    const float fc = bflo(f.z);
    *(f32x4*)(smem + L2_U + p * 32) = (f32x4){bfhi(f.z), bflo(f.w), bfhi(f.w), __uint_as_float(mm)};
    *(u32x2*)(smem + L2_U + p * 32 + 16) = (u32x2){pack2(bflo(f.x) * fc, bfhi(f.x) * fc), pack2(bflo(f.y) * fc, bfhi(f.y) * fc)};
}
__device__ __forceinline__ void put_record_from(char* smem, const char* ws, int p, unsigned gi) {
    const char* rp = ws + WS_REC + (size_t)gi * 20;
    put_record(smem, p, *(const u32x4a4*)rp, *(const unsigned*)(rp + 16));
}
__device__ __forceinline__ unsigned sum2(unsigned x, unsigned c) { return (x & 0xffff) + (x >> 16) + c; }

__device__ __forceinline__ void build_slots(const K2Ctx& c, int m) {
    unsigned short* cnt16 = (unsigned short*)(c.smem + L2_CNT);
    u32x2* slots = (u32x2*)(c.smem + L2_SLOT);
    unsigned mm[NCH / W2], rk[NCH / W2];
#pragma unroll
    for (int i = 0; i < NCH / W2; ++i) {
        const int p = 64 * (c.wv + W2 * i) + c.lane;
        if (64 * (c.wv + W2 * i) < m) {
            const bool valid = p < m;
            mm[i] = lds_rd32(L2_U + min(p, CAP) * 32 + 12);
            const unsigned nl = mm[i] >> 16;
            unsigned long long mk = ballot(valid);
#pragma unroll
            for (int bit = 0; bit < 5; ++bit) { const bool on = (nl >> bit) & 1; const unsigned long long bb = ballot(on); mk &= on ? bb : ~bb; }
            rk[i] = lanes_below(mk);
            if (valid && rk[i] == 0) cnt16[nl * NCH + c.wv + W2 * i] = __popcll(mk);
        }
    }
    raw_barrier();
    if (c.wv == 0 || 64 * c.wv < m) {
        const u32x4 cv = *(const u32x4*)(cnt16 + (c.lane & 31) * NCH);
        const unsigned cnt = sum2(cv[0], sum2(cv[1], sum2(cv[2], sum2(cv[3], 0))));
        const unsigned its = c.lane < c.nn ? max(1u, (cnt + 1) >> 1) : 0u;
        const unsigned inc = wave_incl_scan(its), st = inc - its;
#pragma unroll
        for (int i = 0; i < NCH / W2; ++i) {
            const int ch = c.wv + W2 * i;
            if (64 * ch < m) {
                unsigned below = 0;
#pragma unroll
                for (int d = 0; d < 4; ++d) {
                    const unsigned msk = ch >= 2 * d + 2 ? 0xffffffffu : (ch == 2 * d + 1 ? 0x0000ffffu : 0u);
                    below = sum2(cv[d] & msk, below);
                }
                const unsigned nl = mm[i] >> 16;
                const unsigned a = __shfl((2 * st + below) | (below << 12) | (its << 22), nl);
                const unsigned rank = ((a >> 12) & 1023) + rk[i];
                const unsigned e = ((rank >> 1) + 1 == (a >> 22)) ? nl + 1 : 0u;
                const int p = 64 * ch + c.lane;
                if (p < m) slots[(a & 4095) + rk[i]] = (u32x2){(unsigned)p * 32 | e, (mm[i] & 0xffff) << 10};
            }
        }
        if (c.wv == 0) {
            if (c.lane < c.nn) {
                const u32x2 pe = {PAD_A | (c.lane + 1), PAD_B};
                if ((cnt & 1) | (cnt == 0)) slots[2 * inc - 1] = pe;
                if (cnt == 0) slots[2 * st] = pe;
            } else if (c.lane >= 62) slots[2 * NITMAX + c.lane - 62] = (u32x2){PAD_A, PAD_B};
            const unsigned I = __builtin_amdgcn_readlane(inc, 63);
            unsigned bound = I;
#pragma unroll
            for (int w = 0; w < W2; ++w) {
                const unsigned long long mw = ballot((st >= w * I / W2) & (c.lane < c.nn));
                const unsigned bw = mw ? __builtin_amdgcn_readlane(st, __builtin_ctzll(mw)) : I;
                bound = c.lane == w ? bw : bound;
            }
            if (c.lane <= W2) ((unsigned*)(c.smem + L2_MISC))[1 + c.lane] = bound;
        }
    }
}
__device__ __forceinline__ void read_span(const K2Ctx& c, int& ta, int& tb) {
    const u32x2 x = {lds_rd32(L2_MISC + 4 + 4 * c.wv), lds_rd32(L2_MISC + 8 + 4 * c.wv)};
    ta = __builtin_amdgcn_readfirstlane(x[0]); tb = __builtin_amdgcn_readfirstlane(x[1]);
}

__device__ __forceinline__ float vmul(float a, float b) { float d = a * b; asm("" : "+v"(d)); return d; }
__device__ __forceinline__ float vfma(float a, float b, float c) { float d = __builtin_fmaf(a, b, c); asm("" : "+v"(d)); return d; }
__device__ __forceinline__ f32x2 unpack2(unsigned u) { return (f32x2){bflo(u), bfhi(u)}; }
__device__ __forceinline__ void edge_fma(f32x2 (&acc)[4], const float (&W)[3][2], const unsigned (&ld)[6], f32x4 hat) {
    const f32x2 mv = {vmul(bflo(ld[1]), W[1][0]), vmul(bfhi(ld[1]), W[1][1])};
    const f32x2 mr = {vmul(bflo(ld[2]), W[2][0]), vmul(bfhi(ld[2]), W[2][1])};
    acc[0] = (f32x2){vfma(bflo(ld[0]), W[0][0], acc[0][0]), vfma(bfhi(ld[0]), W[0][1], acc[0][1])};
#pragma unroll
    for (int a = 0; a < 3; ++a) {
        acc[1 + a] = __builtin_elementwise_fma(mv, unpack2(ld[3 + a]), acc[1 + a]);
        acc[1 + a] = __builtin_elementwise_fma(mr, (f32x2){hat[a], hat[a]}, acc[1 + a]);
    }
}
__device__ __forceinline__ f32x4 filter4(u32x2 g, u32x2 wb) {
    return __builtin_amdgcn_mfma_f32_4x4x4bf16_1k(__builtin_bit_cast(s16x4, g), __builtin_bit_cast(s16x4, wb), (f32x4){0.f, 0.f, 0.f, 0.f}, 0, 0, 0);
}
__device__ __forceinline__ void half_reduce(const f32x2 (&acc)[4], f32x2 (&tot)[2]) {
#pragma unroll
    for (int q = 0; q < 2; ++q) {
        const auto s0 = __builtin_amdgcn_permlane32_swap(__float_as_uint(acc[q][0]), __float_as_uint(acc[q + 2][0]), false, false);
        const auto s1 = __builtin_amdgcn_permlane32_swap(__float_as_uint(acc[q][1]), __float_as_uint(acc[q + 2][1]), false, false);
        tot[q] = (f32x2){__uint_as_float(s0[0]), __uint_as_float(s1[0])} + (f32x2){__uint_as_float(s0[1]), __uint_as_float(s1[1])};
    }
}
__device__ __forceinline__ void store_rows(const K2Ctx& c, int nl, const f32x2 (&tot)[2]) {
    const unsigned ra = L2_RES + nl * 512 + c.hh * 256 + c.c2 * 4;
#pragma unroll
    for (int q = 0; q < 2; ++q) {
        const f32x2 o = unpack2(lds_rd32(ra + q * 128)) + tot[q];
        __builtin_amdgcn_raw_buffer_store_b32(pack2(o[0], o[1]), c.rs_out, c.hh * 512 + c.lo + q * 256, (c.n0 + nl) * 1024,   2);
    }
}

struct Batch { unsigned ld[4][6]; unsigned sa[4]; unsigned ga; };
__device__ __forceinline__ void batch_load(Batch& B, const K2Ctx& c, int t4, int tb) {
    unsigned off[4];
#pragma unroll
    for (int j = 0; j < 4; ++j) {
        const int t = (t4 + j < tb) ? t4 + j : NITMAX;
        const u32x2 x = lds_rd64(L2_SLOT + t * 16 + c.hh * 8);
        B.sa[j] = x[0]; off[j] = x[1] | c.lo;
    }
    { const int t = t4 + (c.lane & 3); B.ga = lds_rd32(L2_SLOT + (t < tb ? t : NITMAX) * 16 + c.hh * 8); }
#pragma unroll
    for (int j = 0; j < 4; ++j) {
        B.ld[j][0] = __builtin_amdgcn_raw_buffer_load_b32(c.rs_phi, off[j], 0, 0);
        B.ld[j][1] = __builtin_amdgcn_raw_buffer_load_b32(c.rs_phi, off[j] + 256, 0, 0);
        B.ld[j][2] = __builtin_amdgcn_raw_buffer_load_b32(c.rs_phi, off[j] + 512, 0, 0);
        B.ld[j][3] = __builtin_amdgcn_raw_buffer_load_b32(c.rs_sv, off[j] + 256, 0, 0);
        B.ld[j][4] = __builtin_amdgcn_raw_buffer_load_b32(c.rs_sv, off[j] + 512, 0, 0);
        B.ld[j][5] = __builtin_amdgcn_raw_buffer_load_b32(c.rs_sv, off[j] + 768, 0, 0);
    }
}
__device__ __forceinline__ void batch_compute(const Batch& B, const K2Ctx& c, f32x2 (&acc)[4], const u32x2 (&wb)[3][2], bool first) {
    const u32x2 g = lds_rd64((B.ga & 0x7fe0) + 16);
    f32x4 hat[4];
#pragma unroll
    for (int j = 0; j < 4; ++j) hat[j] = lds_rd128(B.sa[j] & 0x7fe0);
    f32x4 W4[3][2];
#pragma unroll
    for (int q = 0; q < 3; ++q) { W4[q][0] = filter4(g, wb[q][0]); W4[q][1] = filter4(g, wb[q][1]); }
#pragma unroll
    for (int j = 0; j < 4; ++j) {
        const float W[3][2] = {{W4[0][0][j], W4[0][1][j]}, {W4[1][0][j], W4[1][1][j]}, {W4[2][0][j], W4[2][1][j]}};
        edge_fma(acc, W, B.ld[j], hat[j]);
        if (j == 0 && first) asm volatile("s_barrier" : "+v"(acc[0]), "+v"(acc[1]), "+v"(acc[2]), "+v"(acc[3]) :: "memory");
        const int en = __builtin_amdgcn_readfirstlane(B.sa[j]) & 31;
        if (en) {
            f32x2 tot[2];
            half_reduce(acc, tot);
            store_rows(c, en - 1, tot);
#pragma unroll
            for (int a = 0; a < 4; ++a) acc[a] = (f32x2){0.f, 0.f};
        }
    }
}

__global__ __launch_bounds__(T2, 4) void k2_gather(const unsigned short* __restrict__ sv, const unsigned short* __restrict__ wr,
                                                   char* __restrict__ ws, char* __restrict__ out) {
    extern __shared__ __attribute__((aligned(16))) char smem[];
    K2Ctx c;
    c.smem = smem; c.ws = ws;
    c.tid = threadIdx.x; c.lane = c.tid & 63; c.wv = __builtin_amdgcn_readfirstlane(c.tid >> 6);
    c.h = blockIdx.x & 1; const int r = ((blockIdx.x >> 1) & 3) * NRQ + (blockIdx.x >> 3);
    c.n0 = r * RN; c.nn = min(RN, NN - c.n0);
    c.hh = c.lane >> 5; c.c2 = c.lane & 31;
    c.lo = c.h * 128 + c.c2 * 4;
    c.rs_phi = __builtin_amdgcn_make_buffer_rsrc((void*)(ws + WS_PHI), 0, NN * 1024, 0x00020000);
    c.rs_sv = __builtin_amdgcn_make_buffer_rsrc((void*)sv, 0, NN * 1024, 0x00020000);
    c.rs_out = __builtin_amdgcn_make_buffer_rsrc((void*)out, 0, NN * 1024, 0x00020000);
    const int lane = c.lane, tid = c.tid, wv = c.wv, h = c.h, hh = c.hh, c2 = c.c2;
    STAMP_DECL;
    STAMP(1, 0);
    int* tot_lds = (int*)(smem + L2_MISC);
    unsigned myce = 0, mycb = 0;
    const unsigned* cellp = (const unsigned*)(ws + WS_CELL) + r * 256 + (lane >> 4) * 32 + (lane & 15);
    unsigned ce0 = cellp[0], ce1 = cellp[128];
    u32x2 wb[3][2];
    {
        unsigned wraw[3][4];
#pragma unroll
        for (int p = 0; p < 3; ++p)
#pragma unroll
            for (int k = 0; k < 4; ++k) wraw[p][k] = *(const unsigned*)(wr + k * 384 + p * 128 + h * 64 + 2 * c2);
        if (wv >= 2) {
            ((unsigned*)(smem + L2_CNT))[tid - 128] = 0;
            if (tid < 130) ((f32x4*)(smem + L2_U))[2 * CAP + tid - 128] = (f32x4){0.f, 0.f, 0.f, 0.f};
            static_assert(32 * NCH * 2 == 128 * 4, "");
        }
#pragma unroll
        for (int p = 0; p < 3; ++p)
#pragma unroll
            for (int k = 0; k < 2; ++k) { wb[p][0][k] = __builtin_amdgcn_perm(wraw[p][2 * k + 1], wraw[p][2 * k], 0x05040100u); wb[p][1][k] = __builtin_amdgcn_perm(wraw[p][2 * k + 1], wraw[p][2 * k], 0x07060302u); }
        asm volatile("" : "+v"(wb[0][0]), "+v"(wb[0][1]), "+v"(wb[1][0]), "+v"(wb[1][1]), "+v"(wb[2][0]), "+v"(wb[2][1]));
    }
    if (wv < 2) {
        if (8 * (lane & 15) + (lane >> 4) >= NB_E) ce0 = 0;
        if (8 * (lane & 15) + (lane >> 4) + 4 >= NB_E) ce1 = 0;
        const unsigned cc0 = ce0 >> 16, cc1 = ce1 >> 16;
        const unsigned in0 = wave_incl_scan(cc0), t0 = __builtin_amdgcn_readlane(in0, 63);
        const unsigned in1 = wave_incl_scan(cc1) + t0;
        myce = wv == 0 ? ce0 : ce1;
        mycb = wv == 0 ? in0 - cc0 : in1 - cc1;
        if (tid == 63) *tot_lds = in1;
    }
    const unsigned mycc = myce >> 16;
    const unsigned gi0 = (8 * (tid & 15) + (tid >> 4)) * EPB + (myce & 0xffff);
    u32x4 rf[6]; unsigned rm[6];
    if (wv < 2) {
        const __amdgpu_buffer_rsrc_t rs_rec = __builtin_amdgcn_make_buffer_rsrc((void*)(ws + WS_REC), 0, NB_E * EPB * 20, 0x00020000);
#pragma unroll
        for (int k = 0; k < 6; ++k) {
            const unsigned go = ((unsigned)k < mycc && mycb + k < CAP) ? (gi0 + k) * 20 : 0x80000000u;
            asm volatile("buffer_load_dwordx4 %0, %2, %3, 0 offen\n\tbuffer_load_dword %1, %2, %3, 0 offen offset:16" : "=&v"(rf[k]), "=&v"(rm[k]) : "v"(go), "s"(rs_rec) : "memory");
        }
    }
    asm volatile("s_barrier" ::: "memory");
    if (wv >= 2) {
#pragma unroll
        for (int i4 = 0; i4 < (RN + 3) / 4; ++i4) {
            const int i = min(wv - 2 + 2 * i4, (RN + 1) / 2 - 1);
            const int node = min(c.n0 + 2 * i + hh, NN - 1);
            dma16_asm((const char*)sv + (size_t)node * 1024 + ((lane >> 3) & 3) * 256 + h * 128 + (lane & 7) * 16, L2_RES + i * 1024);
        }
    } else {
        asm volatile("s_waitcnt vmcnt(0)" : "+v"(rf[0]), "+v"(rf[1]), "+v"(rf[2]), "+v"(rf[3]), "+v"(rf[4]), "+v"(rf[5]),
                     "+v"(rm[0]), "+v"(rm[1]), "+v"(rm[2]), "+v"(rm[3]), "+v"(rm[4]), "+v"(rm[5]) :: "memory");
#pragma unroll
        for (int k = 0; k < 6; ++k) if ((unsigned)k < mycc && mycb + k < CAP) put_record(smem, mycb + k, rf[k], rm[k]);
        for (unsigned q = 6; q < mycc && mycb + q < CAP; ++q) put_record_from(smem, ws, mycb + q, gi0 + q);
    }
    __builtin_amdgcn_s_waitcnt(0xc07f);
    asm volatile("s_barrier" ::: "memory");
    STAMP(1, 2);
    const int total = __builtin_amdgcn_readfirstlane(*tot_lds);
    int ta, tb;
    build_slots(c, min(total, CAP));
    raw_barrier();
    read_span(c, ta, tb);
    STAMP(1, 3);
    f32x2 acc[4];
#pragma unroll
    for (int a = 0; a < 4; ++a) acc[a] = (f32x2){0.f, 0.f};

    if (total <= CAP) {
        Batch A, B;
        batch_load(A, c, ta, tb);
        STAMP(1, 4);
        __builtin_amdgcn_s_setprio(3);
        const int q1 = ta + (tb - ta) / 4, q2 = ta + (tb - ta) / 2, q3 = tb - (tb - ta) / 4;
        for (int t4 = ta; t4 == ta || t4 < tb; t4 += 8) {
            if (t4 >= q3) __builtin_amdgcn_s_setprio(0); else if (t4 >= q2) __builtin_amdgcn_s_setprio(1); else if (t4 >= q1) __builtin_amdgcn_s_setprio(2);
            batch_load(B, c, t4 + 4, tb);
            batch_compute(A, c, acc, wb, t4 == ta);
            if (t4 + 4 >= tb) break;
            batch_load(A, c, t4 + 8, tb);
            batch_compute(B, c, acc, wb, false);
        }
        STAMP(1, 5);
        STAMP_FLUSH(1);
    } else {
        float* accw = (float*)(ws + WS_ACC);
        asm volatile("s_waitcnt vmcnt(0)\n\ts_barrier" ::: "memory");
        for (int w0 = 0;;) {
            const bool firstw = (w0 == 0), lastw = (w0 + CAP >= total);
#pragma unroll 1
            for (int t = ta; t < tb; ++t) {
                const u32x2 sl = lds_rd64(L2_SLOT + t * 16 + hh * 8);
                const unsigned off = sl[1] | c.lo;
                unsigned ld[6];
#pragma unroll
                for (int a = 0; a < 3; ++a) { ld[a] = __builtin_amdgcn_raw_buffer_load_b32(c.rs_phi, off + a * 256, 0, 0); ld[3 + a] = __builtin_amdgcn_raw_buffer_load_b32(c.rs_sv, off + 256 + a * 256, 0, 0); }
                const u32x2 g = lds_rd64((sl[0] & 0x7fe0) + 16);
                float W[3][2];
#pragma unroll
                for (int q = 0; q < 3; ++q) { W[q][0] = filter4(g, wb[q][0])[0]; W[q][1] = filter4(g, wb[q][1])[0]; }
                edge_fma(acc, W, ld, lds_rd128(sl[0] & 0x7fe0));
                const int en = __builtin_amdgcn_readfirstlane(sl[0]) & 31;
                if (en) {
                    f32x2 tot[2];
                    half_reduce(acc, tot);
                    float* aw = accw + (((size_t)(c.n0 + en - 1) * 2 + h) * 4 + 2 * hh) * 64 + 2 * c2;
                    if (!firstw) {
#pragma unroll
                        for (int q = 0; q < 2; ++q) { tot[q][0] += aw[q * 64]; tot[q][1] += aw[q * 64 + 1]; }
                    }
                    if (lastw) store_rows(c, en - 1, tot);
                    else {
#pragma unroll
                        for (int q = 0; q < 2; ++q) { aw[q * 64] = tot[q][0]; aw[q * 64 + 1] = tot[q][1]; }
                    }
#pragma unroll
                    for (int a = 0; a < 4; ++a) acc[a] = (f32x2){0.f, 0.f};
                }
            }
            w0 += CAP;
            if (w0 >= total) break;
            __syncthreads();
            if (tid < 128) ((unsigned*)(smem + L2_CNT))[tid] = 0;
            for (unsigned q = 0; q < mycc; ++q) {
                const int p = (int)(mycb + q) - w0;
                if (p >= 0 && p < CAP) put_record_from(smem, ws, p, gi0 + q);
            }
            __syncthreads();
            build_slots(c, min(CAP, total - w0));
            __syncthreads();
            read_span(c, ta, tb);
        }
    }
}

extern "C" void kernel_launch(void* const* d_in, const int* in_sizes, int n_in, void* d_out, int out_size, void* d_ws, size_t ws_size,
                              hipStream_t stream) {
    const unsigned short* sv = (const unsigned short*)d_in[0];
    const unsigned short* pos = (const unsigned short*)d_in[1];
    const int* src = (const int*)d_in[2];
    const int* dst = (const int*)d_in[3];
    const unsigned short* freqs = (const unsigned short*)d_in[4];
    const unsigned short* w1 = (const unsigned short*)d_in[5];
    const unsigned short* w2 = (const unsigned short*)d_in[6];
    const unsigned short* wr = (const unsigned short*)d_in[7];
    (void)hipFuncSetAttribute((const void*)k1_prepare, hipFuncAttributeMaxDynamicSharedMemorySize, K1_LDS);
    (void)hipFuncSetAttribute((const void*)k2_gather, hipFuncAttributeMaxDynamicSharedMemorySize, K2_LDS);
    k1_prepare<<<NB_E + NB_G, T1, K1_LDS, stream>>>(sv, pos, src, dst, freqs, w1, w2, (char*)d_ws);
    k2_gather<<<NR * 2, T2, K2_LDS, stream>>>(sv, wr, (char*)d_ws, (char*)d_out);
}
```

```cpp
#include <hip/hip_runtime.h>
#include <hip/hip_bf16.h>
#include <stdint.h>

typedef __attribute__((ext_vector_type(8))) short bf16x8;
typedef __attribute__((ext_vector_type(4))) short s16x4;
typedef __attribute__((ext_vector_type(4))) float f32x4;
typedef __attribute__((ext_vector_type(4))) unsigned u32x4;
typedef __attribute__((ext_vector_type(4), aligned(4))) unsigned u32x4a4;
typedef __attribute__((ext_vector_type(2))) unsigned u32x2;
typedef __attribute__((ext_vector_type(4))) int i32x4;
typedef __attribute__((address_space(3))) void* lds_ptr_t;
typedef const __attribute__((address_space(1))) void* glb_ptr_t;
typedef __attribute__((address_space(3))) s16x4* lds_s16x4_t;

constexpr int NN   = 12500;
constexpr int NE   = 400000;
constexpr int T1   = 1024;
constexpr int EPB  = 3200;
constexpr int NB_E = NE / EPB;
constexpr int GTL  = 6;
constexpr int GT   = 16 * GTL;
constexpr int NB_G = (NN + GT - 1) / GT;
constexpr int WN   = 100;
constexpr int NRQ  = NN / WN;
constexpr int NR   = 4 * NRQ;
constexpr int NC   = 62;
constexpr int NRES = 32;
constexpr int OFS  = 512;
constexpr int T2   = 256;
constexpr int W2   = T2 / 64;
constexpr int CAP  = 512;
static_assert(NB_E <= 128 && NB_E * EPB == NE && T2 == 256 && NR % 4 == 0 && EPB % 4 == 0 && EPB <= 4 * T1, "");
static_assert(OFS >= NR + 1 && OFS <= T1 && NN % WN == 0 && NRQ == NB_E && WN <= 128, "");

constexpr size_t WS_PHI  = 0;
constexpr size_t WS_REC  = 12800000;
constexpr size_t WS_CELL = WS_REC + (size_t)NB_E * EPB * 20;
constexpr size_t WS_WMAP = WS_CELL + (size_t)NR * 1024;
constexpr size_t WS_ACC  = WS_WMAP + (size_t)NRQ * 256;
constexpr size_t WS_END  = WS_ACC + (size_t)NN * 512 * 4;
static_assert(WS_CELL % 128 == 0 && WS_ACC % 128 == 0, "");
#define STAMP_DECL do {} while (0)
#define STAMP(kid, slot) do {} while (0)
#define STAMP_FLUSH(kid) do {} while (0)

__device__ __forceinline__ float bf2f(unsigned h) { return __uint_as_float(h << 16); }
__device__ __forceinline__ float bflo(unsigned u) { return __uint_as_float(u << 16); }
__device__ __forceinline__ float bfhi(unsigned u) { return __uint_as_float(u & 0xffff0000u); }
typedef __attribute__((ext_vector_type(2))) __bf16 bf16x2_t;
typedef __attribute__((ext_vector_type(2))) float f32x2_t;
__device__ __forceinline__ unsigned pack2(float lo, float hi) { f32x2_t f = {lo, hi}; bf16x2_t b = __builtin_convertvector(f, bf16x2_t); return __builtin_bit_cast(unsigned, b); }
__device__ __forceinline__ float rbf(float x) { return bflo(pack2(x, 0.f)); }

__device__ __forceinline__ unsigned wave_incl_scan(unsigned x) {
    x += __builtin_amdgcn_update_dpp(0u, x, 0x111, 0xf, 0xf, false);
    x += __builtin_amdgcn_update_dpp(0u, x, 0x112, 0xf, 0xf, false);
    x += __builtin_amdgcn_update_dpp(0u, x, 0x114, 0xf, 0xf, false);
    x += __builtin_amdgcn_update_dpp(0u, x, 0x118, 0xf, 0xf, false);
    x += __builtin_amdgcn_update_dpp(0u, x, 0x142, 0xa, 0xf, false);
    x += __builtin_amdgcn_update_dpp(0u, x, 0x143, 0xc, 0xf, false);
    return x;
}
__device__ __forceinline__ unsigned lanes_below(unsigned long long m) {
    return __builtin_amdgcn_mbcnt_hi((unsigned)(m >> 32), __builtin_amdgcn_mbcnt_lo((unsigned)m, 0u));
}
__device__ __forceinline__ unsigned long long ballot(bool p) { return __builtin_amdgcn_ballot_w64(p); }
__device__ __forceinline__ unsigned lds_add_rtn(unsigned lds_addr, unsigned v) {
    unsigned old;
    asm volatile("ds_add_rtn_u32 %0, %1, %2\n\ts_waitcnt lgkmcnt(0)" : "=v"(old) : "v"(lds_addr), "v"(v) : "memory");
    return old;
}
__device__ __forceinline__ void dma16(const void* g, void* l) { __builtin_amdgcn_global_load_lds((glb_ptr_t)g, (lds_ptr_t)l, 16, 0, 0); }
__device__ __forceinline__ void dma4(const void* g, void* l) { __builtin_amdgcn_global_load_lds((glb_ptr_t)g, (lds_ptr_t)l, 4, 0, 0); }
__device__ __forceinline__ void dma16_asm(const void* g, unsigned lds_addr) {
    unsigned keep;
    asm volatile("s_mov_b32 %0, m0\n\ts_mov_b32 m0, %2\n\ts_nop 0\n\tglobal_load_lds_dwordx4 %1, off\n\ts_mov_b32 m0, %0" : "=&s"(keep) : "v"(g), "s"(lds_addr) : "memory");
}
__device__ __forceinline__ void bdma16(__amdgpu_buffer_rsrc_t rs, unsigned voff, unsigned soff, unsigned lds_addr) {
    unsigned keep;
    asm volatile("s_mov_b32 %0, m0\n\ts_mov_b32 m0, %4\n\ts_nop 0\n\tbuffer_load_dwordx4 %1, %2, %3 offen lds\n\ts_mov_b32 m0, %0" : "=&s"(keep) : "v"(voff), "s"(rs), "s"(soff), "s"(lds_addr) : "memory");
}
__device__ __forceinline__ void raw_barrier() { asm volatile("s_waitcnt lgkmcnt(0)\n\ts_barrier" ::: "memory"); }
__device__ __forceinline__ void store16_wt(void* p, u32x4 v) { asm volatile("global_store_dwordx4 %0, %1, off sc1\n\ts_nop 1" :: "v"(p), "v"(v) : "memory"); }

__device__ __forceinline__ float rev_frac(float x) {
    const float C_HI = 0.15915494f, C_LO = (float)(0.15915494309189535 - (double)0.15915494f);
    const float r = x * C_HI;
    const float e = __builtin_fmaf(x, C_HI, -r) + x * C_LO;
    return __builtin_amdgcn_fractf(r) + e;
}
__device__ __forceinline__ float fsin(float x) { return __builtin_amdgcn_sinf(rev_frac(x)); }
__device__ __forceinline__ float fcos(float x) { return __builtin_amdgcn_cosf(rev_frac(x)); }

struct Pos3 { float x, y, z; };
typedef __attribute__((address_space(3))) const unsigned* lds_cu32_t;
typedef __attribute__((address_space(3))) u32x2* lds_u32x2w_t;
__device__ __forceinline__ u32x2 pos_words(unsigned j6) { lds_cu32_t p = (lds_cu32_t)(size_t)(j6 & ~3u); return (u32x2){p[0], p[1]}; }
__device__ __forceinline__ Pos3 unpack_pos(u32x2 d, unsigned j6) {
    const unsigned xy = __builtin_amdgcn_alignbyte(d[1], d[0], j6), zz = __builtin_amdgcn_alignbyte(d[1], d[1], j6);
    Pos3 p;
    p.x = bflo(xy); p.y = bfhi(xy); p.z = bflo(zz);
    return p;
}
__device__ __forceinline__ Pos3 lds_pos(unsigned j) { const unsigned j6 = __umul24(j, 6); return unpack_pos(pos_words(j6), j6); }

struct Rec { u32x4 f; unsigned rg; bool act; };
__device__ __forceinline__ unsigned quadrant(float x, float y, float mx, float my) { return (x > mx ? 1u : 0u) | (y > my ? 2u : 0u); }
__device__ __forceinline__ Rec edge_record(unsigned s, unsigned d, float f0, float f1, float f2, float f3, float mx, float my) {
    const Pos3 ps = lds_pos(s), pd = lds_pos(d);
    const unsigned rxy = pack2(ps.x - pd.x, ps.y - pd.y);
    const float rx = bflo(rxy), ry = bfhi(rxy), rz = rbf(ps.z - pd.z);
    const unsigned sxy = pack2(rx * rx, ry * ry);
    const float ss = rbf(bflo(sxy) + bfhi(sxy) + rbf(rz * rz));
    const float dd = rbf(__builtin_amdgcn_sqrtf(rbf(ss + 1e-8f)));
    float fc = rbf(0.5f * rbf(rbf(fcos(rbf(rbf(3.14159265358979f * dd) * 0.2f))) + 1.0f));
    fc = (dd < 5.0f) ? fc : 0.0f * dd;
    const float inv = __builtin_amdgcn_rcpf(dd);
    const unsigned a01 = pack2(dd * f0, dd * f1), a23 = pack2(dd * f2, dd * f3);
    const unsigned s01 = pack2(fsin(bflo(a01)), fsin(bfhi(a01))), s23 = pack2(fsin(bflo(a23)), fsin(bfhi(a23)));
    const float b0 = bflo(s01) * inv, b1 = bfhi(s01) * inv, b2 = bflo(s23) * inv, b3 = bfhi(s23) * inv;
    Rec r;
    r.f = (u32x4){pack2(b0, b1), pack2(b2, b3), pack2(fc, rx * inv), pack2(ry * inv, rz * inv)};
    r.act = !(fc == 0.0f);
    r.rg = quadrant(pd.x, pd.y, mx, my) * NRQ + d / WN;
    return r;
}
__device__ __forceinline__ void store_record(char* ws, unsigned gi, u32x4 f, unsigned m) {
    char* p = ws + WS_REC + (size_t)gi * 20;
    *(u32x4a4*)p = f;
    *(unsigned*)(p + 16) = m;
}

constexpr int L1_HIST = 75008;
constexpr int L1_LIST = L1_HIST + OFS * 4 + 32;
constexpr int L1_CARR = L1_LIST + EPB * 8;
constexpr int L1_EDGE_END = L1_CARR + EPB * 2;

__device__ __forceinline__ void edge_block(int b, const unsigned short* __restrict__ pos, const int* __restrict__ src, const int* __restrict__ dst,
                                           const unsigned short* __restrict__ freqs, char* __restrict__ ws, char* smem) {
    unsigned* hist = (unsigned*)(smem + L1_HIST);
    unsigned* ncand = hist + OFS;
    u32x2* list = (u32x2*)(smem + L1_LIST);
    unsigned short* carr = (unsigned short*)(smem + L1_CARR);
    const int tid = threadIdx.x, lane = tid & 63, wv = __builtin_amdgcn_readfirstlane(tid >> 6);
    const int e0 = b * EPB;
    STAMP_DECL;
    STAMP(0, 0);
    const int q4 = min(tid, EPB / 4 - 1);
    const i32x4 sv4 = __builtin_nontemporal_load((const i32x4*)(src + e0) + q4);
    const i32x4 dv4 = __builtin_nontemporal_load((const i32x4*)(dst + e0) + q4);
    u32x2 fq;
    asm volatile("global_load_dwordx2 %0, %1, %2" : "=&v"(fq) : "v"(0), "s"(freqs) : "memory");
    {
        const __amdgpu_buffer_rsrc_t rs_pos = __builtin_amdgcn_make_buffer_rsrc((void*)pos, 0, NN * 6, 0x00020000);
#pragma unroll
        for (int i = 0; i < 5; ++i) { const int p = wv + 16 * i; if (p < 74) bdma16(rs_pos, p * 1024 + lane * 16, 0, p * 1024); }
    }
    if (tid < OFS) hist[tid] = 0;
    if (tid == 0) ncand[0] = 0;
    asm volatile("s_waitcnt vmcnt(0) lgkmcnt(0)\n\ts_barrier" : "+v"(fq) :: "memory");
    STAMP(0, 1);
    if (4 * tid < EPB) {
        u32x2 pw[4][2]; unsigned j6[4][2];
#pragma unroll
        for (int j = 0; j < 4; ++j) {
            j6[j][0] = __umul24(sv4[j], 6); j6[j][1] = __umul24(dv4[j], 6);
            pw[j][0] = pos_words(j6[j][0]); pw[j][1] = pos_words(j6[j][1]);
        }
        asm volatile("" :: "v"(pw[0][0]), "v"(pw[0][1]), "v"(pw[1][0]), "v"(pw[1][1]), "v"(pw[2][0]), "v"(pw[2][1]), "v"(pw[3][0]), "v"(pw[3][1]));
        bool act[4];
        unsigned below = 0, total = 0;
#pragma unroll
        for (int j = 0; j < 4; ++j) {
            const Pos3 ps = unpack_pos(pw[j][0], j6[j][0]), pd = unpack_pos(pw[j][1], j6[j][1]);
            const float rx = ps.x - pd.x, ry = ps.y - pd.y, rz = ps.z - pd.z;
            const float ss = rx * rx + ry * ry + rz * rz;
            act[j] = __float_as_uint(ss) - 0x41d00000u > 0x7f7fffffu - 0x41d00000u;
            const unsigned long long m = ballot(act[j]);
            below = __builtin_amdgcn_mbcnt_hi((unsigned)(m >> 32), __builtin_amdgcn_mbcnt_lo((unsigned)m, below));
            total += __popcll(m);
        }
        unsigned run = 0;
        if (lane == 0) run = lds_add_rtn(L1_HIST + OFS * 4, total);
        unsigned la = L1_LIST + (__builtin_amdgcn_readfirstlane(run) + below) * 8;
#pragma unroll
        for (int j = 0; j < 4; ++j)
            if (act[j]) { *(lds_u32x2w_t)(size_t)la = (u32x2){(unsigned)sv4[j] | ((unsigned)dv4[j] << 16), (unsigned)(4 * tid + j)}; la += 8; }
    } else if (wv >= 13) {
        float sx = 0.f, sy = 0.f;
#pragma unroll
        for (int k = 0; k < 16; ++k) {
            lds_cu32_t p = (lds_cu32_t)(size_t)(((tid - 832) + 192 * k) * 24);
            const unsigned d0 = p[0], d1 = p[1], d2 = p[2];
            sx += bflo(d0) + bfhi(d1); sy += bfhi(d0) + bflo(d2);
        }
        const float W[2] = {sx, sy};
        float tot[2];
#pragma unroll
        for (int k = 0; k < 2; ++k) {
            float x = W[k];
            x += __uint_as_float(__builtin_amdgcn_update_dpp(0u, __float_as_uint(x), 0x111, 0xf, 0xf, false));
            x += __uint_as_float(__builtin_amdgcn_update_dpp(0u, __float_as_uint(x), 0x112, 0xf, 0xf, false));
            x += __uint_as_float(__builtin_amdgcn_update_dpp(0u, __float_as_uint(x), 0x114, 0xf, 0xf, false));
            x += __uint_as_float(__builtin_amdgcn_update_dpp(0u, __float_as_uint(x), 0x118, 0xf, 0xf, false));
            x += __uint_as_float(__builtin_amdgcn_update_dpp(0u, __float_as_uint(x), 0x142, 0xa, 0xf, false));
            x += __uint_as_float(__builtin_amdgcn_update_dpp(0u, __float_as_uint(x), 0x143, 0xc, 0xf, false));
            tot[k] = x;
        }
        if (lane == 63) { ncand[2 + 2 * (wv - 13)] = __float_as_uint(tot[0]); ncand[3 + 2 * (wv - 13)] = __float_as_uint(tot[1]); }
    }
    __syncthreads();
    STAMP(0, 2);
    const int n = ncand[0];
    const float mx = (__uint_as_float(ncand[2]) + __uint_as_float(ncand[4]) + __uint_as_float(ncand[6])) * (1.0f / 6144.0f);
    const float my = (__uint_as_float(ncand[3]) + __uint_as_float(ncand[5]) + __uint_as_float(ncand[7])) * (1.0f / 6144.0f);
    if (wv == 15) {
        const int na = min(WN * b + lane, NN - 1), nb = min(WN * b + 64 + lane, NN - 1);
        const Pos3 pa = lds_pos(na), pb = lds_pos(nb);
        const unsigned qa = quadrant(pa.x, pa.y, mx, my), qb = lane + 64 < WN ? quadrant(pb.x, pb.y, mx, my) : 4u;
        unsigned ia = 0, ib = 0;
#pragma unroll
        for (unsigned q = 0; q < 4; ++q) {
            const unsigned long long ma = ballot(qa == q), mb = ballot(qb == q);
            ia = qa == q ? lanes_below(ma) : ia;
            ib = qb == q ? lanes_below(mb) + __popcll(ma) : ib;
        }
        unsigned short* wm = (unsigned short*)(ws + WS_WMAP) + b * 128;
        wm[lane] = (unsigned short)(qa << 8 | ia);
        wm[64 + lane] = (unsigned short)(qb << 8 | ib);
    }
    const float f0 = bflo(fq[0]), f1 = bfhi(fq[0]), f2 = bflo(fq[1]), f3 = bfhi(fq[1]);
    u32x2 en = {0u, 0u};
    Rec rc; rc.act = false;
    unsigned rg = 0, slot = 0;
    if ((tid & ~63) < n) {
        en = list[tid];
        const bool valid = tid < n;
        en.x = valid ? en.x : 0u;
        rc = edge_record(en.x & 0xffff, en.x >> 16, f0, f1, f2, f3, mx, my);
        rc.act &= valid;
        rg = rc.rg;
        if (rc.act) slot = atomicAdd(&hist[rg], 1u);
    }
    for (int i = tid + T1; i < n; i += T1) {
        const u32x2 e = list[i];
        const Rec r = edge_record(e.x & 0xffff, e.x >> 16, f0, f1, f2, f3, mx, my);
        list[i].y = e.y | ((r.act ? atomicAdd(&hist[r.rg], 1u) : 0xffffu) << 16);
    }
    __syncthreads();
    STAMP(0, 3);
    if (wv == 0) {
        constexpr int PER = OFS / 64;
        const u32x4 ca = *(const u32x4*)(hist + lane * PER), cb = *(const u32x4*)(hist + lane * PER + 4);
        unsigned c[PER] = {ca[0], ca[1], ca[2], ca[3], cb[0], cb[1], cb[2], cb[3]}, sum = 0;
#pragma unroll
        for (int k = 0; k < PER; ++k) sum += c[k];
        unsigned run = wave_incl_scan(sum) - sum;
        unsigned o[PER];
#pragma unroll
        for (int k = 0; k < PER; ++k) { o[k] = run; run += c[k]; }
        *(u32x4*)(hist + lane * PER) = (u32x4){o[0], o[1], o[2], o[3]};
        *(u32x4*)(hist + lane * PER + 4) = (u32x4){o[4], o[5], o[6], o[7]};
    }
    __syncthreads();
    unsigned c0 = 0, c1 = 0;
    if ((tid & ~63) < n) {
        c0 = hist[rg]; c1 = hist[rg + 1];
        if (rc.act) carr[c0 + slot] = (unsigned short)en.y;
    }
    for (int i = tid + T1; i < n; i += T1) {
        const u32x2 e = list[i];
        if ((e.y >> 16) != 0xffff) { const Pos3 pd = lds_pos(e.x >> 16); carr[hist[quadrant(pd.x, pd.y, mx, my) * NRQ + (e.x >> 16) / WN] + (e.y >> 16)] = (unsigned short)(e.y & 0xffff); }
    }
    if (tid >= T1 - OFS && tid < T1 - OFS + NR) {
        const int r = tid - (T1 - OFS);
        const unsigned a0 = hist[r], a1 = hist[r + 1];
        ((unsigned*)(ws + WS_CELL))[r * 256 + (b & 7) * 32 + (b >> 3)] = a0 | ((a1 - a0) << 16);
    }
    __syncthreads();
    STAMP(0, 4);
    if ((tid & ~63) < n) {
        unsigned rank = 0;
        unsigned cv[4];
#pragma unroll
        for (int q = 0; q < 4; ++q) cv[q] = carr[min(c0 + q, (unsigned)EPB - 1)];
#pragma unroll
        for (int q = 0; q < 4; ++q) rank += (c0 + q < c1) & (cv[q] < en.y);
        if (rc.act) {
            for (unsigned q = c0 + 4; q < c1; ++q) rank += (carr[q] < en.y);
            const unsigned d = en.x >> 16;
            store_record(ws, e0 + c0 + rank, rc.f, (en.x & 0xffff) | ((d % WN) << 16));
        }
    }
    for (int i = tid + T1; i < n; i += T1) {
        const u32x2 e = list[i];
        if ((e.y >> 16) == 0xffff) continue;
        const unsigned s = e.x & 0xffff, d = e.x >> 16, el = e.y & 0xffff;
        const Rec rr = edge_record(s, d, f0, f1, f2, f3, mx, my);
        const unsigned a0 = hist[rr.rg], a1 = hist[rr.rg + 1];
        unsigned rank = 0;
        for (unsigned q = a0; q < a1; ++q) rank += (carr[q] < el);
        store_record(ws, e0 + a0 + rank, rr.f, s | ((d % WN) << 16));
    }
    STAMP(0, 5);
    STAMP_FLUSH(0);
}

constexpr int L1_W1 = 0;
constexpr int L1_W2 = 32768;
constexpr int L1_X  = L1_W2 + 98304;
constexpr int L1_H0 = L1_X + GTL * 4096;
constexpr int OPITCH = 784;
constexpr int L1_GEMM_END = L1_H0 + 8192;
constexpr int K1_LDS = L1_GEMM_END > L1_EDGE_END ? L1_GEMM_END : L1_EDGE_END;
static_assert(32 * OPITCH <= 32768 && GTL == 6 && K1_LDS <= 163840, "");
__host__ __device__ constexpr int l1_h(int s) { return s == 0 ? L1_H0 : L1_X + (s - 1) * 8192; }
__host__ __device__ constexpr int l1_o(int s) { return s == 0 ? L1_W1 : L1_W2 + (s - 1) * 32768; }

__device__ __forceinline__ s16x4 tr_read(unsigned a) { return __builtin_amdgcn_ds_read_tr16_b64_v4i16((lds_s16x4_t)(size_t)a); }
__device__ __forceinline__ bf16x8 cat8(s16x4 a, s16x4 b) { return __builtin_shufflevector(a, b, 0, 1, 2, 3, 4, 5, 6, 7); }
typedef __attribute__((address_space(3))) u32x4* lds_u32x4_t;
__device__ __forceinline__ u32x4 lds_ld16(unsigned a) { return *(lds_u32x4_t)(size_t)a; }

#define PIN8(x) asm volatile("" :: "v"(x[0][0]), "v"(x[0][1]), "v"(x[0][2]), "v"(x[0][3]), "v"(x[1][0]), "v"(x[1][1]), "v"(x[1][2]), "v"(x[1][3]))
__device__ __forceinline__ void gemm_block(int g, const unsigned short* __restrict__ sv, const unsigned short* __restrict__ w1,
                                           const unsigned short* __restrict__ w2, char* __restrict__ ws) {
    const int tid = threadIdx.x;
    const int lane = tid & 63, wv = __builtin_amdgcn_readfirstlane(tid >> 6), slot = lane & 15, q = lane >> 4;
    const int w = wv & 7;
    const int n0 = g * GT;
    STAMP_DECL;
    STAMP(0, 0);
    {
        const __amdgpu_buffer_rsrc_t rs_x = __builtin_amdgcn_make_buffer_rsrc((void*)sv, 0, NN * 1024, 0x00020000);
        const __amdgpu_buffer_rsrc_t rs_w1 = __builtin_amdgcn_make_buffer_rsrc((void*)w1, 0, 128 * 256, 0x00020000);
        const __amdgpu_buffer_rsrc_t rs_w2 = __builtin_amdgcn_make_buffer_rsrc((void*)w2, 0, 128 * 768, 0x00020000);
        const unsigned o1 = q * 256 + ((((slot >> 1) ^ (q | ((wv & 2) << 1))) << 1) | (slot & 1)) * 16;
        bdma16(rs_w1, o1, wv * 1024, L1_W1 + wv * 1024);
        bdma16(rs_w1, o1, (wv + 16) * 1024, L1_W1 + (wv + 16) * 1024);
        const unsigned xo = slot * 1024 + q * 16 + (n0 + 16 * (wv >> 2)) * 1024 + (wv & 3) * 64;
        bdma16(rs_x, xo, 0, L1_X + wv * 1024);
        if (wv < 8) bdma16(rs_x, xo + 64 * 1024, 0, L1_X + (16 + wv) * 1024);
        asm volatile("s_barrier" ::: "memory");
#pragma unroll
        for (int s3 = 0; s3 < 3; ++s3) {
            const int c = 64 * s3 + lane, row = c / 48, cp = c - 48 * row, P = cp >> 1;
            const unsigned o2 = row * 768 + ((((P & ~7) | ((P & 7) ^ ((4 * (wv & 1) + row) & 7))) << 1) | (cp & 1)) * 16;
            bdma16(rs_w2, o2, wv * 3072, L1_W2 + wv * 3072 + s3 * 1024);
            bdma16(rs_w2, o2, (wv + 16) * 3072, L1_W2 + (wv + 16) * 3072 + s3 * 1024);
        }
    }
    const int qq = slot >> 2, p4 = slot & 3;
    asm volatile("s_waitcnt vmcnt(6)\n\ts_barrier" ::: "memory");
    STAMP(0, 1);
    constexpr int NP = GTL / 2;
    const __amdgpu_buffer_rsrc_t rs_phi = __builtin_amdgcn_make_buffer_rsrc((void*)(ws + WS_PHI), 0, NN * 1024, 0x00020000);
    auto ship = [&](int s) {
#pragma unroll
        for (int k = 0; k < 3; ++k) {
            const int c = 192 * w + 64 * k + lane, r = c / 48, cp = c - 48 * r;
            __builtin_amdgcn_raw_buffer_store_b128(lds_ld16(l1_o(s) + r * OPITCH + cp * 16), rs_phi, (n0 + 32 * s + r) * 1024 + cp * 16, 0, 16);
        }
    };
    if (wv < 8) {
        bf16x8 a1[4];
        {
            const unsigned base = L1_W1 + (8 * q + qq) * 256 + (((w ^ (qq | ((q & 1) << 2))) << 1) | (p4 >> 1)) * 16 + 8 * (p4 & 1);
#pragma unroll
            for (int ks = 0; ks < 4; ++ks) a1[ks] = cat8(tr_read(base + ks * 8192), tr_read(base + ks * 8192 + 1024));
        }
        const unsigned hw = (w >> 1) * 1024 + lane * 16 + (w & 1) * 8;
#pragma unroll
        for (int s = 0; s < NP + 1; ++s) {
            if (s >= 2) ship(s - 2);
            if (s < NP) {
                u32x4 xb[2][4];
#pragma unroll
                for (int i = 0; i < 2; ++i)
#pragma unroll
                    for (int ks = 0; ks < 4; ++ks) xb[i][ks] = lds_ld16(L1_X + ((2 * s + i) * 4 + ks) * 1024 + lane * 16);
                PIN8(xb);
                f32x4 acc[2] = {{0, 0, 0, 0}, {0, 0, 0, 0}};
#pragma unroll
                for (int ks = 0; ks < 4; ++ks)
#pragma unroll
                    for (int i = 0; i < 2; ++i) acc[i] = __builtin_amdgcn_mfma_f32_16x16x32_bf16(a1[ks], __builtin_bit_cast(bf16x8, xb[i][ks]), acc[i], 0, 0, 0);
#pragma unroll
                for (int i = 0; i < 2; ++i) {
                    const unsigned x01 = pack2(acc[i][0], acc[i][1]), x23 = pack2(acc[i][2], acc[i][3]);
                    const float x[4] = {bflo(x01), bfhi(x01), bflo(x23), bfhi(x23)};
                    float v[4];
#pragma unroll
                    for (int r = 0; r < 4; ++r) v[r] = x[r] * __builtin_amdgcn_rcpf(1.0f + __builtin_amdgcn_exp2f(-1.44269504f * x[r]));
                    *(lds_u32x2w_t)(size_t)(l1_h(s) + i * 4096 + hw) = (u32x2){pack2(v[0], v[1]), pack2(v[2], v[3])};
                }
            }
            if (s == 0) asm volatile("s_waitcnt vmcnt(0)" ::: "memory");
            raw_barrier();
            if (s == 0) STAMP(0, 2);
        }
        ship(NP - 1);
    } else {
        asm volatile("s_waitcnt vmcnt(0)" ::: "memory");
        raw_barrier();
        STAMP(0, 2);
        bf16x8 a2[3][4];
#pragma unroll
        for (int t = 0; t < 3; ++t) {
            const int P = 3 * w + t, row0 = 4 * q + qq;
            const unsigned base = L1_W2 + row0 * 768 + ((P & ~7) | ((P & 7) ^ (row0 & 7))) * 32 + (p4 >> 1) * 16 + 8 * (p4 & 1);
#pragma unroll
            for (int ks = 0; ks < 4; ++ks) a2[t][ks] = cat8(tr_read(base + ks * 24576), tr_read(base + ks * 24576 + 12288));
        }
        const unsigned obase = slot * OPITCH + (48 * w + 4 * q) * 2;
#pragma unroll
        for (int s = 1; s < NP + 1; ++s) {
            u32x4 hb[2][4];
#pragma unroll
            for (int i = 0; i < 2; ++i)
#pragma unroll
                for (int ks = 0; ks < 4; ++ks) hb[i][ks] = lds_ld16(l1_h(s - 1) + i * 4096 + ks * 1024 + lane * 16);
            PIN8(hb);
            f32x4 acc[2][3];
#pragma unroll
            for (int i = 0; i < 2; ++i)
#pragma unroll
                for (int t = 0; t < 3; ++t) acc[i][t] = (f32x4){0, 0, 0, 0};
#pragma unroll
            for (int i = 0; i < 2; ++i)
#pragma unroll
                for (int ks = 0; ks < 4; ++ks)
#pragma unroll
                    for (int t = 0; t < 3; ++t) acc[i][t] = __builtin_amdgcn_mfma_f32_16x16x32_bf16(a2[t][ks], __builtin_bit_cast(bf16x8, hb[i][ks]), acc[i][t], 0, 0, 0);
#pragma unroll
            for (int i = 0; i < 2; ++i)
#pragma unroll
                for (int t = 0; t < 3; ++t)
                    *(lds_u32x2w_t)(size_t)(l1_o(s - 1) + obase + i * 16 * OPITCH + t * 32) = (u32x2){pack2(acc[i][t][0], acc[i][t][1]), pack2(acc[i][t][2], acc[i][t][3])};
            raw_barrier();
            if (s == 1) STAMP(0, 3);
            if (s == 2) STAMP(0, 4);
        }
    }
    STAMP(0, 5);
    STAMP_FLUSH(0);
}

__global__ __launch_bounds__(T1) void k1_prepare(const unsigned short* __restrict__ sv, const unsigned short* __restrict__ pos,
                                                 const int* __restrict__ src, const int* __restrict__ dst,
                                                 const unsigned short* __restrict__ freqs, const unsigned short* __restrict__ w1,
                                                 const unsigned short* __restrict__ w2, char* __restrict__ ws) {
    extern __shared__ __attribute__((aligned(16))) char smem[];
    const int b = blockIdx.x;
    if (b < NB_E) edge_block(b, pos, src, dst, freqs, ws, smem);
    else gemm_block(b - NB_E, sv, w1, w2, ws);
}

constexpr int NCH     = CAP / 64;
constexpr int NITMAX  = CAP / 2 + NC;
constexpr int L2_U    = 0;
constexpr int L2_RES  = L2_U + (CAP + 1) * 32;
constexpr int L2_CNT  = L2_RES + NRES * 512;
constexpr int L2_SLOT = L2_CNT + 64 * NCH * 2;
constexpr int L2_MAP  = L2_SLOT + (NITMAX + 1) * 16;
constexpr int L2_NOF  = L2_MAP + 128;
constexpr int L2_MISC = L2_NOF + 128;
constexpr int K2_LDS  = L2_MISC + 32;
static_assert(L2_U == 0 && L2_RES % 16 == 0 && L2_CNT % 16 == 0 && L2_SLOT % 16 == 0 && NCH == 8 && NC <= 62 && CAP * 32 <= 0x4000 && 4 * K2_LDS <= 163840, "");
constexpr unsigned PAD_A = CAP * 32, PAD_B = 0x3fffu << 10;
typedef __attribute__((ext_vector_type(2))) float f32x2;
typedef __attribute__((address_space(3))) const u32x2* lds_u32x2_t;
typedef __attribute__((address_space(3))) const f32x4* lds_f32x4_t;
typedef __attribute__((address_space(3))) const unsigned* lds_u32_t;
typedef __attribute__((address_space(3))) unsigned char* lds_u8_t;
__device__ __forceinline__ u32x2 lds_rd64(unsigned a) { return *(lds_u32x2_t)(size_t)a; }
__device__ __forceinline__ f32x4 lds_rd128(unsigned a) { return *(lds_f32x4_t)(size_t)a; }
__device__ __forceinline__ unsigned lds_rd32(unsigned a) { return *(lds_u32_t)(size_t)a; }
__device__ __forceinline__ unsigned lds_rd8(unsigned a) { return *(lds_u8_t)(size_t)a; }
__device__ __forceinline__ void lds_wr8(unsigned a, unsigned v) { *(lds_u8_t)(size_t)a = (unsigned char)v; }

struct K2Ctx {
    char* smem; char* ws;
    __amdgpu_buffer_rsrc_t rs_phi, rs_sv, rs_out;
    int tid, lane, wv, h, n0, hh, c2;
    unsigned lo;
};

__device__ __forceinline__ void put_record(char* smem, int p, u32x4 f, unsigned mm) {
    const float fc = bflo(f.z);
    const unsigned ni = lds_rd8(L2_MAP + (mm >> 16));
    *(f32x4*)(smem + L2_U + p * 32) = (f32x4){bfhi(f.z), bflo(f.w), bfhi(f.w), __uint_as_float((mm & 0xffff) | (ni << 16))};
    *(u32x2*)(smem + L2_U + p * 32 + 16) = (u32x2){pack2(bflo(f.x) * fc, bfhi(f.x) * fc), pack2(bflo(f.y) * fc, bfhi(f.y) * fc)};
}
__device__ __forceinline__ void put_record_from(char* smem, const char* ws, int p, unsigned gi) {
    const char* rp = ws + WS_REC + (size_t)gi * 20;
    put_record(smem, p, *(const u32x4a4*)rp, *(const unsigned*)(rp + 16));
}
__device__ __forceinline__ unsigned sum2(unsigned x, unsigned c) { return (x & 0xffff) + (x >> 16) + c; }

__device__ __forceinline__ void build_slots(const K2Ctx& c, int m, unsigned g0, int nn) {
    unsigned short* cnt16 = (unsigned short*)(c.smem + L2_CNT);
    u32x2* slots = (u32x2*)(c.smem + L2_SLOT);
    unsigned mm[NCH / W2], rk[NCH / W2];
    bool ok[NCH / W2];
#pragma unroll
    for (int i = 0; i < NCH / W2; ++i) {
        const int p = 64 * (c.wv + W2 * i) + c.lane;
        ok[i] = false;
        if (64 * (c.wv + W2 * i) < m) {
            mm[i] = lds_rd32(L2_U + min(p, CAP) * 32 + 12) - (g0 << 16);
            const unsigned nl = mm[i] >> 16;
            ok[i] = (p < m) & (nl < (unsigned)nn);
            unsigned long long mk = ballot(ok[i]);
#pragma unroll
            for (int bit = 0; bit < 6; ++bit) { const bool on = (nl >> bit) & 1; const unsigned long long bb = ballot(on); mk &= on ? bb : ~bb; }
            rk[i] = lanes_below(mk);
            if (ok[i] && rk[i] == 0) cnt16[nl * NCH + c.wv + W2 * i] = __popcll(mk);
        }
    }
    raw_barrier();
    if (c.wv == 0 || 64 * c.wv < m) {
        const u32x4 cv = *(const u32x4*)(cnt16 + c.lane * NCH);
        const unsigned cnt = sum2(cv[0], sum2(cv[1], sum2(cv[2], sum2(cv[3], 0))));
        const unsigned its = c.lane < nn ? max(1u, (cnt + 1) >> 1) : 0u;
        const unsigned inc = wave_incl_scan(its), st = inc - its;
        const unsigned id = (c.lane << 15) | (lds_rd8(L2_NOF + ((g0 + c.lane) & 127)) << 21);
#pragma unroll
        for (int i = 0; i < NCH / W2; ++i) {
            const int ch = c.wv + W2 * i;
            if (64 * ch < m) {
                unsigned below = 0;
#pragma unroll
                for (int d = 0; d < 4; ++d) {
                    const unsigned msk = ch >= 2 * d + 2 ? 0xffffffffu : (ch == 2 * d + 1 ? 0x0000ffffu : 0u);
                    below = sum2(cv[d] & msk, below);
                }
                const unsigned nl = (mm[i] >> 16) & 63;
                const unsigned a = __shfl((2 * st + below) | (below << 12) | (its << 22), nl);
                const unsigned idn = __shfl(id, nl);
                const unsigned rank = ((a >> 12) & 1023) + rk[i];
                const unsigned e = ((rank >> 1) + 1 == (a >> 22)) ? 1u : 0u;
                const int p = 64 * ch + c.lane;
                if (ok[i]) slots[(a & 4095) + rk[i]] = (u32x2){(unsigned)p * 32 | e | idn, (mm[i] & 0xffff) << 10};
            }
        }
        if (c.wv == 0) {
            if (c.lane < nn) {
                const u32x2 pe = {PAD_A | 1u | id, PAD_B};
                if ((cnt & 1) | (cnt == 0)) slots[2 * inc - 1] = pe;
                if (cnt == 0) slots[2 * st] = pe;
            } else if (c.lane >= 62) slots[2 * NITMAX + c.lane - 62] = (u32x2){PAD_A, PAD_B};
            const unsigned I = __builtin_amdgcn_readlane(inc, 63);
            unsigned bound = I;
#pragma unroll
            for (int w = 0; w < W2; ++w) {
                const unsigned long long mw = ballot((st >= w * I / W2) & (c.lane < nn));
                const unsigned bw = mw ? __builtin_amdgcn_readlane(st, __builtin_ctzll(mw)) : I;
                bound = c.lane == w ? bw : bound;
            }
            if (c.lane <= W2) ((unsigned*)(c.smem + L2_MISC))[1 + c.lane] = bound;
        }
    }
}
__device__ __forceinline__ void read_span(const K2Ctx& c, int& ta, int& tb) {
    const u32x2 x = {lds_rd32(L2_MISC + 4 + 4 * c.wv), lds_rd32(L2_MISC + 8 + 4 * c.wv)};
    ta = __builtin_amdgcn_readfirstlane(x[0]); tb = __builtin_amdgcn_readfirstlane(x[1]);
}

__device__ __forceinline__ float vmul(float a, float b) { float d = a * b; asm("" : "+v"(d)); return d; }
__device__ __forceinline__ float vfma(float a, float b, float c) { float d = __builtin_fmaf(a, b, c); asm("" : "+v"(d)); return d; }
__device__ __forceinline__ f32x2 unpack2(unsigned u) { return (f32x2){bflo(u), bfhi(u)}; }
__device__ __forceinline__ void edge_fma(f32x2 (&acc)[4], const float (&W)[3][2], const unsigned (&ld)[6], f32x4 hat) {
    const f32x2 mv = {vmul(bflo(ld[1]), W[1][0]), vmul(bfhi(ld[1]), W[1][1])};
    const f32x2 mr = {vmul(bflo(ld[2]), W[2][0]), vmul(bfhi(ld[2]), W[2][1])};
    acc[0] = (f32x2){vfma(bflo(ld[0]), W[0][0], acc[0][0]), vfma(bfhi(ld[0]), W[0][1], acc[0][1])};
#pragma unroll
    for (int a = 0; a < 3; ++a) {
        acc[1 + a] = __builtin_elementwise_fma(mv, unpack2(ld[3 + a]), acc[1 + a]);
        acc[1 + a] = __builtin_elementwise_fma(mr, (f32x2){hat[a], hat[a]}, acc[1 + a]);
    }
}
__device__ __forceinline__ f32x4 filter4(u32x2 g, u32x2 wb) {
    return __builtin_amdgcn_mfma_f32_4x4x4bf16_1k(__builtin_bit_cast(s16x4, g), __builtin_bit_cast(s16x4, wb), (f32x4){0.f, 0.f, 0.f, 0.f}, 0, 0, 0);
}
__device__ __forceinline__ void half_reduce(const f32x2 (&acc)[4], f32x2 (&tot)[2]) {
#pragma unroll
    for (int q = 0; q < 2; ++q) {
        const auto s0 = __builtin_amdgcn_permlane32_swap(__float_as_uint(acc[q][0]), __float_as_uint(acc[q + 2][0]), false, false);
        const auto s1 = __builtin_amdgcn_permlane32_swap(__float_as_uint(acc[q][1]), __float_as_uint(acc[q + 2][1]), false, false);
        tot[q] = (f32x2){__uint_as_float(s0[0]), __uint_as_float(s1[0])} + (f32x2){__uint_as_float(s0[1]), __uint_as_float(s1[1])};
    }
}
__device__ __forceinline__ void store_rows(const K2Ctx& c, unsigned sw, const f32x2 (&tot)[2], bool staged) {
    const unsigned ni = (sw >> 15) & 63, row0 = (c.n0 + ((sw >> 21) & 127)) * 1024;
    const unsigned vo = c.hh * 512 + c.lo;
    unsigned rs[2];
    if (staged && ni < NRES) {
        const unsigned ra = L2_RES + ni * 512 + c.hh * 256 + c.c2 * 4;
        rs[0] = lds_rd32(ra); rs[1] = lds_rd32(ra + 128);
    } else {
        asm volatile("buffer_load_dword %0, %2, %3, %4 offen\n\tbuffer_load_dword %1, %2, %3, %4 offen offset:256\n\ts_waitcnt vmcnt(0)"
                     : "=&v"(rs[0]), "=&v"(rs[1]) : "v"(vo), "s"(c.rs_sv), "s"(row0) : "memory");
    }
#pragma unroll
    for (int q = 0; q < 2; ++q) {
        const f32x2 o = unpack2(rs[q]) + tot[q];
        __builtin_amdgcn_raw_buffer_store_b32(pack2(o[0], o[1]), c.rs_out, vo + q * 256, row0,   2);
    }
}

struct Batch { unsigned ld[4][6]; };
__device__ __forceinline__ void batch_load(Batch& B, const K2Ctx& c, int t4, int tb) {
    unsigned off[4];
#pragma unroll
    for (int j = 0; j < 4; ++j) {
        const int t = (t4 + j < tb) ? t4 + j : NITMAX;
        off[j] = lds_rd32(L2_SLOT + t * 16 + c.hh * 8 + 4) | c.lo;
    }
#pragma unroll
    for (int j = 0; j < 4; ++j) {
        B.ld[j][0] = __builtin_amdgcn_raw_buffer_load_b32(c.rs_phi, off[j], 0, 0);
        B.ld[j][1] = __builtin_amdgcn_raw_buffer_load_b32(c.rs_phi, off[j] + 256, 0, 0);
        B.ld[j][2] = __builtin_amdgcn_raw_buffer_load_b32(c.rs_phi, off[j] + 512, 0, 0);
        B.ld[j][3] = __builtin_amdgcn_raw_buffer_load_b32(c.rs_sv, off[j] + 256, 0, 0);
        B.ld[j][4] = __builtin_amdgcn_raw_buffer_load_b32(c.rs_sv, off[j] + 512, 0, 0);
        B.ld[j][5] = __builtin_amdgcn_raw_buffer_load_b32(c.rs_sv, off[j] + 768, 0, 0);
    }
}
__device__ __forceinline__ void batch_compute(const Batch& B, const K2Ctx& c, f32x2 (&acc)[4], const u32x2 (&wb)[3][2], int t4, int tb) {
    const int left = tb - t4;
    if (left <= 0) { asm volatile("" :: "v"(B.ld[0][0]), "v"(B.ld[0][1]), "v"(B.ld[0][2]), "v"(B.ld[0][3]), "v"(B.ld[0][4]), "v"(B.ld[0][5]), "v"(B.ld[1][0]), "v"(B.ld[1][1]), "v"(B.ld[1][2]), "v"(B.ld[1][3]), "v"(B.ld[1][4]), "v"(B.ld[1][5]), "v"(B.ld[2][0]), "v"(B.ld[2][1]), "v"(B.ld[2][2]), "v"(B.ld[2][3]), "v"(B.ld[2][4]), "v"(B.ld[2][5]), "v"(B.ld[3][0]), "v"(B.ld[3][1]), "v"(B.ld[3][2]), "v"(B.ld[3][3]), "v"(B.ld[3][4]), "v"(B.ld[3][5])); return; }
    const int tg = t4 + (c.lane & 3);
    const unsigned ga = lds_rd32(L2_SLOT + (tg < tb ? tg : NITMAX) * 16 + c.hh * 8);
    unsigned sa[4];
#pragma unroll
    for (int j = 0; j < 4; ++j) sa[j] = lds_rd32(L2_SLOT + (j < left ? t4 + j : NITMAX) * 16 + c.hh * 8);
    const u32x2 g = lds_rd64((ga & 0x7fe0) + 16);
    f32x4 W4[3][2];
#pragma unroll
    for (int q = 0; q < 3; ++q) { W4[q][0] = filter4(g, wb[q][0]); W4[q][1] = filter4(g, wb[q][1]); }
#pragma unroll
    for (int j = 0; j < 4; ++j) {
        if (j > 0 && j >= left) { asm volatile("" :: "v"(B.ld[3][0]), "v"(B.ld[3][1]), "v"(B.ld[3][2]), "v"(B.ld[3][3]), "v"(B.ld[3][4]), "v"(B.ld[3][5]), "v"(B.ld[2][0]), "v"(B.ld[2][1]), "v"(B.ld[2][2]), "v"(B.ld[2][3]), "v"(B.ld[2][4]), "v"(B.ld[2][5]), "v"(B.ld[1][0]), "v"(B.ld[1][1]), "v"(B.ld[1][2]), "v"(B.ld[1][3]), "v"(B.ld[1][4]), "v"(B.ld[1][5])); break; }
        const float W[3][2] = {{W4[0][0][j], W4[0][1][j]}, {W4[1][0][j], W4[1][1][j]}, {W4[2][0][j], W4[2][1][j]}};
        edge_fma(acc, W, B.ld[j], lds_rd128(sa[j] & 0x7fe0));
        const unsigned sw = __builtin_amdgcn_readfirstlane(sa[j]);
        if (sw & 1) {
            f32x2 tot[2];
            half_reduce(acc, tot);
            store_rows(c, sw, tot, true);
#pragma unroll
            for (int a = 0; a < 4; ++a) acc[a] = (f32x2){0.f, 0.f};
        }
    }
}

__global__ __launch_bounds__(T2, 4) void k2_gather(const unsigned short* __restrict__ sv, const unsigned short* __restrict__ wr,
                                                   char* __restrict__ ws, char* __restrict__ out) {
    extern __shared__ __attribute__((aligned(16))) char smem[];
    K2Ctx c;
    c.smem = smem; c.ws = ws;
    c.tid = threadIdx.x; c.lane = c.tid & 63; c.wv = __builtin_amdgcn_readfirstlane(c.tid >> 6);
    c.h = blockIdx.x & 1;
    const int quad = (blockIdx.x >> 1) & 3, win = blockIdx.x >> 3, r = quad * NRQ + win;
    c.n0 = win * WN;
    c.hh = c.lane >> 5; c.c2 = c.lane & 31;
    c.lo = c.h * 128 + c.c2 * 4;
    c.rs_phi = __builtin_amdgcn_make_buffer_rsrc((void*)(ws + WS_PHI), 0, NN * 1024, 0x00020000);
    c.rs_sv = __builtin_amdgcn_make_buffer_rsrc((void*)sv, 0, NN * 1024, 0x00020000);
    c.rs_out = __builtin_amdgcn_make_buffer_rsrc((void*)out, 0, NN * 1024, 0x00020000);
    const int lane = c.lane, tid = c.tid, wv = c.wv, h = c.h, hh = c.hh, c2 = c.c2;
    STAMP_DECL;
    STAMP(1, 0);
    int* misc = (int*)(smem + L2_MISC);
    const unsigned* cellp = (const unsigned*)(ws + WS_CELL) + r * 256 + (lane >> 4) * 32 + (lane & 15);
    const unsigned short* wm = (const unsigned short*)(ws + WS_WMAP) + win * 128 + lane;
    unsigned ce0, ce1;
    if (wv < 2) { ce0 = cellp[0]; ce1 = cellp[128]; } else { ce0 = wm[0]; ce1 = wm[64]; }
    u32x2 wb[3][2];
    {
        unsigned wraw[3][4];
#pragma unroll
        for (int p = 0; p < 3; ++p)
#pragma unroll
            for (int k = 0; k < 4; ++k) wraw[p][k] = *(const unsigned*)(wr + k * 384 + p * 128 + h * 64 + 2 * c2);
        if (wv >= 2) {
            *(u32x2*)(smem + L2_CNT + (tid - 128) * 8) = (u32x2){0u, 0u};
            if (tid < 130) ((f32x4*)(smem + L2_U))[2 * CAP + tid - 128] = (f32x4){0.f, 0.f, 0.f, 0.f};
            static_assert(64 * NCH * 2 == 128 * 8, "");
        }
#pragma unroll
        for (int p = 0; p < 3; ++p)
#pragma unroll
            for (int k = 0; k < 2; ++k) { wb[p][0][k] = __builtin_amdgcn_perm(wraw[p][2 * k + 1], wraw[p][2 * k], 0x05040100u); wb[p][1][k] = __builtin_amdgcn_perm(wraw[p][2 * k + 1], wraw[p][2 * k], 0x07060302u); }
        asm volatile("" : "+v"(wb[0][0]), "+v"(wb[0][1]), "+v"(wb[1][0]), "+v"(wb[1][1]), "+v"(wb[2][0]), "+v"(wb[2][1]));
    }
    unsigned myce = 0, mycb = 0;
    if (wv < 2) {
        if (8 * (lane & 15) + (lane >> 4) >= NB_E) ce0 = 0;
        if (8 * (lane & 15) + (lane >> 4) + 4 >= NB_E) ce1 = 0;
        const unsigned cc0 = ce0 >> 16, cc1 = ce1 >> 16;
        const unsigned in0 = wave_incl_scan(cc0), t0 = __builtin_amdgcn_readlane(in0, 63);
        const unsigned in1 = wave_incl_scan(cc1) + t0;
        myce = wv == 0 ? ce0 : ce1;
        mycb = wv == 0 ? in0 - cc0 : in1 - cc1;
        if (tid == 63) misc[0] = in1;
    } else if (wv == 2) {
        const bool ma = (ce0 >> 8) == (unsigned)quad, mb = (lane + 64 < WN) & ((ce1 >> 8) == (unsigned)quad);
        lds_wr8(L2_MAP + lane, ce0); lds_wr8(L2_MAP + 64 + lane, ce1);
        if (ma) lds_wr8(L2_NOF + (ce0 & 127), lane);
        if (mb) lds_wr8(L2_NOF + (ce1 & 127), lane + 64);
        const int members = __popcll(ballot(ma)) + __popcll(ballot(mb));
        if (lane == 0) misc[6] = members;
    }
    const unsigned mycc = myce >> 16;
    const unsigned gi0 = (8 * (tid & 15) + (tid >> 4)) * EPB + (myce & 0xffff);
    u32x4 rf[6]; unsigned rm[6];
    if (wv < 2) {
        const __amdgpu_buffer_rsrc_t rs_rec = __builtin_amdgcn_make_buffer_rsrc((void*)(ws + WS_REC), 0, NB_E * EPB * 20, 0x00020000);
#pragma unroll
        for (int k = 0; k < 6; ++k) {
            const unsigned go = ((unsigned)k < mycc && mycb + k < CAP) ? (gi0 + k) * 20 : 0x80000000u;
            asm volatile("buffer_load_dwordx4 %0, %2, %3, 0 offen\n\tbuffer_load_dword %1, %2, %3, 0 offen offset:16" : "=&v"(rf[k]), "=&v"(rm[k]) : "v"(go), "s"(rs_rec) : "memory");
        }
    }
    raw_barrier();
    const int nn = __builtin_amdgcn_readfirstlane(misc[6]);
    if (wv >= 2) {
        for (int i = wv - 2; 2 * i < min(nn, NRES); i += 2) {
            const int node = c.n0 + lds_rd8(L2_NOF + min(2 * i + hh, nn - 1));
            dma16_asm((const char*)sv + (size_t)node * 1024 + ((lane >> 3) & 3) * 256 + h * 128 + (lane & 7) * 16, L2_RES + i * 1024);
        }
    } else {
        asm volatile("s_waitcnt vmcnt(0)" : "+v"(rf[0]), "+v"(rf[1]), "+v"(rf[2]), "+v"(rf[3]), "+v"(rf[4]), "+v"(rf[5]),
                     "+v"(rm[0]), "+v"(rm[1]), "+v"(rm[2]), "+v"(rm[3]), "+v"(rm[4]), "+v"(rm[5]) :: "memory");
#pragma unroll
        for (int k = 0; k < 6; ++k) if ((unsigned)k < mycc && mycb + k < CAP) put_record(smem, mycb + k, rf[k], rm[k]);
        for (unsigned q = 6; q < mycc && mycb + q < CAP; ++q) put_record_from(smem, ws, mycb + q, gi0 + q);
    }
    raw_barrier();
    STAMP(1, 2);
    const int total = __builtin_amdgcn_readfirstlane(misc[0]);
    int ta, tb;
    build_slots(c, min(total, CAP), 0, min(nn, NC));
    raw_barrier();
    read_span(c, ta, tb);
    STAMP(1, 3);
    f32x2 acc[4];
#pragma unroll
    for (int a = 0; a < 4; ++a) acc[a] = (f32x2){0.f, 0.f};

    if (total <= CAP && nn <= NC) {
        Batch A, B;
        batch_load(A, c, ta, tb);
        STAMP(1, 4);
        __builtin_amdgcn_s_setprio(3);
        const int q1 = ta + (tb - ta) / 4, q2 = ta + (tb - ta) / 2, q3 = tb - (tb - ta) / 4;
        const int npair = max(1, (tb - ta + 7) >> 3);
        int t4 = ta;
        batch_load(B, c, t4 + 4, tb);
        asm volatile("s_barrier" :: "v"(A.ld[0][0]) : "memory");
        for (int k = 1; k < npair; ++k) {
            if (t4 >= q3) __builtin_amdgcn_s_setprio(0); else if (t4 >= q2) __builtin_amdgcn_s_setprio(1); else if (t4 >= q1) __builtin_amdgcn_s_setprio(2);
            batch_compute(A, c, acc, wb, t4, tb);
            batch_load(A, c, t4 + 8, tb);
            batch_compute(B, c, acc, wb, t4 + 4, tb);
            batch_load(B, c, t4 + 12, tb);
            t4 += 8;
        }
        __builtin_amdgcn_s_setprio(0);
        batch_compute(A, c, acc, wb, t4, tb);
        batch_compute(B, c, acc, wb, t4 + 4, tb);
        STAMP(1, 5);
        STAMP_FLUSH(1);
    } else {
        float* accw = (float*)(ws + WS_ACC);
        asm volatile("s_waitcnt vmcnt(0)\n\ts_barrier" ::: "memory");
        int g0 = 0, w0 = 0;
        for (;;) {
            const bool firstw = (w0 == 0), lastw = (w0 + CAP >= total);
#pragma unroll 1
            for (int t = ta; t < tb; ++t) {
                const u32x2 sl = lds_rd64(L2_SLOT + t * 16 + hh * 8);
                const unsigned off = sl[1] | c.lo;
                unsigned ld[6];
#pragma unroll
                for (int a = 0; a < 3; ++a) { ld[a] = __builtin_amdgcn_raw_buffer_load_b32(c.rs_phi, off + a * 256, 0, 0); ld[3 + a] = __builtin_amdgcn_raw_buffer_load_b32(c.rs_sv, off + 256 + a * 256, 0, 0); }
                const u32x2 g = lds_rd64((sl[0] & 0x7fe0) + 16);
                float W[3][2];
#pragma unroll
                for (int q = 0; q < 3; ++q) { W[q][0] = filter4(g, wb[q][0])[0]; W[q][1] = filter4(g, wb[q][1])[0]; }
                edge_fma(acc, W, ld, lds_rd128(sl[0] & 0x7fe0));
                const unsigned sw = __builtin_amdgcn_readfirstlane(sl[0]);
                if (sw & 1) {
                    f32x2 tot[2];
                    half_reduce(acc, tot);
                    float* aw = accw + (((size_t)(c.n0 + ((sw >> 21) & 127)) * 2 + h) * 4 + 2 * hh) * 64 + 2 * c2;
                    if (!firstw) {
#pragma unroll
                        for (int q = 0; q < 2; ++q) { tot[q][0] += aw[q * 64]; tot[q][1] += aw[q * 64 + 1]; }
                    }
                    if (lastw) store_rows(c, sw, tot, g0 == 0);
                    else {
#pragma unroll
                        for (int q = 0; q < 2; ++q) { aw[q * 64] = tot[q][0]; aw[q * 64 + 1] = tot[q][1]; }
                    }
#pragma unroll
                    for (int a = 0; a < 4; ++a) acc[a] = (f32x2){0.f, 0.f};
                }
            }
            w0 += CAP;
            if (w0 >= total) { w0 = 0; g0 += NC; if (g0 >= nn) break; }
            __syncthreads();
            *(u32x2*)(smem + L2_CNT + (tid & 127) * 8) = (u32x2){0u, 0u};
            for (unsigned q = 0; q < mycc; ++q) {
                const int p = (int)(mycb + q) - w0;
                if (p >= 0 && p < CAP) put_record_from(smem, ws, p, gi0 + q);
            }
            __syncthreads();
            build_slots(c, min(CAP, total - w0), g0, min(nn - g0, NC));
            __syncthreads();
            read_span(c, ta, tb);
        }
    }
}

extern "C" void kernel_launch(void* const* d_in, const int* in_sizes, int n_in, void* d_out, int out_size, void* d_ws, size_t ws_size,
                              hipStream_t stream) {
    const unsigned short* sv = (const unsigned short*)d_in[0];
    const unsigned short* pos = (const unsigned short*)d_in[1];
    const int* src = (const int*)d_in[2];
    const int* dst = (const int*)d_in[3];
    const unsigned short* freqs = (const unsigned short*)d_in[4];
    const unsigned short* w1 = (const unsigned short*)d_in[5];
    const unsigned short* w2 = (const unsigned short*)d_in[6];
    const unsigned short* wr = (const unsigned short*)d_in[7];
    (void)hipFuncSetAttribute((const void*)k1_prepare, hipFuncAttributeMaxDynamicSharedMemorySize, K1_LDS);
    (void)hipFuncSetAttribute((const void*)k2_gather, hipFuncAttributeMaxDynamicSharedMemorySize, K2_LDS);
    k1_prepare<<<NB_E + NB_G, T1, K1_LDS, stream>>>(sv, pos, src, dst, freqs, w1, w2, (char*)d_ws);
    k2_gather<<<NR * 2, T2, K2_LDS, stream>>>(sv, wr, (char*)d_ws, (char*)d_out);
}
```

```cpp
#include <hip/hip_runtime.h>
#include <hip/hip_bf16.h>
#include <stdint.h>

typedef __attribute__((ext_vector_type(8))) short bf16x8;
typedef __attribute__((ext_vector_type(4))) short s16x4;
typedef __attribute__((ext_vector_type(4))) float f32x4;
typedef __attribute__((ext_vector_type(4))) unsigned u32x4;
typedef __attribute__((ext_vector_type(4), aligned(4))) unsigned u32x4a4;
typedef __attribute__((ext_vector_type(2))) unsigned u32x2;
typedef __attribute__((ext_vector_type(4))) int i32x4;
typedef __attribute__((address_space(3))) void* lds_ptr_t;
typedef const __attribute__((address_space(1))) void* glb_ptr_t;
typedef __attribute__((address_space(3))) s16x4* lds_s16x4_t;

constexpr int NN   = 12500;
constexpr int NE   = 400000;
constexpr int T1   = 1024;
constexpr int EPB  = 3200;
constexpr int NB_E = NE / EPB;
constexpr int GTL  = 6;
constexpr int GT   = 16 * GTL;
constexpr int NB_G = (NN + GT - 1) / GT;
constexpr int RN   = 25;
constexpr int NR   = (NN + RN - 1) / RN;
constexpr int NRQ  = NR / 4;
constexpr int OFS  = 512;
constexpr int T2   = 256;
constexpr int W2   = T2 / 64;
constexpr int CAP  = 512;
static_assert(NB_E <= 128 && NB_E * EPB == NE && T2 == 256 && NR % 4 == 0 && EPB % 4 == 0 && EPB <= 4 * T1, "");
static_assert(OFS >= NR + 1 && RN <= 62 && OFS <= T1, "");

constexpr size_t WS_PHI  = 0;
constexpr size_t WS_REC  = 12800000;
constexpr size_t WS_CELL = WS_REC + (size_t)NB_E * EPB * 20;
constexpr size_t WS_ACC  = WS_CELL + (size_t)NR * 1024;
constexpr size_t WS_END  = WS_ACC + (size_t)NN * 512 * 4;
static_assert(WS_CELL % 128 == 0 && WS_ACC % 128 == 0, "");
#define STAMP_DECL do {} while (0)
#define STAMP(kid, slot) do {} while (0)
#define STAMP_FLUSH(kid) do {} while (0)

__device__ __forceinline__ float bf2f(unsigned h) { return __uint_as_float(h << 16); }
__device__ __forceinline__ float bflo(unsigned u) { return __uint_as_float(u << 16); }
__device__ __forceinline__ float bfhi(unsigned u) { return __uint_as_float(u & 0xffff0000u); }
typedef __attribute__((ext_vector_type(2))) __bf16 bf16x2_t;
typedef __attribute__((ext_vector_type(2))) float f32x2_t;
__device__ __forceinline__ unsigned pack2(float lo, float hi) { f32x2_t f = {lo, hi}; bf16x2_t b = __builtin_convertvector(f, bf16x2_t); return __builtin_bit_cast(unsigned, b); }
__device__ __forceinline__ float rbf(float x) { return bflo(pack2(x, 0.f)); }

__device__ __forceinline__ unsigned wave_incl_scan(unsigned x) {
    x += __builtin_amdgcn_update_dpp(0u, x, 0x111, 0xf, 0xf, false);
    x += __builtin_amdgcn_update_dpp(0u, x, 0x112, 0xf, 0xf, false);
    x += __builtin_amdgcn_update_dpp(0u, x, 0x114, 0xf, 0xf, false);
    x += __builtin_amdgcn_update_dpp(0u, x, 0x118, 0xf, 0xf, false);
    x += __builtin_amdgcn_update_dpp(0u, x, 0x142, 0xa, 0xf, false);
    x += __builtin_amdgcn_update_dpp(0u, x, 0x143, 0xc, 0xf, false);
    return x;
}
__device__ __forceinline__ unsigned lanes_below(unsigned long long m) {
    return __builtin_amdgcn_mbcnt_hi((unsigned)(m >> 32), __builtin_amdgcn_mbcnt_lo((unsigned)m, 0u));
}
__device__ __forceinline__ unsigned long long ballot(bool p) { return __builtin_amdgcn_ballot_w64(p); }
__device__ __forceinline__ unsigned lds_add_rtn(unsigned lds_addr, unsigned v) {
    unsigned old;
    asm volatile("ds_add_rtn_u32 %0, %1, %2\n\ts_waitcnt lgkmcnt(0)" : "=v"(old) : "v"(lds_addr), "v"(v) : "memory");
    return old;
}
__device__ __forceinline__ void dma16(const void* g, void* l) { __builtin_amdgcn_global_load_lds((glb_ptr_t)g, (lds_ptr_t)l, 16, 0, 0); }
__device__ __forceinline__ void dma4(const void* g, void* l) { __builtin_amdgcn_global_load_lds((glb_ptr_t)g, (lds_ptr_t)l, 4, 0, 0); }
__device__ __forceinline__ void dma16_asm(const void* g, unsigned lds_addr) {
    unsigned keep;
    asm volatile("s_mov_b32 %0, m0\n\ts_mov_b32 m0, %2\n\ts_nop 0\n\tglobal_load_lds_dwordx4 %1, off\n\ts_mov_b32 m0, %0" : "=&s"(keep) : "v"(g), "s"(lds_addr) : "memory");
}
__device__ __forceinline__ void bdma16(__amdgpu_buffer_rsrc_t rs, unsigned voff, unsigned soff, unsigned lds_addr) {
    unsigned keep;
    asm volatile("s_mov_b32 %0, m0\n\ts_mov_b32 m0, %4\n\ts_nop 0\n\tbuffer_load_dwordx4 %1, %2, %3 offen lds\n\ts_mov_b32 m0, %0" : "=&s"(keep) : "v"(voff), "s"(rs), "s"(soff), "s"(lds_addr) : "memory");
}
__device__ __forceinline__ void raw_barrier() { asm volatile("s_waitcnt lgkmcnt(0)\n\ts_barrier" ::: "memory"); }
__device__ __forceinline__ void store16_wt(void* p, u32x4 v) { asm volatile("global_store_dwordx4 %0, %1, off sc1\n\ts_nop 1" :: "v"(p), "v"(v) : "memory"); }

__device__ __forceinline__ float rev_frac(float x) {
    const float C_HI = 0.15915494f, C_LO = (float)(0.15915494309189535 - (double)0.15915494f);
    const float r = x * C_HI;
    const float e = __builtin_fmaf(x, C_HI, -r) + x * C_LO;
    return __builtin_amdgcn_fractf(r) + e;
}
__device__ __forceinline__ float fsin(float x) { return __builtin_amdgcn_sinf(rev_frac(x)); }
__device__ __forceinline__ float fcos(float x) { return __builtin_amdgcn_cosf(rev_frac(x)); }

struct Pos3 { float x, y, z; };
typedef __attribute__((address_space(3))) const unsigned* lds_cu32_t;
typedef __attribute__((address_space(3))) u32x2* lds_u32x2w_t;
__device__ __forceinline__ u32x2 pos_words(unsigned j6) { lds_cu32_t p = (lds_cu32_t)(size_t)(j6 & ~3u); return (u32x2){p[0], p[1]}; }
__device__ __forceinline__ Pos3 unpack_pos(u32x2 d, unsigned j6) {
    const unsigned xy = __builtin_amdgcn_alignbyte(d[1], d[0], j6), zz = __builtin_amdgcn_alignbyte(d[1], d[1], j6);
    Pos3 p;
    p.x = bflo(xy); p.y = bfhi(xy); p.z = bflo(zz);
    return p;
}
__device__ __forceinline__ Pos3 lds_pos(unsigned j) { const unsigned j6 = __umul24(j, 6); return unpack_pos(pos_words(j6), j6); }

struct Rec { u32x4 f; bool act; };
__device__ __forceinline__ Rec edge_record(unsigned s, unsigned d, float f0, float f1, float f2, float f3) {
    const Pos3 ps = lds_pos(s), pd = lds_pos(d);
    const unsigned rxy = pack2(ps.x - pd.x, ps.y - pd.y);
    const float rx = bflo(rxy), ry = bfhi(rxy), rz = rbf(ps.z - pd.z);
    const unsigned sxy = pack2(rx * rx, ry * ry);
    const float ss = rbf(bflo(sxy) + bfhi(sxy) + rbf(rz * rz));
    const float dd = rbf(__builtin_amdgcn_sqrtf(rbf(ss + 1e-8f)));
    float fc = rbf(0.5f * rbf(rbf(fcos(rbf(rbf(3.14159265358979f * dd) * 0.2f))) + 1.0f));
    fc = (dd < 5.0f) ? fc : 0.0f * dd;
    const float inv = __builtin_amdgcn_rcpf(dd);
    const unsigned a01 = pack2(dd * f0, dd * f1), a23 = pack2(dd * f2, dd * f3);
    const unsigned s01 = pack2(fsin(bflo(a01)), fsin(bfhi(a01))), s23 = pack2(fsin(bflo(a23)), fsin(bfhi(a23)));
    const float b0 = bflo(s01) * inv, b1 = bfhi(s01) * inv, b2 = bflo(s23) * inv, b3 = bfhi(s23) * inv;
    Rec r;
    r.f = (u32x4){pack2(b0, b1), pack2(b2, b3), pack2(fc, rx * inv), pack2(ry * inv, rz * inv)};
    r.act = !(fc == 0.0f);
    return r;
}
__device__ __forceinline__ void store_record(char* ws, unsigned gi, u32x4 f, unsigned m) {
    char* p = ws + WS_REC + (size_t)gi * 20;
    *(u32x4a4*)p = f;
    *(unsigned*)(p + 16) = m;
}

constexpr int L1_HIST = 75008;
constexpr int L1_LIST = L1_HIST + OFS * 4 + 16;
constexpr int L1_CARR = L1_LIST + EPB * 8;
constexpr int L1_EDGE_END = L1_CARR + EPB * 2;

__device__ __forceinline__ void edge_block(int b, const unsigned short* __restrict__ pos, const int* __restrict__ src, const int* __restrict__ dst,
                                           const unsigned short* __restrict__ freqs, char* __restrict__ ws, char* smem) {
    unsigned* hist = (unsigned*)(smem + L1_HIST);
    unsigned* ncand = hist + OFS;
    u32x2* list = (u32x2*)(smem + L1_LIST);
    unsigned short* carr = (unsigned short*)(smem + L1_CARR);
    const int tid = threadIdx.x, lane = tid & 63, wv = __builtin_amdgcn_readfirstlane(tid >> 6);
    const int e0 = b * EPB;
    STAMP_DECL;
    STAMP(0, 0);
    const int q4 = min(tid, EPB / 4 - 1);
    const i32x4 sv4 = __builtin_nontemporal_load((const i32x4*)(src + e0) + q4);
    const i32x4 dv4 = __builtin_nontemporal_load((const i32x4*)(dst + e0) + q4);
    u32x2 fq;
    asm volatile("global_load_dwordx2 %0, %1, %2" : "=&v"(fq) : "v"(0), "s"(freqs) : "memory");
    {
        const __amdgpu_buffer_rsrc_t rs_pos = __builtin_amdgcn_make_buffer_rsrc((void*)pos, 0, NN * 6, 0x00020000);
#pragma unroll
        for (int i = 0; i < 5; ++i) { const int p = wv + 16 * i; if (p < 74) bdma16(rs_pos, p * 1024 + lane * 16, 0, p * 1024); }
    }
    if (tid < OFS) hist[tid] = 0;
    if (tid == 0) *ncand = 0;
    asm volatile("s_waitcnt vmcnt(0) lgkmcnt(0)\n\ts_barrier" : "+v"(fq) :: "memory");
    STAMP(0, 1);
    if (4 * tid < EPB) {
        u32x2 pw[4][2]; unsigned j6[4][2];
#pragma unroll
        for (int j = 0; j < 4; ++j) {
            j6[j][0] = __umul24(sv4[j], 6); j6[j][1] = __umul24(dv4[j], 6);
            pw[j][0] = pos_words(j6[j][0]); pw[j][1] = pos_words(j6[j][1]);
        }
        asm volatile("" :: "v"(pw[0][0]), "v"(pw[0][1]), "v"(pw[1][0]), "v"(pw[1][1]), "v"(pw[2][0]), "v"(pw[2][1]), "v"(pw[3][0]), "v"(pw[3][1]));
        bool act[4];
        unsigned below = 0, total = 0;
#pragma unroll
        for (int j = 0; j < 4; ++j) {
            const Pos3 ps = unpack_pos(pw[j][0], j6[j][0]), pd = unpack_pos(pw[j][1], j6[j][1]);
            const float rx = ps.x - pd.x, ry = ps.y - pd.y, rz = ps.z - pd.z;
            const float ss = rx * rx + ry * ry + rz * rz;
            act[j] = __float_as_uint(ss) - 0x41d00000u > 0x7f7fffffu - 0x41d00000u;
            const unsigned long long m = ballot(act[j]);
            below = __builtin_amdgcn_mbcnt_hi((unsigned)(m >> 32), __builtin_amdgcn_mbcnt_lo((unsigned)m, below));
            total += __popcll(m);
        }
        unsigned run = 0;
        if (lane == 0) run = lds_add_rtn(L1_HIST + OFS * 4, total);
        unsigned la = L1_LIST + (__builtin_amdgcn_readfirstlane(run) + below) * 8;
#pragma unroll
        for (int j = 0; j < 4; ++j)
            if (act[j]) { *(lds_u32x2w_t)(size_t)la = (u32x2){(unsigned)sv4[j] | ((unsigned)dv4[j] << 16), (unsigned)(4 * tid + j)}; la += 8; }
    }
    __syncthreads();
    STAMP(0, 2);
    const int n = *ncand;
    const float f0 = bflo(fq[0]), f1 = bfhi(fq[0]), f2 = bflo(fq[1]), f3 = bfhi(fq[1]);
    u32x2 en = {0u, 0u};
    Rec rc; rc.act = false;
    unsigned rg = 0, slot = 0;
    if ((tid & ~63) < n) {
        en = list[tid];
        const bool valid = tid < n;
        en.x = valid ? en.x : 0u;
        rc = edge_record(en.x & 0xffff, en.x >> 16, f0, f1, f2, f3);
        rc.act &= valid;
        rg = (en.x >> 16) / RN;
        if (rc.act) slot = atomicAdd(&hist[rg], 1u);
    }
    for (int i = tid + T1; i < n; i += T1) {
        const u32x2 e = list[i];
        const Rec r = edge_record(e.x & 0xffff, e.x >> 16, f0, f1, f2, f3);
        list[i].y = e.y | ((r.act ? atomicAdd(&hist[(e.x >> 16) / RN], 1u) : 0xffffu) << 16);
    }
    __syncthreads();
    STAMP(0, 3);
    if (wv == 0) {
        constexpr int PER = OFS / 64;
        const u32x4 ca = *(const u32x4*)(hist + lane * PER), cb = *(const u32x4*)(hist + lane * PER + 4);
        unsigned c[PER] = {ca[0], ca[1], ca[2], ca[3], cb[0], cb[1], cb[2], cb[3]}, sum = 0;
#pragma unroll
        for (int k = 0; k < PER; ++k) sum += c[k];
        unsigned run = wave_incl_scan(sum) - sum;
        unsigned o[PER];
#pragma unroll
        for (int k = 0; k < PER; ++k) { o[k] = run; run += c[k]; }
        *(u32x4*)(hist + lane * PER) = (u32x4){o[0], o[1], o[2], o[3]};
        *(u32x4*)(hist + lane * PER + 4) = (u32x4){o[4], o[5], o[6], o[7]};
    }
    __syncthreads();
    unsigned c0 = 0, c1 = 0;
    if ((tid & ~63) < n) {
        c0 = hist[rg]; c1 = hist[rg + 1];
        if (rc.act) carr[c0 + slot] = (unsigned short)en.y;
    }
    for (int i = tid + T1; i < n; i += T1) {
        const u32x2 e = list[i];
        if ((e.y >> 16) != 0xffff) carr[hist[(e.x >> 16) / RN] + (e.y >> 16)] = (unsigned short)(e.y & 0xffff);
    }
    if (tid >= T1 - OFS && tid < T1 - OFS + NR) {
        const int r = tid - (T1 - OFS);
        const unsigned a0 = hist[r], a1 = hist[r + 1];
        ((unsigned*)(ws + WS_CELL))[r * 256 + (b & 7) * 32 + (b >> 3)] = a0 | ((a1 - a0) << 16);
    }
    __syncthreads();
    STAMP(0, 4);
    if ((tid & ~63) < n) {
        unsigned rank = 0;
        unsigned cv[4];
#pragma unroll
        for (int q = 0; q < 4; ++q) cv[q] = carr[min(c0 + q, (unsigned)EPB - 1)];
#pragma unroll
        for (int q = 0; q < 4; ++q) rank += (c0 + q < c1) & (cv[q] < en.y);
        if (rc.act) {
            for (unsigned q = c0 + 4; q < c1; ++q) rank += (carr[q] < en.y);
            const unsigned d = en.x >> 16;
            store_record(ws, e0 + c0 + rank, rc.f, (en.x & 0xffff) | ((d - rg * RN) << 16));
        }
    }
    for (int i = tid + T1; i < n; i += T1) {
        const u32x2 e = list[i];
        if ((e.y >> 16) == 0xffff) continue;
        const unsigned s = e.x & 0xffff, d = e.x >> 16, el = e.y & 0xffff, r = d / RN;
        const unsigned a0 = hist[r], a1 = hist[r + 1];
        unsigned rank = 0;
        for (unsigned q = a0; q < a1; ++q) rank += (carr[q] < el);
        const Rec rr = edge_record(s, d, f0, f1, f2, f3);
        store_record(ws, e0 + a0 + rank, rr.f, s | ((d - r * RN) << 16));
    }
    STAMP(0, 5);
    STAMP_FLUSH(0);
}

constexpr int L1_W1 = 0;
constexpr int L1_W2 = 32768;
constexpr int L1_X  = L1_W2 + 98304;
constexpr int L1_H0 = L1_X + GTL * 4096;
constexpr int OPITCH = 784;
constexpr int L1_GEMM_END = L1_H0 + 8192;
constexpr int K1_LDS = L1_GEMM_END > L1_EDGE_END ? L1_GEMM_END : L1_EDGE_END;
static_assert(32 * OPITCH <= 32768 && GTL == 6 && K1_LDS <= 163840, "");
__host__ __device__ constexpr int l1_h(int s) { return s == 0 ? L1_H0 : L1_X + (s - 1) * 8192; }
__host__ __device__ constexpr int l1_o(int s) { return s == 0 ? L1_W1 : L1_W2 + (s - 1) * 32768; }

__device__ __forceinline__ s16x4 tr_read(unsigned a) { return __builtin_amdgcn_ds_read_tr16_b64_v4i16((lds_s16x4_t)(size_t)a); }
__device__ __forceinline__ bf16x8 cat8(s16x4 a, s16x4 b) { return __builtin_shufflevector(a, b, 0, 1, 2, 3, 4, 5, 6, 7); }
typedef __attribute__((address_space(3))) u32x4* lds_u32x4_t;
__device__ __forceinline__ u32x4 lds_ld16(unsigned a) { return *(lds_u32x4_t)(size_t)a; }

#define PIN8(x) asm volatile("" :: "v"(x[0][0]), "v"(x[0][1]), "v"(x[0][2]), "v"(x[0][3]), "v"(x[1][0]), "v"(x[1][1]), "v"(x[1][2]), "v"(x[1][3]))
__device__ __forceinline__ void gemm_block(int g, const unsigned short* __restrict__ sv, const unsigned short* __restrict__ w1,
                                           const unsigned short* __restrict__ w2, char* __restrict__ ws) {
    const int tid = threadIdx.x;
    const int lane = tid & 63, wv = __builtin_amdgcn_readfirstlane(tid >> 6), slot = lane & 15, q = lane >> 4;
    const int w = wv & 7;
    const int n0 = g * GT;
    STAMP_DECL;
    STAMP(0, 0);
    {
        const __amdgpu_buffer_rsrc_t rs_x = __builtin_amdgcn_make_buffer_rsrc((void*)sv, 0, NN * 1024, 0x00020000);
        const __amdgpu_buffer_rsrc_t rs_w1 = __builtin_amdgcn_make_buffer_rsrc((void*)w1, 0, 128 * 256, 0x00020000);
        const __amdgpu_buffer_rsrc_t rs_w2 = __builtin_amdgcn_make_buffer_rsrc((void*)w2, 0, 128 * 768, 0x00020000);
        const unsigned o1 = q * 256 + ((((slot >> 1) ^ (q | ((wv & 2) << 1))) << 1) | (slot & 1)) * 16;
        bdma16(rs_w1, o1, wv * 1024, L1_W1 + wv * 1024);
        bdma16(rs_w1, o1, (wv + 16) * 1024, L1_W1 + (wv + 16) * 1024);
        const unsigned xo = slot * 1024 + q * 16 + (n0 + 16 * (wv >> 2)) * 1024 + (wv & 3) * 64;
        bdma16(rs_x, xo, 0, L1_X + wv * 1024);
        if (wv < 8) bdma16(rs_x, xo + 64 * 1024, 0, L1_X + (16 + wv) * 1024);
        asm volatile("s_barrier" ::: "memory");
#pragma unroll
        for (int s3 = 0; s3 < 3; ++s3) {
            const int c = 64 * s3 + lane, row = c / 48, cp = c - 48 * row, P = cp >> 1;
            const unsigned o2 = row * 768 + ((((P & ~7) | ((P & 7) ^ ((4 * (wv & 1) + row) & 7))) << 1) | (cp & 1)) * 16;
            bdma16(rs_w2, o2, wv * 3072, L1_W2 + wv * 3072 + s3 * 1024);
            bdma16(rs_w2, o2, (wv + 16) * 3072, L1_W2 + (wv + 16) * 3072 + s3 * 1024);
        }
    }
    const int qq = slot >> 2, p4 = slot & 3;
    asm volatile("s_waitcnt vmcnt(6)\n\ts_barrier" ::: "memory");
    STAMP(0, 1);
    constexpr int NP = GTL / 2;
    const __amdgpu_buffer_rsrc_t rs_phi = __builtin_amdgcn_make_buffer_rsrc((void*)(ws + WS_PHI), 0, NN * 1024, 0x00020000);
    auto ship = [&](int s) {
#pragma unroll
        for (int k = 0; k < 3; ++k) {
            const int c = 192 * w + 64 * k + lane, r = c / 48, cp = c - 48 * r;
            __builtin_amdgcn_raw_buffer_store_b128(lds_ld16(l1_o(s) + r * OPITCH + cp * 16), rs_phi, (n0 + 32 * s + r) * 1024 + cp * 16, 0, 16);
        }
    };
    if (wv < 8) {
        bf16x8 a1[4];
        {
            const unsigned base = L1_W1 + (8 * q + qq) * 256 + (((w ^ (qq | ((q & 1) << 2))) << 1) | (p4 >> 1)) * 16 + 8 * (p4 & 1);
#pragma unroll
            for (int ks = 0; ks < 4; ++ks) a1[ks] = cat8(tr_read(base + ks * 8192), tr_read(base + ks * 8192 + 1024));
        }
        const unsigned hw = (w >> 1) * 1024 + lane * 16 + (w & 1) * 8;
#pragma unroll
        for (int s = 0; s < NP + 1; ++s) {
            if (s >= 2) ship(s - 2);
            if (s < NP) {
                u32x4 xb[2][4];
#pragma unroll
                for (int i = 0; i < 2; ++i)
#pragma unroll
                    for (int ks = 0; ks < 4; ++ks) xb[i][ks] = lds_ld16(L1_X + ((2 * s + i) * 4 + ks) * 1024 + lane * 16);
                PIN8(xb);
                f32x4 acc[2] = {{0, 0, 0, 0}, {0, 0, 0, 0}};
#pragma unroll
                for (int ks = 0; ks < 4; ++ks)
#pragma unroll
                    for (int i = 0; i < 2; ++i) acc[i] = __builtin_amdgcn_mfma_f32_16x16x32_bf16(a1[ks], __builtin_bit_cast(bf16x8, xb[i][ks]), acc[i], 0, 0, 0);
#pragma unroll
                for (int i = 0; i < 2; ++i) {
                    const unsigned x01 = pack2(acc[i][0], acc[i][1]), x23 = pack2(acc[i][2], acc[i][3]);
                    const float x[4] = {bflo(x01), bfhi(x01), bflo(x23), bfhi(x23)};
                    float v[4];
#pragma unroll
                    for (int r = 0; r < 4; ++r) v[r] = x[r] * __builtin_amdgcn_rcpf(1.0f + __builtin_amdgcn_exp2f(-1.44269504f * x[r]));
                    *(lds_u32x2w_t)(size_t)(l1_h(s) + i * 4096 + hw) = (u32x2){pack2(v[0], v[1]), pack2(v[2], v[3])};
                }
            }
            if (s == 0) asm volatile("s_waitcnt vmcnt(0)" ::: "memory");
            raw_barrier();
            if (s == 0) STAMP(0, 2);
        }
        ship(NP - 1);
    } else {
        asm volatile("s_waitcnt vmcnt(0)" ::: "memory");
        raw_barrier();
        STAMP(0, 2);
        bf16x8 a2[3][4];
#pragma unroll
        for (int t = 0; t < 3; ++t) {
            const int P = 3 * w + t, row0 = 4 * q + qq;
            const unsigned base = L1_W2 + row0 * 768 + ((P & ~7) | ((P & 7) ^ (row0 & 7))) * 32 + (p4 >> 1) * 16 + 8 * (p4 & 1);
#pragma unroll
            for (int ks = 0; ks < 4; ++ks) a2[t][ks] = cat8(tr_read(base + ks * 24576), tr_read(base + ks * 24576 + 12288));
        }
        const unsigned obase = slot * OPITCH + (48 * w + 4 * q) * 2;
#pragma unroll
        for (int s = 1; s < NP + 1; ++s) {
            u32x4 hb[2][4];
#pragma unroll
            for (int i = 0; i < 2; ++i)
#pragma unroll
                for (int ks = 0; ks < 4; ++ks) hb[i][ks] = lds_ld16(l1_h(s - 1) + i * 4096 + ks * 1024 + lane * 16);
            PIN8(hb);
            f32x4 acc[2][3];
#pragma unroll
            for (int i = 0; i < 2; ++i)
#pragma unroll
                for (int t = 0; t < 3; ++t) acc[i][t] = (f32x4){0, 0, 0, 0};
#pragma unroll
            for (int i = 0; i < 2; ++i)
#pragma unroll
                for (int ks = 0; ks < 4; ++ks)
#pragma unroll
                    for (int t = 0; t < 3; ++t) acc[i][t] = __builtin_amdgcn_mfma_f32_16x16x32_bf16(a2[t][ks], __builtin_bit_cast(bf16x8, hb[i][ks]), acc[i][t], 0, 0, 0);
#pragma unroll
            for (int i = 0; i < 2; ++i)
#pragma unroll
                for (int t = 0; t < 3; ++t)
                    *(lds_u32x2w_t)(size_t)(l1_o(s - 1) + obase + i * 16 * OPITCH + t * 32) = (u32x2){pack2(acc[i][t][0], acc[i][t][1]), pack2(acc[i][t][2], acc[i][t][3])};
            raw_barrier();
            if (s == 1) STAMP(0, 3);
            if (s == 2) STAMP(0, 4);
        }
    }
    STAMP(0, 5);
    STAMP_FLUSH(0);
}

__global__ __launch_bounds__(T1) void k1_prepare(const unsigned short* __restrict__ sv, const unsigned short* __restrict__ pos,
                                                 const int* __restrict__ src, const int* __restrict__ dst,
                                                 const unsigned short* __restrict__ freqs, const unsigned short* __restrict__ w1,
                                                 const unsigned short* __restrict__ w2, char* __restrict__ ws) {
    extern __shared__ __attribute__((aligned(16))) char smem[];
    const int b = blockIdx.x;
    if (b < NB_E) edge_block(b, pos, src, dst, freqs, ws, smem);
    else gemm_block(b - NB_E, sv, w1, w2, ws);
}

constexpr int NCH     = CAP / 64;
constexpr int NITMAX  = CAP / 2 + RN;
constexpr int L2_U    = 0;
constexpr int L2_RES  = L2_U + (CAP + 1) * 32;
constexpr int L2_CNT  = L2_RES + (RN + 1) * 512;
constexpr int L2_SLOT = L2_CNT + 32 * NCH * 2;
constexpr int L2_MISC = L2_SLOT + (NITMAX + 1) * 16;
constexpr int K2_LDS  = L2_MISC + 32;
static_assert(L2_U == 0 && L2_RES % 16 == 0 && L2_CNT % 16 == 0 && L2_SLOT % 16 == 0 && NCH == 8 && RN <= 31 && CAP * 32 <= 0x4000, "");
constexpr unsigned PAD_A = CAP * 32, PAD_B = 0x3fffu << 10;
typedef __attribute__((ext_vector_type(2))) float f32x2;
typedef __attribute__((address_space(3))) const u32x2* lds_u32x2_t;
typedef __attribute__((address_space(3))) const f32x4* lds_f32x4_t;
typedef __attribute__((address_space(3))) const unsigned* lds_u32_t;
__device__ __forceinline__ u32x2 lds_rd64(unsigned a) { return *(lds_u32x2_t)(size_t)a; }
__device__ __forceinline__ f32x4 lds_rd128(unsigned a) { return *(lds_f32x4_t)(size_t)a; }
__device__ __forceinline__ unsigned lds_rd32(unsigned a) { return *(lds_u32_t)(size_t)a; }

struct K2Ctx {
    char* smem; char* ws;
    __amdgpu_buffer_rsrc_t rs_phi, rs_sv, rs_out;
    int tid, lane, wv, h, n0, nn, hh, c2;
    unsigned lo;
};

__device__ __forceinline__ void put_record(char* smem, int p, u32x4 f, unsigned mm) {
    const float fc = bflo(f.z);
    *(f32x4*)(smem + L2_U + p * 32) = (f32x4){bfhi(f.z), bflo(f.w), bfhi(f.w), __uint_as_float(mm)};
    *(u32x2*)(smem + L2_U + p * 32 + 16) = (u32x2){pack2(bflo(f.x) * fc, bfhi(f.x) * fc), pack2(bflo(f.y) * fc, bfhi(f.y) * fc)};
}
__device__ __forceinline__ void put_record_from(char* smem, const char* ws, int p, unsigned gi) {
    const char* rp = ws + WS_REC + (size_t)gi * 20;
    put_record(smem, p, *(const u32x4a4*)rp, *(const unsigned*)(rp + 16));
}
__device__ __forceinline__ unsigned sum2(unsigned x, unsigned c) { return (x & 0xffff) + (x >> 16) + c; }

__device__ __forceinline__ void build_slots(const K2Ctx& c, int m) {
    unsigned short* cnt16 = (unsigned short*)(c.smem + L2_CNT);
    u32x2* slots = (u32x2*)(c.smem + L2_SLOT);
    unsigned mm[NCH / W2], rk[NCH / W2];
#pragma unroll
    for (int i = 0; i < NCH / W2; ++i) {
        const int p = 64 * (c.wv + W2 * i) + c.lane;
        if (64 * (c.wv + W2 * i) < m) {
            const bool valid = p < m;
            mm[i] = lds_rd32(L2_U + min(p, CAP) * 32 + 12);
            const unsigned nl = mm[i] >> 16;
            unsigned long long mk = ballot(valid);
#pragma unroll
            for (int bit = 0; bit < 5; ++bit) { const bool on = (nl >> bit) & 1; const unsigned long long bb = ballot(on); mk &= on ? bb : ~bb; }
            rk[i] = lanes_below(mk);
            if (valid && rk[i] == 0) cnt16[nl * NCH + c.wv + W2 * i] = __popcll(mk);
        }
    }
    raw_barrier();
    if (c.wv == 0 || 64 * c.wv < m) {
        const u32x4 cv = *(const u32x4*)(cnt16 + (c.lane & 31) * NCH);
        const unsigned cnt = sum2(cv[0], sum2(cv[1], sum2(cv[2], sum2(cv[3], 0))));
        const unsigned its = c.lane < c.nn ? max(1u, (cnt + 1) >> 1) : 0u;
        const unsigned inc = wave_incl_scan(its), st = inc - its;
#pragma unroll
        for (int i = 0; i < NCH / W2; ++i) {
            const int ch = c.wv + W2 * i;
            if (64 * ch < m) {
                unsigned below = 0;
#pragma unroll
                for (int d = 0; d < 4; ++d) {
                    const unsigned msk = ch >= 2 * d + 2 ? 0xffffffffu : (ch == 2 * d + 1 ? 0x0000ffffu : 0u);
                    below = sum2(cv[d] & msk, below);
                }
                const unsigned nl = mm[i] >> 16;
                const unsigned a = __shfl((2 * st + below) | (below << 12) | (its << 22), nl);
                const unsigned rank = ((a >> 12) & 1023) + rk[i];
                const unsigned e = ((rank >> 1) + 1 == (a >> 22)) ? nl + 1 : 0u;
                const int p = 64 * ch + c.lane;
                if (p < m) slots[(a & 4095) + rk[i]] = (u32x2){(unsigned)p * 32 | e, (mm[i] & 0xffff) << 10};
            }
        }
        if (c.wv == 0) {
            if (c.lane < c.nn) {
                const u32x2 pe = {PAD_A | (c.lane + 1), PAD_B};
                if ((cnt & 1) | (cnt == 0)) slots[2 * inc - 1] = pe;
                if (cnt == 0) slots[2 * st] = pe;
            } else if (c.lane >= 62) slots[2 * NITMAX + c.lane - 62] = (u32x2){PAD_A, PAD_B};
            const unsigned I = __builtin_amdgcn_readlane(inc, 63);
            unsigned bound = I;
#pragma unroll
            for (int w = 0; w < W2; ++w) {
                const unsigned long long mw = ballot((st >= w * I / W2) & (c.lane < c.nn));
                const unsigned bw = mw ? __builtin_amdgcn_readlane(st, __builtin_ctzll(mw)) : I;
                bound = c.lane == w ? bw : bound;
            }
            if (c.lane <= W2) ((unsigned*)(c.smem + L2_MISC))[1 + c.lane] = bound;
        }
    }
}
__device__ __forceinline__ void read_span(const K2Ctx& c, int& ta, int& tb) {
    const u32x2 x = {lds_rd32(L2_MISC + 4 + 4 * c.wv), lds_rd32(L2_MISC + 8 + 4 * c.wv)};
    ta = __builtin_amdgcn_readfirstlane(x[0]); tb = __builtin_amdgcn_readfirstlane(x[1]);
}

__device__ __forceinline__ float vmul(float a, float b) { float d = a * b; asm("" : "+v"(d)); return d; }
__device__ __forceinline__ float vfma(float a, float b, float c) { float d = __builtin_fmaf(a, b, c); asm("" : "+v"(d)); return d; }
__device__ __forceinline__ f32x2 unpack2(unsigned u) { return (f32x2){bflo(u), bfhi(u)}; }
__device__ __forceinline__ void edge_fma(f32x2 (&acc)[4], const float (&W)[3][2], const unsigned (&ld)[6], f32x4 hat) {
    const f32x2 mv = {vmul(bflo(ld[1]), W[1][0]), vmul(bfhi(ld[1]), W[1][1])};
    const f32x2 mr = {vmul(bflo(ld[2]), W[2][0]), vmul(bfhi(ld[2]), W[2][1])};
    acc[0] = (f32x2){vfma(bflo(ld[0]), W[0][0], acc[0][0]), vfma(bfhi(ld[0]), W[0][1], acc[0][1])};
#pragma unroll
    for (int a = 0; a < 3; ++a) {
        acc[1 + a] = __builtin_elementwise_fma(mv, unpack2(ld[3 + a]), acc[1 + a]);
        acc[1 + a] = __builtin_elementwise_fma(mr, (f32x2){hat[a], hat[a]}, acc[1 + a]);
    }
}
__device__ __forceinline__ f32x4 filter4(u32x2 g, u32x2 wb) {
    return __builtin_amdgcn_mfma_f32_4x4x4bf16_1k(__builtin_bit_cast(s16x4, g), __builtin_bit_cast(s16x4, wb), (f32x4){0.f, 0.f, 0.f, 0.f}, 0, 0, 0);
}
__device__ __forceinline__ void half_reduce(const f32x2 (&acc)[4], f32x2 (&tot)[2]) {
#pragma unroll
    for (int q = 0; q < 2; ++q) {
        const auto s0 = __builtin_amdgcn_permlane32_swap(__float_as_uint(acc[q][0]), __float_as_uint(acc[q + 2][0]), false, false);
        const auto s1 = __builtin_amdgcn_permlane32_swap(__float_as_uint(acc[q][1]), __float_as_uint(acc[q + 2][1]), false, false);
        tot[q] = (f32x2){__uint_as_float(s0[0]), __uint_as_float(s1[0])} + (f32x2){__uint_as_float(s0[1]), __uint_as_float(s1[1])};
    }
}
__device__ __forceinline__ void store_rows(const K2Ctx& c, int nl, const f32x2 (&tot)[2]) {
    const unsigned ra = L2_RES + nl * 512 + c.hh * 256 + c.c2 * 4;
#pragma unroll
    for (int q = 0; q < 2; ++q) {
        const f32x2 o = unpack2(lds_rd32(ra + q * 128)) + tot[q];
        __builtin_amdgcn_raw_buffer_store_b32(pack2(o[0], o[1]), c.rs_out, c.hh * 512 + c.lo + q * 256, (c.n0 + nl) * 1024,   2);
    }
}

struct Batch { unsigned ld[4][6]; unsigned sa[4]; unsigned ga; };
__device__ __forceinline__ void batch_load(Batch& B, const K2Ctx& c, int t4, int tb) {
    unsigned off[4];
#pragma unroll
    for (int j = 0; j < 4; ++j) {
        const int t = (t4 + j < tb) ? t4 + j : NITMAX;
        const u32x2 x = lds_rd64(L2_SLOT + t * 16 + c.hh * 8);
        B.sa[j] = x[0]; off[j] = x[1] | c.lo;
    }
    { const int t = t4 + (c.lane & 3); B.ga = lds_rd32(L2_SLOT + (t < tb ? t : NITMAX) * 16 + c.hh * 8); }
#pragma unroll
    for (int j = 0; j < 4; ++j) {
        B.ld[j][0] = __builtin_amdgcn_raw_buffer_load_b32(c.rs_phi, off[j], 0, 0);
        B.ld[j][1] = __builtin_amdgcn_raw_buffer_load_b32(c.rs_phi, off[j] + 256, 0, 0);
        B.ld[j][2] = __builtin_amdgcn_raw_buffer_load_b32(c.rs_phi, off[j] + 512, 0, 0);
        B.ld[j][3] = __builtin_amdgcn_raw_buffer_load_b32(c.rs_sv, off[j] + 256, 0, 0);
        B.ld[j][4] = __builtin_amdgcn_raw_buffer_load_b32(c.rs_sv, off[j] + 512, 0, 0);
        B.ld[j][5] = __builtin_amdgcn_raw_buffer_load_b32(c.rs_sv, off[j] + 768, 0, 0);
    }
}
__device__ __forceinline__ void batch_compute(const Batch& B, const K2Ctx& c, f32x2 (&acc)[4], const u32x2 (&wb)[3][2], bool first) {
    const u32x2 g = lds_rd64((B.ga & 0x7fe0) + 16);
    f32x4 hat[4];
#pragma unroll
    for (int j = 0; j < 4; ++j) hat[j] = lds_rd128(B.sa[j] & 0x7fe0);
    f32x4 W4[3][2];
#pragma unroll
    for (int q = 0; q < 3; ++q) { W4[q][0] = filter4(g, wb[q][0]); W4[q][1] = filter4(g, wb[q][1]); }
#pragma unroll
    for (int j = 0; j < 4; ++j) {
        const float W[3][2] = {{W4[0][0][j], W4[0][1][j]}, {W4[1][0][j], W4[1][1][j]}, {W4[2][0][j], W4[2][1][j]}};
        edge_fma(acc, W, B.ld[j], hat[j]);
        if (j == 0 && first) asm volatile("s_barrier" : "+v"(acc[0]), "+v"(acc[1]), "+v"(acc[2]), "+v"(acc[3]) :: "memory");
        const int en = __builtin_amdgcn_readfirstlane(B.sa[j]) & 31;
        if (en) {
            f32x2 tot[2];
            half_reduce(acc, tot);
            store_rows(c, en - 1, tot);
#pragma unroll
            for (int a = 0; a < 4; ++a) acc[a] = (f32x2){0.f, 0.f};
        }
    }
}

__global__ __launch_bounds__(T2, 4) void k2_gather(const unsigned short* __restrict__ sv, const unsigned short* __restrict__ wr,
                                                   char* __restrict__ ws, char* __restrict__ out) {
    extern __shared__ __attribute__((aligned(16))) char smem[];
    K2Ctx c;
    c.smem = smem; c.ws = ws;
    c.tid = threadIdx.x; c.lane = c.tid & 63; c.wv = __builtin_amdgcn_readfirstlane(c.tid >> 6);
    c.h = blockIdx.x & 1; const int r = ((blockIdx.x >> 1) & 3) * NRQ + (blockIdx.x >> 3);
    c.n0 = r * RN; c.nn = min(RN, NN - c.n0);
    c.hh = c.lane >> 5; c.c2 = c.lane & 31;
    c.lo = c.h * 128 + c.c2 * 4;
    c.rs_phi = __builtin_amdgcn_make_buffer_rsrc((void*)(ws + WS_PHI), 0, NN * 1024, 0x00020000);
    c.rs_sv = __builtin_amdgcn_make_buffer_rsrc((void*)sv, 0, NN * 1024, 0x00020000);
    c.rs_out = __builtin_amdgcn_make_buffer_rsrc((void*)out, 0, NN * 1024, 0x00020000);
    const int lane = c.lane, tid = c.tid, wv = c.wv, h = c.h, hh = c.hh, c2 = c.c2;
    STAMP_DECL;
    STAMP(1, 0);
    int* tot_lds = (int*)(smem + L2_MISC);
    unsigned myce = 0, mycb = 0;
    const unsigned* cellp = (const unsigned*)(ws + WS_CELL) + r * 256 + (lane >> 4) * 32 + (lane & 15);
    unsigned ce0 = cellp[0], ce1 = cellp[128];
    u32x2 wb[3][2];
    {
        unsigned wraw[3][4];
#pragma unroll
        for (int p = 0; p < 3; ++p)
#pragma unroll
            for (int k = 0; k < 4; ++k) wraw[p][k] = *(const unsigned*)(wr + k * 384 + p * 128 + h * 64 + 2 * c2);
        if (wv >= 2) {
            ((unsigned*)(smem + L2_CNT))[tid - 128] = 0;
            if (tid < 130) ((f32x4*)(smem + L2_U))[2 * CAP + tid - 128] = (f32x4){0.f, 0.f, 0.f, 0.f};
            static_assert(32 * NCH * 2 == 128 * 4, "");
        }
#pragma unroll
        for (int p = 0; p < 3; ++p)
#pragma unroll
            for (int k = 0; k < 2; ++k) { wb[p][0][k] = __builtin_amdgcn_perm(wraw[p][2 * k + 1], wraw[p][2 * k], 0x05040100u); wb[p][1][k] = __builtin_amdgcn_perm(wraw[p][2 * k + 1], wraw[p][2 * k], 0x07060302u); }
        asm volatile("" : "+v"(wb[0][0]), "+v"(wb[0][1]), "+v"(wb[1][0]), "+v"(wb[1][1]), "+v"(wb[2][0]), "+v"(wb[2][1]));
    }
    if (wv < 2) {
        if (8 * (lane & 15) + (lane >> 4) >= NB_E) ce0 = 0;
        if (8 * (lane & 15) + (lane >> 4) + 4 >= NB_E) ce1 = 0;
        const unsigned cc0 = ce0 >> 16, cc1 = ce1 >> 16;
        const unsigned in0 = wave_incl_scan(cc0), t0 = __builtin_amdgcn_readlane(in0, 63);
        const unsigned in1 = wave_incl_scan(cc1) + t0;
        myce = wv == 0 ? ce0 : ce1;
        mycb = wv == 0 ? in0 - cc0 : in1 - cc1;
        if (tid == 63) *tot_lds = in1;
    }
    const unsigned mycc = myce >> 16;
    const unsigned gi0 = (8 * (tid & 15) + (tid >> 4)) * EPB + (myce & 0xffff);
    u32x4 rf[6]; unsigned rm[6];
    if (wv < 2) {
        const __amdgpu_buffer_rsrc_t rs_rec = __builtin_amdgcn_make_buffer_rsrc((void*)(ws + WS_REC), 0, NB_E * EPB * 20, 0x00020000);
#pragma unroll
        for (int k = 0; k < 6; ++k) {
            const unsigned go = ((unsigned)k < mycc && mycb + k < CAP) ? (gi0 + k) * 20 : 0x80000000u;
            asm volatile("buffer_load_dwordx4 %0, %2, %3, 0 offen\n\tbuffer_load_dword %1, %2, %3, 0 offen offset:16" : "=&v"(rf[k]), "=&v"(rm[k]) : "v"(go), "s"(rs_rec) : "memory");
        }
    }
    asm volatile("s_barrier" ::: "memory");
    if (wv >= 2) {
#pragma unroll
        for (int i4 = 0; i4 < (RN + 3) / 4; ++i4) {
            const int i = min(wv - 2 + 2 * i4, (RN + 1) / 2 - 1);
            const int node = min(c.n0 + 2 * i + hh, NN - 1);
            dma16_asm((const char*)sv + (size_t)node * 1024 + ((lane >> 3) & 3) * 256 + h * 128 + (lane & 7) * 16, L2_RES + i * 1024);
        }
    } else {
        asm volatile("s_waitcnt vmcnt(0)" : "+v"(rf[0]), "+v"(rf[1]), "+v"(rf[2]), "+v"(rf[3]), "+v"(rf[4]), "+v"(rf[5]),
                     "+v"(rm[0]), "+v"(rm[1]), "+v"(rm[2]), "+v"(rm[3]), "+v"(rm[4]), "+v"(rm[5]) :: "memory");
#pragma unroll
        for (int k = 0; k < 6; ++k) if ((unsigned)k < mycc && mycb + k < CAP) put_record(smem, mycb + k, rf[k], rm[k]);
        for (unsigned q = 6; q < mycc && mycb + q < CAP; ++q) put_record_from(smem, ws, mycb + q, gi0 + q);
    }
    __builtin_amdgcn_s_waitcnt(0xc07f);
    asm volatile("s_barrier" ::: "memory");
    STAMP(1, 2);
    const int total = __builtin_amdgcn_readfirstlane(*tot_lds);
    int ta, tb;
    build_slots(c, min(total, CAP));
    raw_barrier();
    read_span(c, ta, tb);
    STAMP(1, 3);
    f32x2 acc[4];
#pragma unroll
    for (int a = 0; a < 4; ++a) acc[a] = (f32x2){0.f, 0.f};

    if (total <= CAP) {
        Batch A;
        STAMP(1, 4);
        __builtin_amdgcn_s_setprio(3);
        const int q1 = ta + (tb - ta) / 4, q2 = ta + (tb - ta) / 2, q3 = tb - (tb - ta) / 4;
        for (int t4 = ta; t4 == ta || t4 < tb; t4 += 4) {
            if (t4 >= q3) __builtin_amdgcn_s_setprio(0); else if (t4 >= q2) __builtin_amdgcn_s_setprio(1); else if (t4 >= q1) __builtin_amdgcn_s_setprio(2);
            batch_load(A, c, t4, tb);
            batch_compute(A, c, acc, wb, t4 == ta);
        }
        STAMP(1, 5);
        STAMP_FLUSH(1);
    } else {
        float* accw = (float*)(ws + WS_ACC);
        asm volatile("s_waitcnt vmcnt(0)\n\ts_barrier" ::: "memory");
        for (int w0 = 0;;) {
            const bool firstw = (w0 == 0), lastw = (w0 + CAP >= total);
#pragma unroll 1
            for (int t = ta; t < tb; ++t) {
                const u32x2 sl = lds_rd64(L2_SLOT + t * 16 + hh * 8);
                const unsigned off = sl[1] | c.lo;
                unsigned ld[6];
#pragma unroll
                for (int a = 0; a < 3; ++a) { ld[a] = __builtin_amdgcn_raw_buffer_load_b32(c.rs_phi, off + a * 256, 0, 0); ld[3 + a] = __builtin_amdgcn_raw_buffer_load_b32(c.rs_sv, off + 256 + a * 256, 0, 0); }
                const u32x2 g = lds_rd64((sl[0] & 0x7fe0) + 16);
                float W[3][2];
#pragma unroll
                for (int q = 0; q < 3; ++q) { W[q][0] = filter4(g, wb[q][0])[0]; W[q][1] = filter4(g, wb[q][1])[0]; }
                edge_fma(acc, W, ld, lds_rd128(sl[0] & 0x7fe0));
                const int en = __builtin_amdgcn_readfirstlane(sl[0]) & 31;
                if (en) {
                    f32x2 tot[2];
                    half_reduce(acc, tot);
                    float* aw = accw + (((size_t)(c.n0 + en - 1) * 2 + h) * 4 + 2 * hh) * 64 + 2 * c2;
                    if (!firstw) {
#pragma unroll
                        for (int q = 0; q < 2; ++q) { tot[q][0] += aw[q * 64]; tot[q][1] += aw[q * 64 + 1]; }
                    }
                    if (lastw) store_rows(c, en - 1, tot);
                    else {
#pragma unroll
                        for (int q = 0; q < 2; ++q) { aw[q * 64] = tot[q][0]; aw[q * 64 + 1] = tot[q][1]; }
                    }
#pragma unroll
                    for (int a = 0; a < 4; ++a) acc[a] = (f32x2){0.f, 0.f};
                }
            }
            w0 += CAP;
            if (w0 >= total) break;
            __syncthreads();
            if (tid < 128) ((unsigned*)(smem + L2_CNT))[tid] = 0;
            for (unsigned q = 0; q < mycc; ++q) {
                const int p = (int)(mycb + q) - w0;
                if (p >= 0 && p < CAP) put_record_from(smem, ws, p, gi0 + q);
            }
            __syncthreads();
            build_slots(c, min(CAP, total - w0));
            __syncthreads();
            read_span(c, ta, tb);
        }
    }
}

extern "C" void kernel_launch(void* const* d_in, const int* in_sizes, int n_in, void* d_out, int out_size, void* d_ws, size_t ws_size,
                              hipStream_t stream) {
    const unsigned short* sv = (const unsigned short*)d_in[0];
    const unsigned short* pos = (const unsigned short*)d_in[1];
    const int* src = (const int*)d_in[2];
    const int* dst = (const int*)d_in[3];
    const unsigned short* freqs = (const unsigned short*)d_in[4];
    const unsigned short* w1 = (const unsigned short*)d_in[5];
    const unsigned short* w2 = (const unsigned short*)d_in[6];
    const unsigned short* wr = (const unsigned short*)d_in[7];
    (void)hipFuncSetAttribute((const void*)k1_prepare, hipFuncAttributeMaxDynamicSharedMemorySize, K1_LDS);
    (void)hipFuncSetAttribute((const void*)k2_gather, hipFuncAttributeMaxDynamicSharedMemorySize, K2_LDS);
    k1_prepare<<<NB_E + NB_G, T1, K1_LDS, stream>>>(sv, pos, src, dst, freqs, w1, w2, (char*)d_ws);
    k2_gather<<<NR * 2, T2, K2_LDS, stream>>>(sv, wr, (char*)d_ws, (char*)d_out);
}
```

```cpp
#include <hip/hip_runtime.h>
#include <hip/hip_bf16.h>
#include <stdint.h>

typedef __attribute__((ext_vector_type(8))) short bf16x8;
typedef __attribute__((ext_vector_type(4))) short s16x4;
typedef __attribute__((ext_vector_type(4))) float f32x4;
typedef __attribute__((ext_vector_type(4))) unsigned u32x4;
typedef __attribute__((ext_vector_type(4), aligned(4))) unsigned u32x4a4;
typedef __attribute__((ext_vector_type(2))) unsigned u32x2;
typedef __attribute__((ext_vector_type(4))) int i32x4;
typedef __attribute__((address_space(3))) s16x4* lds_s16x4_t;

constexpr int NN   = 12500;
constexpr int NE   = 400000;
constexpr int T1   = 1024;
constexpr int EPB  = 3200;
constexpr int NB_E = NE / EPB;
constexpr int GTL  = 6;
constexpr int GT   = 16 * GTL;
constexpr int NB_G = (NN + GT - 1) / GT;
constexpr int RN   = 25;
constexpr int NR   = (NN + RN - 1) / RN;
constexpr int NRQ  = NR / 4;
constexpr int OFS  = 512;
constexpr int T2   = 256;
constexpr int W2   = T2 / 64;
constexpr int CAP  = 512;
static_assert(NB_E <= 128 && NB_E * EPB == NE && T2 == 256 && NR % 4 == 0 && EPB % 4 == 0 && EPB <= 4 * T1, "");
static_assert(OFS >= NR + 1 && RN <= 62 && OFS <= T1, "");

constexpr size_t WS_PHI  = 0;
constexpr size_t WS_REC  = 12800000;
constexpr size_t WS_CELL = WS_REC + (size_t)NB_E * EPB * 20;
constexpr size_t WS_ACC  = WS_CELL + (size_t)NR * 1024;
constexpr size_t WS_END  = WS_ACC + (size_t)NN * 512 * 4;
static_assert(WS_CELL % 128 == 0 && WS_ACC % 128 == 0, "");

__device__ __forceinline__ float bflo(unsigned u) { return __uint_as_float(u << 16); }
__device__ __forceinline__ float bfhi(unsigned u) { return __uint_as_float(u & 0xffff0000u); }
typedef __attribute__((ext_vector_type(2))) __bf16 bf16x2_t;
typedef __attribute__((ext_vector_type(2))) float f32x2_t;
__device__ __forceinline__ unsigned pack2(float lo, float hi) { f32x2_t f = {lo, hi}; bf16x2_t b = __builtin_convertvector(f, bf16x2_t); return __builtin_bit_cast(unsigned, b); }
__device__ __forceinline__ float rbf(float x) { return bflo(pack2(x, 0.f)); }

__device__ __forceinline__ unsigned wave_incl_scan(unsigned x) {
    x += __builtin_amdgcn_update_dpp(0u, x, 0x111, 0xf, 0xf, false);
    x += __builtin_amdgcn_update_dpp(0u, x, 0x112, 0xf, 0xf, false);
    x += __builtin_amdgcn_update_dpp(0u, x, 0x114, 0xf, 0xf, false);
    x += __builtin_amdgcn_update_dpp(0u, x, 0x118, 0xf, 0xf, false);
    x += __builtin_amdgcn_update_dpp(0u, x, 0x142, 0xa, 0xf, false);
    x += __builtin_amdgcn_update_dpp(0u, x, 0x143, 0xc, 0xf, false);
    return x;
}
__device__ __forceinline__ unsigned lanes_below(unsigned long long m) {
    return __builtin_amdgcn_mbcnt_hi((unsigned)(m >> 32), __builtin_amdgcn_mbcnt_lo((unsigned)m, 0u));
}
__device__ __forceinline__ unsigned long long ballot(bool p) { return __builtin_amdgcn_ballot_w64(p); }
__device__ __forceinline__ unsigned lds_add_rtn(unsigned lds_addr, unsigned v) {
    unsigned old;
    asm volatile("ds_add_rtn_u32 %0, %1, %2\n\ts_waitcnt lgkmcnt(0)" : "=v"(old) : "v"(lds_addr), "v"(v) : "memory");
    return old;
}
__device__ __forceinline__ void dma16_asm(const void* g, unsigned lds_addr) {
    unsigned keep;
    asm volatile("s_mov_b32 %0, m0\n\ts_mov_b32 m0, %2\n\ts_nop 0\n\tglobal_load_lds_dwordx4 %1, off\n\ts_mov_b32 m0, %0" : "=&s"(keep) : "v"(g), "s"(lds_addr) : "memory");
}
__device__ __forceinline__ void bdma16(__amdgpu_buffer_rsrc_t rs, unsigned voff, unsigned soff, unsigned lds_addr) {
    unsigned keep;
    asm volatile("s_mov_b32 %0, m0\n\ts_mov_b32 m0, %4\n\ts_nop 0\n\tbuffer_load_dwordx4 %1, %2, %3 offen lds\n\ts_mov_b32 m0, %0" : "=&s"(keep) : "v"(voff), "s"(rs), "s"(soff), "s"(lds_addr) : "memory");
}
__device__ __forceinline__ void raw_barrier() { asm volatile("s_waitcnt lgkmcnt(0)\n\ts_barrier" ::: "memory"); }

__device__ __forceinline__ float rev_frac(float x) {
    const float C_HI = 0.15915494f, C_LO = (float)(0.15915494309189535 - (double)0.15915494f);
    const float r = x * C_HI;
    const float e = __builtin_fmaf(x, C_HI, -r) + x * C_LO;
    return __builtin_amdgcn_fractf(r) + e;
}
__device__ __forceinline__ float fsin(float x) { return __builtin_amdgcn_sinf(rev_frac(x)); }
__device__ __forceinline__ float fcos(float x) { return __builtin_amdgcn_cosf(rev_frac(x)); }

struct Pos3 { float x, y, z; };
typedef __attribute__((address_space(3))) const unsigned* lds_cu32_t;
typedef __attribute__((address_space(3))) u32x2* lds_u32x2w_t;
__device__ __forceinline__ u32x2 pos_words(unsigned j6) { lds_cu32_t p = (lds_cu32_t)(size_t)(j6 & ~3u); return (u32x2){p[0], p[1]}; }
__device__ __forceinline__ Pos3 unpack_pos(u32x2 d, unsigned j6) {
    const unsigned xy = __builtin_amdgcn_alignbyte(d[1], d[0], j6), zz = __builtin_amdgcn_alignbyte(d[1], d[1], j6);
    Pos3 p;
    p.x = bflo(xy); p.y = bfhi(xy); p.z = bflo(zz);
    return p;
}
__device__ __forceinline__ Pos3 lds_pos(unsigned j) { const unsigned j6 = __umul24(j, 6); return unpack_pos(pos_words(j6), j6); }

struct Rec { u32x4 f; bool act; };
__device__ __forceinline__ Rec edge_record(unsigned s, unsigned d, float f0, float f1, float f2, float f3) {
    const Pos3 ps = lds_pos(s), pd = lds_pos(d);
    const unsigned rxy = pack2(ps.x - pd.x, ps.y - pd.y);
    const float rx = bflo(rxy), ry = bfhi(rxy), rz = rbf(ps.z - pd.z);
    const unsigned sxy = pack2(rx * rx, ry * ry);
    const float ss = rbf(bflo(sxy) + bfhi(sxy) + rbf(rz * rz));
    const float dd = rbf(__builtin_amdgcn_sqrtf(rbf(ss + 1e-8f)));
    float fc = rbf(0.5f * rbf(rbf(fcos(rbf(rbf(3.14159265358979f * dd) * 0.2f))) + 1.0f));
    fc = (dd < 5.0f) ? fc : 0.0f * dd;
    const float inv = __builtin_amdgcn_rcpf(dd);
    const unsigned a01 = pack2(dd * f0, dd * f1), a23 = pack2(dd * f2, dd * f3);
    const unsigned s01 = pack2(fsin(bflo(a01)), fsin(bfhi(a01))), s23 = pack2(fsin(bflo(a23)), fsin(bfhi(a23)));
    const float b0 = bflo(s01) * inv, b1 = bfhi(s01) * inv, b2 = bflo(s23) * inv, b3 = bfhi(s23) * inv;
    Rec r;
    r.f = (u32x4){pack2(b0, b1), pack2(b2, b3), pack2(fc, rx * inv), pack2(ry * inv, rz * inv)};
    r.act = !(fc == 0.0f);
    return r;
}
__device__ __forceinline__ void store_record(char* ws, unsigned gi, u32x4 f, unsigned m) {
    char* p = ws + WS_REC + (size_t)gi * 20;
    *(u32x4a4*)p = f;
    *(unsigned*)(p + 16) = m;
}

constexpr int L1_HIST = 75008;
constexpr int L1_LIST = L1_HIST + OFS * 4 + 16;
constexpr int L1_CARR = L1_LIST + EPB * 8;
constexpr int L1_EDGE_END = L1_CARR + EPB * 2;

__device__ __forceinline__ void edge_block(int b, const unsigned short* __restrict__ pos, const int* __restrict__ src, const int* __restrict__ dst,
                                           const unsigned short* __restrict__ freqs, char* __restrict__ ws, char* smem) {
    unsigned* hist = (unsigned*)(smem + L1_HIST);
    unsigned* ncand = hist + OFS;
    u32x2* list = (u32x2*)(smem + L1_LIST);
    unsigned short* carr = (unsigned short*)(smem + L1_CARR);
    const int tid = threadIdx.x, lane = tid & 63, wv = __builtin_amdgcn_readfirstlane(tid >> 6);
    const int e0 = b * EPB;
    const int q4 = min(tid, EPB / 4 - 1);
    const i32x4 sv4 = __builtin_nontemporal_load((const i32x4*)(src + e0) + q4);
    const i32x4 dv4 = __builtin_nontemporal_load((const i32x4*)(dst + e0) + q4);
    u32x2 fq;
    asm volatile("global_load_dwordx2 %0, %1, %2" : "=&v"(fq) : "v"(0), "s"(freqs) : "memory");
    {
        const __amdgpu_buffer_rsrc_t rs_pos = __builtin_amdgcn_make_buffer_rsrc((void*)pos, 0, NN * 6, 0x00020000);
#pragma unroll
        for (int i = 0; i < 5; ++i) { const int p = wv + 16 * i; if (p < 74) bdma16(rs_pos, p * 1024 + lane * 16, 0, p * 1024); }
    }
    if (tid < OFS) hist[tid] = 0;
    if (tid == 0) *ncand = 0;
    asm volatile("s_waitcnt vmcnt(0) lgkmcnt(0)\n\ts_barrier" : "+v"(fq) :: "memory");
    if (4 * tid < EPB) {
        u32x2 pw[4][2]; unsigned j6[4][2];
#pragma unroll
        for (int j = 0; j < 4; ++j) {
            j6[j][0] = __umul24(sv4[j], 6); j6[j][1] = __umul24(dv4[j], 6);
            pw[j][0] = pos_words(j6[j][0]); pw[j][1] = pos_words(j6[j][1]);
        }
        asm volatile("" :: "v"(pw[0][0]), "v"(pw[0][1]), "v"(pw[1][0]), "v"(pw[1][1]), "v"(pw[2][0]), "v"(pw[2][1]), "v"(pw[3][0]), "v"(pw[3][1]));
        bool act[4];
        unsigned below = 0, total = 0;
#pragma unroll
        for (int j = 0; j < 4; ++j) {
            const Pos3 ps = unpack_pos(pw[j][0], j6[j][0]), pd = unpack_pos(pw[j][1], j6[j][1]);
            const float rx = ps.x - pd.x, ry = ps.y - pd.y, rz = ps.z - pd.z;
            const float ss = rx * rx + ry * ry + rz * rz;
            act[j] = __float_as_uint(ss) - 0x41d00000u > 0x7f7fffffu - 0x41d00000u;
            const unsigned long long m = ballot(act[j]);
            below = __builtin_amdgcn_mbcnt_hi((unsigned)(m >> 32), __builtin_amdgcn_mbcnt_lo((unsigned)m, below));
            total += __popcll(m);
        }
        unsigned run = 0;
        if (lane == 0) run = lds_add_rtn(L1_HIST + OFS * 4, total);
        unsigned la = L1_LIST + (__builtin_amdgcn_readfirstlane(run) + below) * 8;
#pragma unroll
        for (int j = 0; j < 4; ++j)
            if (act[j]) { *(lds_u32x2w_t)(size_t)la = (u32x2){(unsigned)sv4[j] | ((unsigned)dv4[j] << 16), (unsigned)(4 * tid + j)}; la += 8; }
    }
    __syncthreads();
    const int n = *ncand;
    const float f0 = bflo(fq[0]), f1 = bfhi(fq[0]), f2 = bflo(fq[1]), f3 = bfhi(fq[1]);
    u32x2 en = {0u, 0u};
    Rec rc; rc.act = false;
    unsigned rg = 0, slot = 0;
    if ((tid & ~63) < n) {
        en = list[tid];
        const bool valid = tid < n;
        en.x = valid ? en.x : 0u;
        rc = edge_record(en.x & 0xffff, en.x >> 16, f0, f1, f2, f3);
        rc.act &= valid;
        rg = (en.x >> 16) / RN;
        if (rc.act) slot = atomicAdd(&hist[rg], 1u);
    }
    for (int i = tid + T1; i < n; i += T1) {
        const u32x2 e = list[i];
        const Rec r = edge_record(e.x & 0xffff, e.x >> 16, f0, f1, f2, f3);
        list[i].y = e.y | ((r.act ? atomicAdd(&hist[(e.x >> 16) / RN], 1u) : 0xffffu) << 16);
    }
    __syncthreads();
    if (wv == 0) {
        constexpr int PER = OFS / 64;
        const u32x4 ca = *(const u32x4*)(hist + lane * PER), cb = *(const u32x4*)(hist + lane * PER + 4);
        unsigned c[PER] = {ca[0], ca[1], ca[2], ca[3], cb[0], cb[1], cb[2], cb[3]}, sum = 0;
#pragma unroll
        for (int k = 0; k < PER; ++k) sum += c[k];
        unsigned run = wave_incl_scan(sum) - sum;
        unsigned o[PER];
#pragma unroll
        for (int k = 0; k < PER; ++k) { o[k] = run; run += c[k]; }
        *(u32x4*)(hist + lane * PER) = (u32x4){o[0], o[1], o[2], o[3]};
        *(u32x4*)(hist + lane * PER + 4) = (u32x4){o[4], o[5], o[6], o[7]};
    }
    __syncthreads();
    unsigned c0 = 0, c1 = 0;
    if ((tid & ~63) < n) {
        c0 = hist[rg]; c1 = hist[rg + 1];
        if (rc.act) carr[c0 + slot] = (unsigned short)en.y;
    }
    for (int i = tid + T1; i < n; i += T1) {
        const u32x2 e = list[i];
        if ((e.y >> 16) != 0xffff) carr[hist[(e.x >> 16) / RN] + (e.y >> 16)] = (unsigned short)(e.y & 0xffff);
    }
    if (tid >= T1 - OFS && tid < T1 - OFS + NR) {
        const int r = tid - (T1 - OFS);
        const unsigned a0 = hist[r], a1 = hist[r + 1];
        ((unsigned*)(ws + WS_CELL))[r * 256 + (b & 7) * 32 + (b >> 3)] = a0 | ((a1 - a0) << 16);
    }
    __syncthreads();
    const bool staged = n <= T1;
    if ((tid & ~63) < n) {
        unsigned rank = 0;
        unsigned cv[4];
#pragma unroll
        for (int q = 0; q < 4; ++q) cv[q] = carr[min(c0 + q, (unsigned)EPB - 1)];
#pragma unroll
        for (int q = 0; q < 4; ++q) rank += (c0 + q < c1) & (cv[q] < en.y);
        if (rc.act) {
            for (unsigned q = c0 + 4; q < c1; ++q) rank += (carr[q] < en.y);
            const unsigned d = en.x >> 16, m = (en.x & 0xffff) | ((d - rg * RN) << 16);
            if (staged) {
                unsigned* p = (unsigned*)(smem + (c0 + rank) * 20);
                p[0] = rc.f[0]; p[1] = rc.f[1]; p[2] = rc.f[2]; p[3] = rc.f[3]; p[4] = m;
            } else store_record(ws, e0 + c0 + rank, rc.f, m);
        }
    }
    if (staged) {
        __syncthreads();
        const unsigned bytes = hist[NR] * 20;
        const __amdgpu_buffer_rsrc_t rs_rec = __builtin_amdgcn_make_buffer_rsrc((void*)(ws + WS_REC + (size_t)e0 * 20), 0, EPB * 20, 0x00020000);
        for (unsigned o = tid * 16; o < ((bytes + 127) & ~127u); o += T1 * 16)
            __builtin_amdgcn_raw_buffer_store_b128(*(const u32x4*)(smem + o), rs_rec, o, 0, 16);
    }
    for (int i = tid + T1; i < n; i += T1) {
        const u32x2 e = list[i];
        if ((e.y >> 16) == 0xffff) continue;
        const unsigned s = e.x & 0xffff, d = e.x >> 16, el = e.y & 0xffff, r = d / RN;
        const unsigned a0 = hist[r], a1 = hist[r + 1];
        unsigned rank = 0;
        for (unsigned q = a0; q < a1; ++q) rank += (carr[q] < el);
        const Rec rr = edge_record(s, d, f0, f1, f2, f3);
        store_record(ws, e0 + a0 + rank, rr.f, s | ((d - r * RN) << 16));
    }
}

constexpr int L1_W1 = 0;
constexpr int L1_W2 = 32768;
constexpr int L1_X  = L1_W2 + 98304;
constexpr int L1_H0 = L1_X + GTL * 4096;
constexpr int OPITCH = 784;
constexpr int L1_GEMM_END = L1_H0 + 8192;
constexpr int K1_LDS = L1_GEMM_END > L1_EDGE_END ? L1_GEMM_END : L1_EDGE_END;
static_assert(32 * OPITCH <= 32768 && GTL == 6 && K1_LDS <= 163840, "");
__host__ __device__ constexpr int l1_h(int s) { return s == 0 ? L1_H0 : L1_X + (s - 1) * 8192; }
__host__ __device__ constexpr int l1_o(int s) { return s == 0 ? L1_W1 : L1_W2 + (s - 1) * 32768; }

__device__ __forceinline__ s16x4 tr_read(unsigned a) { return __builtin_amdgcn_ds_read_tr16_b64_v4i16((lds_s16x4_t)(size_t)a); }
__device__ __forceinline__ bf16x8 cat8(s16x4 a, s16x4 b) { return __builtin_shufflevector(a, b, 0, 1, 2, 3, 4, 5, 6, 7); }
typedef __attribute__((address_space(3))) u32x4* lds_u32x4_t;
__device__ __forceinline__ u32x4 lds_ld16(unsigned a) { return *(lds_u32x4_t)(size_t)a; }

#define PIN8(x) asm volatile("" :: "v"(x[0][0]), "v"(x[0][1]), "v"(x[0][2]), "v"(x[0][3]), "v"(x[1][0]), "v"(x[1][1]), "v"(x[1][2]), "v"(x[1][3]))
__device__ __forceinline__ void gemm_block(int g, const unsigned short* __restrict__ sv, const unsigned short* __restrict__ w1,
                                           const unsigned short* __restrict__ w2, char* __restrict__ ws) {
    const int tid = threadIdx.x;
    const int lane = tid & 63, wv = __builtin_amdgcn_readfirstlane(tid >> 6), slot = lane & 15, q = lane >> 4;
    const int w = wv & 7;
    const int n0 = g * GT;
    {
        const __amdgpu_buffer_rsrc_t rs_x = __builtin_amdgcn_make_buffer_rsrc((void*)sv, 0, NN * 1024, 0x00020000);
        const __amdgpu_buffer_rsrc_t rs_w1 = __builtin_amdgcn_make_buffer_rsrc((void*)w1, 0, 128 * 256, 0x00020000);
        const __amdgpu_buffer_rsrc_t rs_w2 = __builtin_amdgcn_make_buffer_rsrc((void*)w2, 0, 128 * 768, 0x00020000);
        const unsigned o1 = q * 256 + ((((slot >> 1) ^ (q | ((wv & 2) << 1))) << 1) | (slot & 1)) * 16;
        bdma16(rs_w1, o1, wv * 1024, L1_W1 + wv * 1024);
        bdma16(rs_w1, o1, (wv + 16) * 1024, L1_W1 + (wv + 16) * 1024);
        const unsigned xo = slot * 1024 + q * 16 + (n0 + 16 * (wv >> 2)) * 1024 + (wv & 3) * 64;
        bdma16(rs_x, xo, 0, L1_X + wv * 1024);
        if (wv < 8) bdma16(rs_x, xo + 64 * 1024, 0, L1_X + (16 + wv) * 1024);
        asm volatile("s_barrier" ::: "memory");
#pragma unroll
        for (int s3 = 0; s3 < 3; ++s3) {
            const int c = 64 * s3 + lane, row = c / 48, cp = c - 48 * row, P = cp >> 1;
            const unsigned o2 = row * 768 + ((((P & ~7) | ((P & 7) ^ ((4 * (wv & 1) + row) & 7))) << 1) | (cp & 1)) * 16;
            bdma16(rs_w2, o2, wv * 3072, L1_W2 + wv * 3072 + s3 * 1024);
            bdma16(rs_w2, o2, (wv + 16) * 3072, L1_W2 + (wv + 16) * 3072 + s3 * 1024);
        }
    }
    const int qq = slot >> 2, p4 = slot & 3;
    asm volatile("s_waitcnt vmcnt(6)\n\ts_barrier" ::: "memory");
    constexpr int NP = GTL / 2;
    const __amdgpu_buffer_rsrc_t rs_phi = __builtin_amdgcn_make_buffer_rsrc((void*)(ws + WS_PHI), 0, NN * 1024, 0x00020000);
    auto ship = [&](int s) {
#pragma unroll
        for (int k = 0; k < 3; ++k) {
            const int c = 192 * w + 64 * k + lane, r = c / 48, cp = c - 48 * r;
            __builtin_amdgcn_raw_buffer_store_b128(lds_ld16(l1_o(s) + r * OPITCH + cp * 16), rs_phi, (n0 + 32 * s + r) * 1024 + cp * 16, 0, 16);
        }
    };
    if (wv < 8) {
        bf16x8 a1[4];
        {
            const unsigned base = L1_W1 + (8 * q + qq) * 256 + (((w ^ (qq | ((q & 1) << 2))) << 1) | (p4 >> 1)) * 16 + 8 * (p4 & 1);
#pragma unroll
            for (int ks = 0; ks < 4; ++ks) a1[ks] = cat8(tr_read(base + ks * 8192), tr_read(base + ks * 8192 + 1024));
        }
        const unsigned hw = (w >> 1) * 1024 + lane * 16 + (w & 1) * 8;
#pragma unroll
        for (int s = 0; s < NP + 1; ++s) {
            if (s >= 2) ship(s - 2);
            if (s < NP) {
                u32x4 xb[2][4];
#pragma unroll
                for (int i = 0; i < 2; ++i)
#pragma unroll
                    for (int ks = 0; ks < 4; ++ks) xb[i][ks] = lds_ld16(L1_X + ((2 * s + i) * 4 + ks) * 1024 + lane * 16);
                PIN8(xb);
                f32x4 acc[2] = {{0, 0, 0, 0}, {0, 0, 0, 0}};
#pragma unroll
                for (int ks = 0; ks < 4; ++ks)
#pragma unroll
                    for (int i = 0; i < 2; ++i) acc[i] = __builtin_amdgcn_mfma_f32_16x16x32_bf16(a1[ks], __builtin_bit_cast(bf16x8, xb[i][ks]), acc[i], 0, 0, 0);
#pragma unroll
                for (int i = 0; i < 2; ++i) {
                    const unsigned x01 = pack2(acc[i][0], acc[i][1]), x23 = pack2(acc[i][2], acc[i][3]);
                    const float x[4] = {bflo(x01), bfhi(x01), bflo(x23), bfhi(x23)};
                    float v[4];
#pragma unroll
                    for (int r = 0; r < 4; ++r) v[r] = x[r] * __builtin_amdgcn_rcpf(1.0f + __builtin_amdgcn_exp2f(-1.44269504f * x[r]));
                    *(lds_u32x2w_t)(size_t)(l1_h(s) + i * 4096 + hw) = (u32x2){pack2(v[0], v[1]), pack2(v[2], v[3])};
                }
            }
            if (s == 0) asm volatile("s_waitcnt vmcnt(0)" ::: "memory");
            raw_barrier();
        }
        ship(NP - 1);
    } else {
        asm volatile("s_waitcnt vmcnt(0)" ::: "memory");
        raw_barrier();
        bf16x8 a2[3][4];
#pragma unroll
        for (int t = 0; t < 3; ++t) {
            const int P = 3 * w + t, row0 = 4 * q + qq;
            const unsigned base = L1_W2 + row0 * 768 + ((P & ~7) | ((P & 7) ^ (row0 & 7))) * 32 + (p4 >> 1) * 16 + 8 * (p4 & 1);
#pragma unroll
            for (int ks = 0; ks < 4; ++ks) a2[t][ks] = cat8(tr_read(base + ks * 24576), tr_read(base + ks * 24576 + 12288));
        }
        const unsigned obase = slot * OPITCH + (48 * w + 4 * q) * 2;
#pragma unroll
        for (int s = 1; s < NP + 1; ++s) {
            u32x4 hb[2][4];
#pragma unroll
            for (int i = 0; i < 2; ++i)
#pragma unroll
                for (int ks = 0; ks < 4; ++ks) hb[i][ks] = lds_ld16(l1_h(s - 1) + i * 4096 + ks * 1024 + lane * 16);
            PIN8(hb);
            f32x4 acc[2][3];
#pragma unroll
            for (int i = 0; i < 2; ++i)
#pragma unroll
                for (int t = 0; t < 3; ++t) acc[i][t] = (f32x4){0, 0, 0, 0};
#pragma unroll
            for (int i = 0; i < 2; ++i)
#pragma unroll
                for (int ks = 0; ks < 4; ++ks)
#pragma unroll
                    for (int t = 0; t < 3; ++t) acc[i][t] = __builtin_amdgcn_mfma_f32_16x16x32_bf16(a2[t][ks], __builtin_bit_cast(bf16x8, hb[i][ks]), acc[i][t], 0, 0, 0);
#pragma unroll
            for (int i = 0; i < 2; ++i)
#pragma unroll
                for (int t = 0; t < 3; ++t)
                    *(lds_u32x2w_t)(size_t)(l1_o(s - 1) + obase + i * 16 * OPITCH + t * 32) = (u32x2){pack2(acc[i][t][0], acc[i][t][1]), pack2(acc[i][t][2], acc[i][t][3])};
            raw_barrier();
        }
    }
}

__global__ __launch_bounds__(T1) void k1_prepare(const unsigned short* __restrict__ sv, const unsigned short* __restrict__ pos,
                                                 const int* __restrict__ src, const int* __restrict__ dst,
                                                 const unsigned short* __restrict__ freqs, const unsigned short* __restrict__ w1,
                                                 const unsigned short* __restrict__ w2, char* __restrict__ ws) {
    extern __shared__ __attribute__((aligned(16))) char smem[];
    const int b = blockIdx.x;
    if (b < NB_E) edge_block(b, pos, src, dst, freqs, ws, smem);
    else gemm_block(b - NB_E, sv, w1, w2, ws);
}

constexpr int NCH     = CAP / 64;
constexpr int NITMAX  = CAP / 2 + RN;
constexpr int L2_U    = 0;
constexpr int L2_RES  = L2_U + (CAP + 1) * 32;
constexpr int L2_CNT  = L2_RES + (RN + 1) * 512;
constexpr int L2_SLOT = L2_CNT + 32 * NCH * 2;
constexpr int L2_MISC = L2_SLOT + (NITMAX + 1) * 16;
constexpr int K2_LDS  = L2_MISC + 32;
static_assert(L2_U == 0 && L2_RES % 16 == 0 && L2_CNT % 16 == 0 && L2_SLOT % 16 == 0 && NCH == 8 && RN <= 31 && CAP * 32 <= 0x4000, "");
constexpr unsigned PAD_A = CAP * 32, PAD_B = 0x3fffu << 10;
typedef __attribute__((ext_vector_type(2))) float f32x2;
typedef __attribute__((address_space(3))) const u32x2* lds_u32x2_t;
typedef __attribute__((address_space(3))) const f32x4* lds_f32x4_t;
typedef __attribute__((address_space(3))) const unsigned* lds_u32_t;
__device__ __forceinline__ u32x2 lds_rd64(unsigned a) { return *(lds_u32x2_t)(size_t)a; }
__device__ __forceinline__ f32x4 lds_rd128(unsigned a) { return *(lds_f32x4_t)(size_t)a; }
__device__ __forceinline__ unsigned lds_rd32(unsigned a) { return *(lds_u32_t)(size_t)a; }

struct K2Ctx {
    char* smem; char* ws;
    __amdgpu_buffer_rsrc_t rs_phi, rs_sv, rs_out;
    int tid, lane, wv, h, n0, nn, hh, c2;
    unsigned lo;
};

__device__ __forceinline__ void put_record(char* smem, int p, u32x4 f, unsigned mm) {
    const float fc = bflo(f.z);
    *(f32x4*)(smem + L2_U + p * 32) = (f32x4){bfhi(f.z), bflo(f.w), bfhi(f.w), __uint_as_float(mm)};
    *(u32x2*)(smem + L2_U + p * 32 + 16) = (u32x2){pack2(bflo(f.x) * fc, bfhi(f.x) * fc), pack2(bflo(f.y) * fc, bfhi(f.y) * fc)};
}
__device__ __forceinline__ void put_record_from(char* smem, const char* ws, int p, unsigned gi) {
    const char* rp = ws + WS_REC + (size_t)gi * 20;
    put_record(smem, p, *(const u32x4a4*)rp, *(const unsigned*)(rp + 16));
}
__device__ __forceinline__ unsigned sum2(unsigned x, unsigned c) { return (x & 0xffff) + (x >> 16) + c; }

__device__ __forceinline__ void build_slots(const K2Ctx& c, int m) {
    unsigned short* cnt16 = (unsigned short*)(c.smem + L2_CNT);
    u32x2* slots = (u32x2*)(c.smem + L2_SLOT);
    unsigned mm[NCH / W2], rk[NCH / W2];
#pragma unroll
    for (int i = 0; i < NCH / W2; ++i) {
        const int p = 64 * (c.wv + W2 * i) + c.lane;
        if (64 * (c.wv + W2 * i) < m) {
            const bool valid = p < m;
            mm[i] = lds_rd32(L2_U + min(p, CAP) * 32 + 12);
            const unsigned nl = mm[i] >> 16;
            unsigned long long mk = ballot(valid);
#pragma unroll
            for (int bit = 0; bit < 5; ++bit) { const bool on = (nl >> bit) & 1; const unsigned long long bb = ballot(on); mk &= on ? bb : ~bb; }
            rk[i] = lanes_below(mk);
            if (valid && rk[i] == 0) cnt16[nl * NCH + c.wv + W2 * i] = __popcll(mk);
        }
    }
    raw_barrier();
    if (c.wv == 0 || 64 * c.wv < m) {
        const u32x4 cv = *(const u32x4*)(cnt16 + (c.lane & 31) * NCH);
        const unsigned cnt = sum2(cv[0], sum2(cv[1], sum2(cv[2], sum2(cv[3], 0))));
        const unsigned its = c.lane < c.nn ? max(1u, (cnt + 1) >> 1) : 0u;
        const unsigned inc = wave_incl_scan(its), st = inc - its;
#pragma unroll
        for (int i = 0; i < NCH / W2; ++i) {
            const int ch = c.wv + W2 * i;
            if (64 * ch < m) {
                unsigned below = 0;
#pragma unroll
                for (int d = 0; d < 4; ++d) {
                    const unsigned msk = ch >= 2 * d + 2 ? 0xffffffffu : (ch == 2 * d + 1 ? 0x0000ffffu : 0u);
                    below = sum2(cv[d] & msk, below);
                }
                const unsigned nl = mm[i] >> 16;
                const unsigned a = __shfl((2 * st + below) | (below << 12) | (its << 22), nl);
                const unsigned rank = ((a >> 12) & 1023) + rk[i];
                const unsigned e = ((rank >> 1) + 1 == (a >> 22)) ? nl + 1 : 0u;
                const int p = 64 * ch + c.lane;
                if (p < m) slots[(a & 4095) + rk[i]] = (u32x2){(unsigned)p * 32 | e, (mm[i] & 0xffff) << 10};
            }
        }
        if (c.wv == 0) {
            if (c.lane < c.nn) {
                const u32x2 pe = {PAD_A | (c.lane + 1), PAD_B};
                if ((cnt & 1) | (cnt == 0)) slots[2 * inc - 1] = pe;
                if (cnt == 0) slots[2 * st] = pe;
            } else if (c.lane >= 62) slots[2 * NITMAX + c.lane - 62] = (u32x2){PAD_A, PAD_B};
            const unsigned I = __builtin_amdgcn_readlane(inc, 63);
            unsigned bound = I;
#pragma unroll
            for (int w = 0; w < W2; ++w) {
                const unsigned long long mw = ballot((st >= w * I / W2) & (c.lane < c.nn));
                const unsigned bw = mw ? __builtin_amdgcn_readlane(st, __builtin_ctzll(mw)) : I;
                bound = c.lane == w ? bw : bound;
            }
            if (c.lane <= W2) ((unsigned*)(c.smem + L2_MISC))[1 + c.lane] = bound;
        }
    }
}
__device__ __forceinline__ void read_span(const K2Ctx& c, int& ta, int& tb) {
    const u32x2 x = {lds_rd32(L2_MISC + 4 + 4 * c.wv), lds_rd32(L2_MISC + 8 + 4 * c.wv)};
    ta = __builtin_amdgcn_readfirstlane(x[0]); tb = __builtin_amdgcn_readfirstlane(x[1]);
}

__device__ __forceinline__ float vmul(float a, float b) { float d = a * b; asm("" : "+v"(d)); return d; }
__device__ __forceinline__ float vfma(float a, float b, float c) { float d = __builtin_fmaf(a, b, c); asm("" : "+v"(d)); return d; }
__device__ __forceinline__ f32x2 unpack2(unsigned u) { return (f32x2){bflo(u), bfhi(u)}; }
__device__ __forceinline__ void edge_fma(f32x2 (&acc)[4], const float (&W)[3][2], const unsigned (&ld)[6], f32x4 hat) {
    const f32x2 mv = {vmul(bflo(ld[1]), W[1][0]), vmul(bfhi(ld[1]), W[1][1])};
    const f32x2 mr = {vmul(bflo(ld[2]), W[2][0]), vmul(bfhi(ld[2]), W[2][1])};
    acc[0] = (f32x2){vfma(bflo(ld[0]), W[0][0], acc[0][0]), vfma(bfhi(ld[0]), W[0][1], acc[0][1])};
#pragma unroll
    for (int a = 0; a < 3; ++a) {
        acc[1 + a] = __builtin_elementwise_fma(mv, unpack2(ld[3 + a]), acc[1 + a]);
        acc[1 + a] = __builtin_elementwise_fma(mr, (f32x2){hat[a], hat[a]}, acc[1 + a]);
    }
}
__device__ __forceinline__ f32x4 filter4(u32x2 g, u32x2 wb) {
    return __builtin_amdgcn_mfma_f32_4x4x4bf16_1k(__builtin_bit_cast(s16x4, g), __builtin_bit_cast(s16x4, wb), (f32x4){0.f, 0.f, 0.f, 0.f}, 0, 0, 0);
}
__device__ __forceinline__ void half_reduce(const f32x2 (&acc)[4], f32x2 (&tot)[2]) {
#pragma unroll
    for (int q = 0; q < 2; ++q) {
        const auto s0 = __builtin_amdgcn_permlane32_swap(__float_as_uint(acc[q][0]), __float_as_uint(acc[q + 2][0]), false, false);
        const auto s1 = __builtin_amdgcn_permlane32_swap(__float_as_uint(acc[q][1]), __float_as_uint(acc[q + 2][1]), false, false);
        tot[q] = (f32x2){__uint_as_float(s0[0]), __uint_as_float(s1[0])} + (f32x2){__uint_as_float(s0[1]), __uint_as_float(s1[1])};
    }
}
__device__ __forceinline__ void store_rows(const K2Ctx& c, int nl, const f32x2 (&tot)[2]) {
    const unsigned ra = L2_RES + nl * 512 + c.hh * 256 + c.c2 * 4;
#pragma unroll
    for (int q = 0; q < 2; ++q) {
        const f32x2 o = unpack2(lds_rd32(ra + q * 128)) + tot[q];
        __builtin_amdgcn_raw_buffer_store_b32(pack2(o[0], o[1]), c.rs_out, c.hh * 512 + c.lo + q * 256, (c.n0 + nl) * 1024,   2);
    }
}

struct Batch { unsigned ld[4][6]; unsigned sa[4]; unsigned ga; };
__device__ __forceinline__ void batch_load(Batch& B, const K2Ctx& c, int t4, int tb) {
    unsigned off[4];
#pragma unroll
    for (int j = 0; j < 4; ++j) {
        const int t = (t4 + j < tb) ? t4 + j : NITMAX;
        const u32x2 x = lds_rd64(L2_SLOT + t * 16 + c.hh * 8);
        B.sa[j] = x[0]; off[j] = x[1] | c.lo;
    }
    { const int t = t4 + (c.lane & 3); B.ga = lds_rd32(L2_SLOT + (t < tb ? t : NITMAX) * 16 + c.hh * 8); }
#pragma unroll
    for (int j = 0; j < 4; ++j) {
        B.ld[j][0] = __builtin_amdgcn_raw_buffer_load_b32(c.rs_phi, off[j], 0, 0);
        B.ld[j][1] = __builtin_amdgcn_raw_buffer_load_b32(c.rs_phi, off[j] + 256, 0, 0);
        B.ld[j][2] = __builtin_amdgcn_raw_buffer_load_b32(c.rs_phi, off[j] + 512, 0, 0);
        B.ld[j][3] = __builtin_amdgcn_raw_buffer_load_b32(c.rs_sv, off[j] + 256, 0, 0);
        B.ld[j][4] = __builtin_amdgcn_raw_buffer_load_b32(c.rs_sv, off[j] + 512, 0, 0);
        B.ld[j][5] = __builtin_amdgcn_raw_buffer_load_b32(c.rs_sv, off[j] + 768, 0, 0);
    }
}
__device__ __forceinline__ void batch_compute(const Batch& B, const K2Ctx& c, f32x2 (&acc)[4], const u32x2 (&wb)[3][2], bool first) {
    const u32x2 g = lds_rd64((B.ga & 0x7fe0) + 16);
    f32x4 hat[4];
#pragma unroll
    for (int j = 0; j < 4; ++j) hat[j] = lds_rd128(B.sa[j] & 0x7fe0);
    f32x4 W4[3][2];
#pragma unroll
    for (int q = 0; q < 3; ++q) { W4[q][0] = filter4(g, wb[q][0]); W4[q][1] = filter4(g, wb[q][1]); }
#pragma unroll
    for (int j = 0; j < 4; ++j) {
        const float W[3][2] = {{W4[0][0][j], W4[0][1][j]}, {W4[1][0][j], W4[1][1][j]}, {W4[2][0][j], W4[2][1][j]}};
        edge_fma(acc, W, B.ld[j], hat[j]);
        if (j == 0 && first) asm volatile("s_barrier" : "+v"(acc[0]), "+v"(acc[1]), "+v"(acc[2]), "+v"(acc[3]) :: "memory");
        const int en = __builtin_amdgcn_readfirstlane(B.sa[j]) & 31;
        if (en) {
            f32x2 tot[2];
            half_reduce(acc, tot);
            store_rows(c, en - 1, tot);
#pragma unroll
            for (int a = 0; a < 4; ++a) acc[a] = (f32x2){0.f, 0.f};
        }
    }
}

__global__ __launch_bounds__(T2, 4) void k2_gather(const unsigned short* __restrict__ sv, const unsigned short* __restrict__ wr,
                                                   char* __restrict__ ws, char* __restrict__ out) {
    extern __shared__ __attribute__((aligned(16))) char smem[];
    K2Ctx c;
    c.smem = smem; c.ws = ws;
    c.tid = threadIdx.x; c.lane = c.tid & 63; c.wv = __builtin_amdgcn_readfirstlane(c.tid >> 6);
    c.h = blockIdx.x & 1; const int r = ((blockIdx.x >> 1) & 3) * NRQ + (blockIdx.x >> 3);
    c.n0 = r * RN; c.nn = min(RN, NN - c.n0);
    c.hh = c.lane >> 5; c.c2 = c.lane & 31;
    c.lo = c.h * 128 + c.c2 * 4;
    c.rs_phi = __builtin_amdgcn_make_buffer_rsrc((void*)(ws + WS_PHI), 0, NN * 1024, 0x00020000);
    c.rs_sv = __builtin_amdgcn_make_buffer_rsrc((void*)sv, 0, NN * 1024, 0x00020000);
    c.rs_out = __builtin_amdgcn_make_buffer_rsrc((void*)out, 0, NN * 1024, 0x00020000);
    const int lane = c.lane, tid = c.tid, wv = c.wv, h = c.h, hh = c.hh, c2 = c.c2;
    int* tot_lds = (int*)(smem + L2_MISC);
    unsigned myce = 0, mycb = 0;
    const unsigned* cellp = (const unsigned*)(ws + WS_CELL) + r * 256 + (lane >> 4) * 32 + (lane & 15);
    unsigned ce0 = cellp[0], ce1 = cellp[128];
    u32x2 wb[3][2];
    {
        unsigned wraw[3][4];
#pragma unroll
        for (int p = 0; p < 3; ++p)
#pragma unroll
            for (int k = 0; k < 4; ++k) wraw[p][k] = *(const unsigned*)(wr + k * 384 + p * 128 + h * 64 + 2 * c2);
        if (wv >= 2) {
            ((unsigned*)(smem + L2_CNT))[tid - 128] = 0;
            if (tid < 130) ((f32x4*)(smem + L2_U))[2 * CAP + tid - 128] = (f32x4){0.f, 0.f, 0.f, 0.f};
            static_assert(32 * NCH * 2 == 128 * 4, "");
        }
#pragma unroll
        for (int p = 0; p < 3; ++p)
#pragma unroll
            for (int k = 0; k < 2; ++k) { wb[p][0][k] = __builtin_amdgcn_perm(wraw[p][2 * k + 1], wraw[p][2 * k], 0x05040100u); wb[p][1][k] = __builtin_amdgcn_perm(wraw[p][2 * k + 1], wraw[p][2 * k], 0x07060302u); }
        asm volatile("" : "+v"(wb[0][0]), "+v"(wb[0][1]), "+v"(wb[1][0]), "+v"(wb[1][1]), "+v"(wb[2][0]), "+v"(wb[2][1]));
    }
    if (wv < 2) {
        if (8 * (lane & 15) + (lane >> 4) >= NB_E) ce0 = 0;
        if (8 * (lane & 15) + (lane >> 4) + 4 >= NB_E) ce1 = 0;
        const unsigned cc0 = ce0 >> 16, cc1 = ce1 >> 16;
        const unsigned in0 = wave_incl_scan(cc0), t0 = __builtin_amdgcn_readlane(in0, 63);
        const unsigned in1 = wave_incl_scan(cc1) + t0;
        myce = wv == 0 ? ce0 : ce1;
        mycb = wv == 0 ? in0 - cc0 : in1 - cc1;
        if (tid == 63) *tot_lds = in1;
    }
    const unsigned mycc = myce >> 16;
    const unsigned gi0 = (8 * (tid & 15) + (tid >> 4)) * EPB + (myce & 0xffff);
    u32x4 rf[6]; unsigned rm[6];
    if (wv < 2) {
        const __amdgpu_buffer_rsrc_t rs_rec = __builtin_amdgcn_make_buffer_rsrc((void*)(ws + WS_REC), 0, NB_E * EPB * 20, 0x00020000);
#pragma unroll
        for (int k = 0; k < 6; ++k) {
            const unsigned go = ((unsigned)k < mycc && mycb + k < CAP) ? (gi0 + k) * 20 : 0x80000000u;
            asm volatile("buffer_load_dwordx4 %0, %2, %3, 0 offen\n\tbuffer_load_dword %1, %2, %3, 0 offen offset:16" : "=&v"(rf[k]), "=&v"(rm[k]) : "v"(go), "s"(rs_rec) : "memory");
        }
    }
    asm volatile("s_barrier" ::: "memory");
    if (wv >= 2) {
#pragma unroll
        for (int i4 = 0; i4 < (RN + 3) / 4; ++i4) {
            const int i = min(wv - 2 + 2 * i4, (RN + 1) / 2 - 1);
            const int node = min(c.n0 + 2 * i + hh, NN - 1);
            dma16_asm((const char*)sv + (size_t)node * 1024 + ((lane >> 3) & 3) * 256 + h * 128 + (lane & 7) * 16, L2_RES + i * 1024);
        }
    } else {
        asm volatile("s_waitcnt vmcnt(0)" : "+v"(rf[0]), "+v"(rf[1]), "+v"(rf[2]), "+v"(rf[3]), "+v"(rf[4]), "+v"(rf[5]),
                     "+v"(rm[0]), "+v"(rm[1]), "+v"(rm[2]), "+v"(rm[3]), "+v"(rm[4]), "+v"(rm[5]) :: "memory");
#pragma unroll
        for (int k = 0; k < 6; ++k) if ((unsigned)k < mycc && mycb + k < CAP) put_record(smem, mycb + k, rf[k], rm[k]);
        for (unsigned q = 6; q < mycc && mycb + q < CAP; ++q) put_record_from(smem, ws, mycb + q, gi0 + q);
    }
    __builtin_amdgcn_s_waitcnt(0xc07f);
    asm volatile("s_barrier" ::: "memory");
    const int total = __builtin_amdgcn_readfirstlane(*tot_lds);
    int ta, tb;
    build_slots(c, min(total, CAP));
    raw_barrier();
    read_span(c, ta, tb);
    f32x2 acc[4];
#pragma unroll
    for (int a = 0; a < 4; ++a) acc[a] = (f32x2){0.f, 0.f};

    if (total <= CAP) {
        Batch A;
        __builtin_amdgcn_s_setprio(3);
        const int q1 = ta + (tb - ta) / 4, q2 = ta + (tb - ta) / 2, q3 = tb - (tb - ta) / 4;
        for (int t4 = ta; t4 == ta || t4 < tb; t4 += 4) {
            if (t4 >= q3) __builtin_amdgcn_s_setprio(0); else if (t4 >= q2) __builtin_amdgcn_s_setprio(1); else if (t4 >= q1) __builtin_amdgcn_s_setprio(2);
            batch_load(A, c, t4, tb);
            batch_compute(A, c, acc, wb, t4 == ta);
        }
    } else {
        float* accw = (float*)(ws + WS_ACC);
        asm volatile("s_waitcnt vmcnt(0)\n\ts_barrier" ::: "memory");
        for (int w0 = 0;;) {
            const bool firstw = (w0 == 0), lastw = (w0 + CAP >= total);
#pragma unroll 1
            for (int t = ta; t < tb; ++t) {
                const u32x2 sl = lds_rd64(L2_SLOT + t * 16 + hh * 8);
                const unsigned off = sl[1] | c.lo;
                unsigned ld[6];
#pragma unroll
                for (int a = 0; a < 3; ++a) { ld[a] = __builtin_amdgcn_raw_buffer_load_b32(c.rs_phi, off + a * 256, 0, 0); ld[3 + a] = __builtin_amdgcn_raw_buffer_load_b32(c.rs_sv, off + 256 + a * 256, 0, 0); }
                const u32x2 g = lds_rd64((sl[0] & 0x7fe0) + 16);
                float W[3][2];
#pragma unroll
                for (int q = 0; q < 3; ++q) { W[q][0] = filter4(g, wb[q][0])[0]; W[q][1] = filter4(g, wb[q][1])[0]; }
                edge_fma(acc, W, ld, lds_rd128(sl[0] & 0x7fe0));
                const int en = __builtin_amdgcn_readfirstlane(sl[0]) & 31;
                if (en) {
                    f32x2 tot[2];
                    half_reduce(acc, tot);
                    float* aw = accw + (((size_t)(c.n0 + en - 1) * 2 + h) * 4 + 2 * hh) * 64 + 2 * c2;
                    if (!firstw) {
#pragma unroll
                        for (int q = 0; q < 2; ++q) { tot[q][0] += aw[q * 64]; tot[q][1] += aw[q * 64 + 1]; }
                    }
                    if (lastw) store_rows(c, en - 1, tot);
                    else {
#pragma unroll
                        for (int q = 0; q < 2; ++q) { aw[q * 64] = tot[q][0]; aw[q * 64 + 1] = tot[q][1]; }
                    }
#pragma unroll
                    for (int a = 0; a < 4; ++a) acc[a] = (f32x2){0.f, 0.f};
                }
            }
            w0 += CAP;
            if (w0 >= total) break;
            __syncthreads();
            if (tid < 128) ((unsigned*)(smem + L2_CNT))[tid] = 0;
            for (unsigned q = 0; q < mycc; ++q) {
                const int p = (int)(mycb + q) - w0;
                if (p >= 0 && p < CAP) put_record_from(smem, ws, p, gi0 + q);
            }
            __syncthreads();
            build_slots(c, min(CAP, total - w0));
            __syncthreads();
            read_span(c, ta, tb);
        }
    }
}

extern "C" void kernel_launch(void* const* d_in, const int* in_sizes, int n_in, void* d_out, int out_size, void* d_ws, size_t ws_size,
                              hipStream_t stream) {
    const unsigned short* sv = (const unsigned short*)d_in[0];
    const unsigned short* pos = (const unsigned short*)d_in[1];
    const int* src = (const int*)d_in[2];
    const int* dst = (const int*)d_in[3];
    const unsigned short* freqs = (const unsigned short*)d_in[4];
    const unsigned short* w1 = (const unsigned short*)d_in[5];
    const unsigned short* w2 = (const unsigned short*)d_in[6];
    const unsigned short* wr = (const unsigned short*)d_in[7];
    (void)hipFuncSetAttribute((const void*)k1_prepare, hipFuncAttributeMaxDynamicSharedMemorySize, K1_LDS);
    (void)hipFuncSetAttribute((const void*)k2_gather, hipFuncAttributeMaxDynamicSharedMemorySize, K2_LDS);
    k1_prepare<<<NB_E + NB_G, T1, K1_LDS, stream>>>(sv, pos, src, dst, freqs, w1, w2, (char*)d_ws);
    k2_gather<<<NR * 2, T2, K2_LDS, stream>>>(sv, wr, (char*)d_ws, (char*)d_out);
}
```

```cpp
#include <hip/hip_runtime.h>
#include <hip/hip_bf16.h>
#include <stdint.h>

typedef __attribute__((ext_vector_type(8))) short bf16x8;
typedef __attribute__((ext_vector_type(4))) short s16x4;
typedef __attribute__((ext_vector_type(4))) float f32x4;
typedef __attribute__((ext_vector_type(4))) unsigned u32x4;
typedef __attribute__((ext_vector_type(4), aligned(4))) unsigned u32x4a4;
typedef __attribute__((ext_vector_type(2))) unsigned u32x2;
typedef __attribute__((ext_vector_type(4))) int i32x4;
typedef __attribute__((address_space(3))) s16x4* lds_s16x4_t;

constexpr int NN   = 12500;
constexpr int NE   = 400000;
constexpr int T1   = 1024;
constexpr int EPB  = 3200;
constexpr int NB_E = NE / EPB;
constexpr int GTL  = 6;
constexpr int GT   = 16 * GTL;
constexpr int NB_G = (NN + GT - 1) / GT;
constexpr int RN   = 25;
constexpr int NR   = (NN + RN - 1) / RN;
constexpr int NRQ  = NR / 4;
constexpr int OFS  = 512;
constexpr int T2   = 256;
constexpr int W2   = T2 / 64;
constexpr int CAP  = 512;
static_assert(NB_E <= 128 && NB_E * EPB == NE && T2 == 256 && NR % 4 == 0 && EPB % 4 == 0 && EPB <= 4 * T1, "");
static_assert(OFS >= NR + 1 && RN <= 62 && OFS <= T1, "");

constexpr size_t WS_PHI  = 0;
constexpr size_t WS_REC  = 12800000;
constexpr size_t WS_CELL = WS_REC + (size_t)NB_E * EPB * 20;
constexpr size_t WS_ACC  = WS_CELL + (size_t)NR * 1024;
constexpr size_t WS_END  = WS_ACC + (size_t)NN * 512 * 4;
static_assert(WS_CELL % 128 == 0 && WS_ACC % 128 == 0, "");

__device__ __forceinline__ float bflo(unsigned u) { return __uint_as_float(u << 16); }
__device__ __forceinline__ float bfhi(unsigned u) { return __uint_as_float(u & 0xffff0000u); }
typedef __attribute__((ext_vector_type(2))) __bf16 bf16x2_t;
typedef __attribute__((ext_vector_type(2))) float f32x2_t;
__device__ __forceinline__ unsigned pack2(float lo, float hi) { f32x2_t f = {lo, hi}; bf16x2_t b = __builtin_convertvector(f, bf16x2_t); return __builtin_bit_cast(unsigned, b); }
__device__ __forceinline__ float rbf(float x) { return bflo(pack2(x, 0.f)); }

__device__ __forceinline__ unsigned wave_incl_scan(unsigned x) {
    x += __builtin_amdgcn_update_dpp(0u, x, 0x111, 0xf, 0xf, false);
    x += __builtin_amdgcn_update_dpp(0u, x, 0x112, 0xf, 0xf, false);
    x += __builtin_amdgcn_update_dpp(0u, x, 0x114, 0xf, 0xf, false);
    x += __builtin_amdgcn_update_dpp(0u, x, 0x118, 0xf, 0xf, false);
    x += __builtin_amdgcn_update_dpp(0u, x, 0x142, 0xa, 0xf, false);
    x += __builtin_amdgcn_update_dpp(0u, x, 0x143, 0xc, 0xf, false);
    return x;
}
__device__ __forceinline__ unsigned lanes_below(unsigned long long m) {
    return __builtin_amdgcn_mbcnt_hi((unsigned)(m >> 32), __builtin_amdgcn_mbcnt_lo((unsigned)m, 0u));
}
__device__ __forceinline__ unsigned long long ballot(bool p) { return __builtin_amdgcn_ballot_w64(p); }
__device__ __forceinline__ unsigned lds_add_rtn(unsigned lds_addr, unsigned v) {
    unsigned old;
    asm volatile("ds_add_rtn_u32 %0, %1, %2\n\ts_waitcnt lgkmcnt(0)" : "=v"(old) : "v"(lds_addr), "v"(v) : "memory");
    return old;
}
__device__ __forceinline__ void dma16_asm(const void* g, unsigned lds_addr) {
    unsigned keep;
    asm volatile("s_mov_b32 %0, m0\n\ts_mov_b32 m0, %2\n\ts_nop 0\n\tglobal_load_lds_dwordx4 %1, off\n\ts_mov_b32 m0, %0" : "=&s"(keep) : "v"(g), "s"(lds_addr) : "memory");
}
__device__ __forceinline__ void bdma16(__amdgpu_buffer_rsrc_t rs, unsigned voff, unsigned soff, unsigned lds_addr) {
    unsigned keep;
    asm volatile("s_mov_b32 %0, m0\n\ts_mov_b32 m0, %4\n\ts_nop 0\n\tbuffer_load_dwordx4 %1, %2, %3 offen lds\n\ts_mov_b32 m0, %0" : "=&s"(keep) : "v"(voff), "s"(rs), "s"(soff), "s"(lds_addr) : "memory");
}
__device__ __forceinline__ void raw_barrier() { asm volatile("s_waitcnt lgkmcnt(0)\n\ts_barrier" ::: "memory"); }

__device__ __forceinline__ float rev_frac(float x) {
    const float C_HI = 0.15915494f, C_LO = (float)(0.15915494309189535 - (double)0.15915494f);
    const float r = x * C_HI;
    const float e = __builtin_fmaf(x, C_HI, -r) + x * C_LO;
    return __builtin_amdgcn_fractf(r) + e;
}
__device__ __forceinline__ float fsin(float x) { return __builtin_amdgcn_sinf(rev_frac(x)); }
__device__ __forceinline__ float fcos(float x) { return __builtin_amdgcn_cosf(rev_frac(x)); }

struct Pos3 { float x, y, z; };
typedef __attribute__((address_space(3))) const unsigned* lds_cu32_t;
typedef __attribute__((address_space(3))) u32x2* lds_u32x2w_t;
__device__ __forceinline__ u32x2 pos_words(unsigned j6) { lds_cu32_t p = (lds_cu32_t)(size_t)(j6 & ~3u); return (u32x2){p[0], p[1]}; }
__device__ __forceinline__ Pos3 unpack_pos(u32x2 d, unsigned j6) {
    const unsigned xy = __builtin_amdgcn_alignbyte(d[1], d[0], j6), zz = __builtin_amdgcn_alignbyte(d[1], d[1], j6);
    Pos3 p;
    p.x = bflo(xy); p.y = bfhi(xy); p.z = bflo(zz);
    return p;
}
__device__ __forceinline__ Pos3 lds_pos(unsigned j) { const unsigned j6 = __umul24(j, 6); return unpack_pos(pos_words(j6), j6); }

struct Rec { u32x4 f; bool act; };
__device__ __forceinline__ Rec edge_record(unsigned s, unsigned d, float f0, float f1, float f2, float f3) {
    const Pos3 ps = lds_pos(s), pd = lds_pos(d);
    const unsigned rxy = pack2(ps.x - pd.x, ps.y - pd.y);
    const float rx = bflo(rxy), ry = bfhi(rxy), rz = rbf(ps.z - pd.z);
    const unsigned sxy = pack2(rx * rx, ry * ry);
    const float ss = rbf(bflo(sxy) + bfhi(sxy) + rbf(rz * rz));
    const float dd = rbf(__builtin_amdgcn_sqrtf(rbf(ss + 1e-8f)));
    float fc = rbf(0.5f * rbf(rbf(fcos(rbf(rbf(3.14159265358979f * dd) * 0.2f))) + 1.0f));
    fc = (dd < 5.0f) ? fc : 0.0f * dd;
    const float inv = __builtin_amdgcn_rcpf(dd);
    const unsigned a01 = pack2(dd * f0, dd * f1), a23 = pack2(dd * f2, dd * f3);
    const unsigned s01 = pack2(fsin(bflo(a01)), fsin(bfhi(a01))), s23 = pack2(fsin(bflo(a23)), fsin(bfhi(a23)));
    const float b0 = bflo(s01) * inv, b1 = bfhi(s01) * inv, b2 = bflo(s23) * inv, b3 = bfhi(s23) * inv;
    Rec r;
    r.f = (u32x4){pack2(b0, b1), pack2(b2, b3), pack2(fc, rx * inv), pack2(ry * inv, rz * inv)};
    r.act = !(fc == 0.0f);
    return r;
}
__device__ __forceinline__ void store_record(char* ws, unsigned gi, u32x4 f, unsigned m) {
    char* p = ws + WS_REC + (size_t)gi * 20;
    *(u32x4a4*)p = f;
    *(unsigned*)(p + 16) = m;
}

constexpr int L1_HIST = 75008;
constexpr int L1_LIST = L1_HIST + OFS * 4 + 16;
constexpr int L1_CARR = L1_LIST + EPB * 8;
constexpr int L1_EDGE_END = L1_CARR + EPB * 2;

__device__ __forceinline__ void edge_block(int b, const unsigned short* __restrict__ pos, const int* __restrict__ src, const int* __restrict__ dst,
                                           const unsigned short* __restrict__ freqs, char* __restrict__ ws, char* smem) {
    unsigned* hist = (unsigned*)(smem + L1_HIST);
    unsigned* ncand = hist + OFS;
    u32x2* list = (u32x2*)(smem + L1_LIST);
    unsigned short* carr = (unsigned short*)(smem + L1_CARR);
    const int tid = threadIdx.x, lane = tid & 63, wv = __builtin_amdgcn_readfirstlane(tid >> 6);
    const int e0 = b * EPB;
    const int q4 = min(tid, EPB / 4 - 1);
    const i32x4 sv4 = __builtin_nontemporal_load((const i32x4*)(src + e0) + q4);
    const i32x4 dv4 = __builtin_nontemporal_load((const i32x4*)(dst + e0) + q4);
    u32x2 fq;
    asm volatile("global_load_dwordx2 %0, %1, %2" : "=&v"(fq) : "v"(0), "s"(freqs) : "memory");
    {
        const __amdgpu_buffer_rsrc_t rs_pos = __builtin_amdgcn_make_buffer_rsrc((void*)pos, 0, NN * 6, 0x00020000);
#pragma unroll
        for (int i = 0; i < 5; ++i) { const int p = wv + 16 * i; if (p < 74) bdma16(rs_pos, p * 1024 + lane * 16, 0, p * 1024); }
    }
    if (tid < OFS) hist[tid] = 0;
    if (tid == 0) *ncand = 0;
    asm volatile("s_waitcnt vmcnt(0) lgkmcnt(0)\n\ts_barrier" : "+v"(fq) :: "memory");
    if (4 * tid < EPB) {
        u32x2 pw[4][2]; unsigned j6[4][2];
#pragma unroll
        for (int j = 0; j < 4; ++j) {
            j6[j][0] = __umul24(sv4[j], 6); j6[j][1] = __umul24(dv4[j], 6);
            pw[j][0] = pos_words(j6[j][0]); pw[j][1] = pos_words(j6[j][1]);
        }
        asm volatile("" :: "v"(pw[0][0]), "v"(pw[0][1]), "v"(pw[1][0]), "v"(pw[1][1]), "v"(pw[2][0]), "v"(pw[2][1]), "v"(pw[3][0]), "v"(pw[3][1]));
        bool act[4];
        unsigned below = 0, total = 0;
#pragma unroll
        for (int j = 0; j < 4; ++j) {
            const Pos3 ps = unpack_pos(pw[j][0], j6[j][0]), pd = unpack_pos(pw[j][1], j6[j][1]);
            const float rx = ps.x - pd.x, ry = ps.y - pd.y, rz = ps.z - pd.z;
            const float ss = rx * rx + ry * ry + rz * rz;
            act[j] = __float_as_uint(ss) - 0x41d00000u > 0x7f7fffffu - 0x41d00000u;
            const unsigned long long m = ballot(act[j]);
            below = __builtin_amdgcn_mbcnt_hi((unsigned)(m >> 32), __builtin_amdgcn_mbcnt_lo((unsigned)m, below));
            total += __popcll(m);
        }
        unsigned run = 0;
        if (lane == 0) run = lds_add_rtn(L1_HIST + OFS * 4, total);
        unsigned la = L1_LIST + (__builtin_amdgcn_readfirstlane(run) + below) * 8;
#pragma unroll
        for (int j = 0; j < 4; ++j)
            if (act[j]) { *(lds_u32x2w_t)(size_t)la = (u32x2){(unsigned)sv4[j] | ((unsigned)dv4[j] << 16), (unsigned)(4 * tid + j)}; la += 8; }
    } else if (wv >= 13) {
        unsigned long long pc; unsigned delta;
        asm volatile("s_getpc_b64 %0\n.Lpf%=:\n\ts_mov_b32 %1, _Z9k2_gatherPKtS0_PcS1_-.Lpf%=" : "=s"(pc), "=s"(delta));
        pc += delta;
        u32x4 sink[4];
        const unsigned vo = (tid - 832) * 16;
        asm volatile("global_load_dwordx4 %0, %4, %5\n\tglobal_load_dwordx4 %1, %4, %5 offset:3072\n\tglobal_load_dwordx4 %2, %6, %5\n\tglobal_load_dwordx4 %3, %6, %5 offset:3072\n\ts_waitcnt vmcnt(0)"
                     : "=&v"(sink[0]), "=&v"(sink[1]), "=&v"(sink[2]), "=&v"(sink[3]) : "v"(vo), "s"(pc), "v"(vo + 6144) : "memory");
    }
    __syncthreads();
    const int n = *ncand;
    const float f0 = bflo(fq[0]), f1 = bfhi(fq[0]), f2 = bflo(fq[1]), f3 = bfhi(fq[1]);
    u32x2 en = {0u, 0u};
    Rec rc; rc.act = false;
    unsigned rg = 0, slot = 0;
    if ((tid & ~63) < n) {
        en = list[tid];
        const bool valid = tid < n;
        en.x = valid ? en.x : 0u;
        rc = edge_record(en.x & 0xffff, en.x >> 16, f0, f1, f2, f3);
        rc.act &= valid;
        rg = (en.x >> 16) / RN;
        if (rc.act) slot = atomicAdd(&hist[rg], 1u);
    }
    for (int i = tid + T1; i < n; i += T1) {
        const u32x2 e = list[i];
        const Rec r = edge_record(e.x & 0xffff, e.x >> 16, f0, f1, f2, f3);
        list[i].y = e.y | ((r.act ? atomicAdd(&hist[(e.x >> 16) / RN], 1u) : 0xffffu) << 16);
    }
    __syncthreads();
    if (wv == 0) {
        constexpr int PER = OFS / 64;
        const u32x4 ca = *(const u32x4*)(hist + lane * PER), cb = *(const u32x4*)(hist + lane * PER + 4);
        unsigned c[PER] = {ca[0], ca[1], ca[2], ca[3], cb[0], cb[1], cb[2], cb[3]}, sum = 0;
#pragma unroll
        for (int k = 0; k < PER; ++k) sum += c[k];
        unsigned run = wave_incl_scan(sum) - sum;
        unsigned o[PER];
#pragma unroll
        for (int k = 0; k < PER; ++k) { o[k] = run; run += c[k]; }
        *(u32x4*)(hist + lane * PER) = (u32x4){o[0], o[1], o[2], o[3]};
        *(u32x4*)(hist + lane * PER + 4) = (u32x4){o[4], o[5], o[6], o[7]};
    }
    __syncthreads();
    unsigned c0 = 0, c1 = 0;
    if ((tid & ~63) < n) {
        c0 = hist[rg]; c1 = hist[rg + 1];
        if (rc.act) carr[c0 + slot] = (unsigned short)en.y;
    }
    for (int i = tid + T1; i < n; i += T1) {
        const u32x2 e = list[i];
        if ((e.y >> 16) != 0xffff) carr[hist[(e.x >> 16) / RN] + (e.y >> 16)] = (unsigned short)(e.y & 0xffff);
    }
    if (tid >= T1 - OFS && tid < T1 - OFS + NR) {
        const int r = tid - (T1 - OFS);
        const unsigned a0 = hist[r], a1 = hist[r + 1];
        ((unsigned*)(ws + WS_CELL))[r * 256 + (b & 7) * 32 + (b >> 3)] = a0 | ((a1 - a0) << 16);
    }
    __syncthreads();
    const bool staged = n <= T1;
    if ((tid & ~63) < n) {
        unsigned rank = 0;
        unsigned cv[4];
#pragma unroll
        for (int q = 0; q < 4; ++q) cv[q] = carr[min(c0 + q, (unsigned)EPB - 1)];
#pragma unroll
        for (int q = 0; q < 4; ++q) rank += (c0 + q < c1) & (cv[q] < en.y);
        if (rc.act) {
            for (unsigned q = c0 + 4; q < c1; ++q) rank += (carr[q] < en.y);
            const unsigned d = en.x >> 16, m = (en.x & 0xffff) | ((d - rg * RN) << 16);
            if (staged) {
                unsigned* p = (unsigned*)(smem + (c0 + rank) * 20);
                p[0] = rc.f[0]; p[1] = rc.f[1]; p[2] = rc.f[2]; p[3] = rc.f[3]; p[4] = m;
            } else store_record(ws, e0 + c0 + rank, rc.f, m);
        }
    }
    if (staged) {
        __syncthreads();
        const unsigned bytes = hist[NR] * 20;
        const __amdgpu_buffer_rsrc_t rs_rec = __builtin_amdgcn_make_buffer_rsrc((void*)(ws + WS_REC + (size_t)e0 * 20), 0, EPB * 20, 0x00020000);
        for (unsigned o = tid * 16; o < ((bytes + 127) & ~127u); o += T1 * 16)
            __builtin_amdgcn_raw_buffer_store_b128(*(const u32x4*)(smem + o), rs_rec, o, 0, 16);
    }
    for (int i = tid + T1; i < n; i += T1) {
        const u32x2 e = list[i];
        if ((e.y >> 16) == 0xffff) continue;
        const unsigned s = e.x & 0xffff, d = e.x >> 16, el = e.y & 0xffff, r = d / RN;
        const unsigned a0 = hist[r], a1 = hist[r + 1];
        unsigned rank = 0;
        for (unsigned q = a0; q < a1; ++q) rank += (carr[q] < el);
        const Rec rr = edge_record(s, d, f0, f1, f2, f3);
        store_record(ws, e0 + a0 + rank, rr.f, s | ((d - r * RN) << 16));
    }
}

constexpr int L1_W1 = 0;
constexpr int L1_W2 = 32768;
constexpr int L1_X  = L1_W2 + 98304;
constexpr int L1_H0 = L1_X + GTL * 4096;
constexpr int OPITCH = 784;
constexpr int L1_GEMM_END = L1_H0 + 8192;
constexpr int K1_LDS = L1_GEMM_END > L1_EDGE_END ? L1_GEMM_END : L1_EDGE_END;
static_assert(32 * OPITCH <= 32768 && GTL == 6 && K1_LDS <= 163840, "");
__host__ __device__ constexpr int l1_h(int s) { return s == 0 ? L1_H0 : L1_X + (s - 1) * 8192; }
__host__ __device__ constexpr int l1_o(int s) { return s == 0 ? L1_W1 : L1_W2 + (s - 1) * 32768; }

__device__ __forceinline__ s16x4 tr_read(unsigned a) { return __builtin_amdgcn_ds_read_tr16_b64_v4i16((lds_s16x4_t)(size_t)a); }
__device__ __forceinline__ bf16x8 cat8(s16x4 a, s16x4 b) { return __builtin_shufflevector(a, b, 0, 1, 2, 3, 4, 5, 6, 7); }
typedef __attribute__((address_space(3))) u32x4* lds_u32x4_t;
__device__ __forceinline__ u32x4 lds_ld16(unsigned a) { return *(lds_u32x4_t)(size_t)a; }

#define PIN8(x) asm volatile("" :: "v"(x[0][0]), "v"(x[0][1]), "v"(x[0][2]), "v"(x[0][3]), "v"(x[1][0]), "v"(x[1][1]), "v"(x[1][2]), "v"(x[1][3]))
__device__ __forceinline__ void gemm_block(int g, const unsigned short* __restrict__ sv, const unsigned short* __restrict__ w1,
                                           const unsigned short* __restrict__ w2, char* __restrict__ ws) {
    const int tid = threadIdx.x;
    const int lane = tid & 63, wv = __builtin_amdgcn_readfirstlane(tid >> 6), slot = lane & 15, q = lane >> 4;
    const int w = wv & 7;
    const int n0 = g * GT;
    {
        const __amdgpu_buffer_rsrc_t rs_x = __builtin_amdgcn_make_buffer_rsrc((void*)sv, 0, NN * 1024, 0x00020000);
        const __amdgpu_buffer_rsrc_t rs_w1 = __builtin_amdgcn_make_buffer_rsrc((void*)w1, 0, 128 * 256, 0x00020000);
        const __amdgpu_buffer_rsrc_t rs_w2 = __builtin_amdgcn_make_buffer_rsrc((void*)w2, 0, 128 * 768, 0x00020000);
        const unsigned o1 = q * 256 + ((((slot >> 1) ^ (q | ((wv & 2) << 1))) << 1) | (slot & 1)) * 16;
        bdma16(rs_w1, o1, wv * 1024, L1_W1 + wv * 1024);
        bdma16(rs_w1, o1, (wv + 16) * 1024, L1_W1 + (wv + 16) * 1024);
        const unsigned xo = slot * 1024 + q * 16 + (n0 + 16 * (wv >> 2)) * 1024 + (wv & 3) * 64;
        bdma16(rs_x, xo, 0, L1_X + wv * 1024);
        if (wv < 8) bdma16(rs_x, xo + 64 * 1024, 0, L1_X + (16 + wv) * 1024);
        asm volatile("s_barrier" ::: "memory");
#pragma unroll
        for (int s3 = 0; s3 < 3; ++s3) {
            const int c = 64 * s3 + lane, row = c / 48, cp = c - 48 * row, P = cp >> 1;
            const unsigned o2 = row * 768 + ((((P & ~7) | ((P & 7) ^ ((4 * (wv & 1) + row) & 7))) << 1) | (cp & 1)) * 16;
            bdma16(rs_w2, o2, wv * 3072, L1_W2 + wv * 3072 + s3 * 1024);
            bdma16(rs_w2, o2, (wv + 16) * 3072, L1_W2 + (wv + 16) * 3072 + s3 * 1024);
        }
    }
    const int qq = slot >> 2, p4 = slot & 3;
    asm volatile("s_waitcnt vmcnt(6)\n\ts_barrier" ::: "memory");
    constexpr int NP = GTL / 2;
    const __amdgpu_buffer_rsrc_t rs_phi = __builtin_amdgcn_make_buffer_rsrc((void*)(ws + WS_PHI), 0, NN * 1024, 0x00020000);
    auto ship = [&](int s) {
#pragma unroll
        for (int k = 0; k < 3; ++k) {
            const int c = 192 * w + 64 * k + lane, r = c / 48, cp = c - 48 * r;
            __builtin_amdgcn_raw_buffer_store_b128(lds_ld16(l1_o(s) + r * OPITCH + cp * 16), rs_phi, (n0 + 32 * s + r) * 1024 + cp * 16, 0, 16);
        }
    };
    if (wv < 8) {
        bf16x8 a1[4];
        {
            const unsigned base = L1_W1 + (8 * q + qq) * 256 + (((w ^ (qq | ((q & 1) << 2))) << 1) | (p4 >> 1)) * 16 + 8 * (p4 & 1);
#pragma unroll
            for (int ks = 0; ks < 4; ++ks) a1[ks] = cat8(tr_read(base + ks * 8192), tr_read(base + ks * 8192 + 1024));
        }
        const unsigned hw = (w >> 1) * 1024 + lane * 16 + (w & 1) * 8;
#pragma unroll
        for (int s = 0; s < NP + 1; ++s) {
            if (s >= 2) ship(s - 2);
            if (s < NP) {
                u32x4 xb[2][4];
#pragma unroll
                for (int i = 0; i < 2; ++i)
#pragma unroll
                    for (int ks = 0; ks < 4; ++ks) xb[i][ks] = lds_ld16(L1_X + ((2 * s + i) * 4 + ks) * 1024 + lane * 16);
                PIN8(xb);
                f32x4 acc[2] = {{0, 0, 0, 0}, {0, 0, 0, 0}};
#pragma unroll
                for (int ks = 0; ks < 4; ++ks)
#pragma unroll
                    for (int i = 0; i < 2; ++i) acc[i] = __builtin_amdgcn_mfma_f32_16x16x32_bf16(a1[ks], __builtin_bit_cast(bf16x8, xb[i][ks]), acc[i], 0, 0, 0);
#pragma unroll
                for (int i = 0; i < 2; ++i) {
                    const unsigned x01 = pack2(acc[i][0], acc[i][1]), x23 = pack2(acc[i][2], acc[i][3]);
                    const float x[4] = {bflo(x01), bfhi(x01), bflo(x23), bfhi(x23)};
                    float v[4];
#pragma unroll
                    for (int r = 0; r < 4; ++r) v[r] = x[r] * __builtin_amdgcn_rcpf(1.0f + __builtin_amdgcn_exp2f(-1.44269504f * x[r]));
                    *(lds_u32x2w_t)(size_t)(l1_h(s) + i * 4096 + hw) = (u32x2){pack2(v[0], v[1]), pack2(v[2], v[3])};
                }
            }
            if (s == 0) asm volatile("s_waitcnt vmcnt(0)" ::: "memory");
            raw_barrier();
        }
        ship(NP - 1);
    } else {
        asm volatile("s_waitcnt vmcnt(0)" ::: "memory");
        raw_barrier();
        bf16x8 a2[3][4];
#pragma unroll
        for (int t = 0; t < 3; ++t) {
            const int P = 3 * w + t, row0 = 4 * q + qq;
            const unsigned base = L1_W2 + row0 * 768 + ((P & ~7) | ((P & 7) ^ (row0 & 7))) * 32 + (p4 >> 1) * 16 + 8 * (p4 & 1);
#pragma unroll
            for (int ks = 0; ks < 4; ++ks) a2[t][ks] = cat8(tr_read(base + ks * 24576), tr_read(base + ks * 24576 + 12288));
        }
        const unsigned obase = slot * OPITCH + (48 * w + 4 * q) * 2;
#pragma unroll
        for (int s = 1; s < NP + 1; ++s) {
            u32x4 hb[2][4];
#pragma unroll
            for (int i = 0; i < 2; ++i)
#pragma unroll
                for (int ks = 0; ks < 4; ++ks) hb[i][ks] = lds_ld16(l1_h(s - 1) + i * 4096 + ks * 1024 + lane * 16);
            PIN8(hb);
            f32x4 acc[2][3];
#pragma unroll
            for (int i = 0; i < 2; ++i)
#pragma unroll
                for (int t = 0; t < 3; ++t) acc[i][t] = (f32x4){0, 0, 0, 0};
#pragma unroll
            for (int i = 0; i < 2; ++i)
#pragma unroll
                for (int ks = 0; ks < 4; ++ks)
#pragma unroll
                    for (int t = 0; t < 3; ++t) acc[i][t] = __builtin_amdgcn_mfma_f32_16x16x32_bf16(a2[t][ks], __builtin_bit_cast(bf16x8, hb[i][ks]), acc[i][t], 0, 0, 0);
#pragma unroll
            for (int i = 0; i < 2; ++i)
#pragma unroll
                for (int t = 0; t < 3; ++t)
                    *(lds_u32x2w_t)(size_t)(l1_o(s - 1) + obase + i * 16 * OPITCH + t * 32) = (u32x2){pack2(acc[i][t][0], acc[i][t][1]), pack2(acc[i][t][2], acc[i][t][3])};
            raw_barrier();
        }
    }
}

__global__ __launch_bounds__(T1) void k1_prepare(const unsigned short* __restrict__ sv, const unsigned short* __restrict__ pos,
                                                 const int* __restrict__ src, const int* __restrict__ dst,
                                                 const unsigned short* __restrict__ freqs, const unsigned short* __restrict__ w1,
                                                 const unsigned short* __restrict__ w2, char* __restrict__ ws) {
    extern __shared__ __attribute__((aligned(16))) char smem[];
    const int b = blockIdx.x;
    if (b < NB_E) edge_block(b, pos, src, dst, freqs, ws, smem);
    else gemm_block(b - NB_E, sv, w1, w2, ws);
}

constexpr int NCH     = CAP / 64;
constexpr int NITMAX  = CAP / 2 + RN;
constexpr int L2_U    = 0;
constexpr int L2_RES  = L2_U + (CAP + 1) * 32;
constexpr int L2_CNT  = L2_RES + (RN + 1) * 512;
constexpr int L2_SLOT = L2_CNT + 32 * NCH * 2;
constexpr int L2_MISC = L2_SLOT + (NITMAX + 1) * 16;
constexpr int K2_LDS  = L2_MISC + 32;
static_assert(L2_U == 0 && L2_RES % 16 == 0 && L2_CNT % 16 == 0 && L2_SLOT % 16 == 0 && NCH == 8 && RN <= 31 && CAP * 32 <= 0x4000, "");
constexpr unsigned PAD_A = CAP * 32, PAD_B = 0x3fffu << 10;
typedef __attribute__((ext_vector_type(2))) float f32x2;
typedef __attribute__((address_space(3))) const u32x2* lds_u32x2_t;
typedef __attribute__((address_space(3))) const f32x4* lds_f32x4_t;
typedef __attribute__((address_space(3))) const unsigned* lds_u32_t;
__device__ __forceinline__ u32x2 lds_rd64(unsigned a) { return *(lds_u32x2_t)(size_t)a; }
__device__ __forceinline__ f32x4 lds_rd128(unsigned a) { return *(lds_f32x4_t)(size_t)a; }
__device__ __forceinline__ unsigned lds_rd32(unsigned a) { return *(lds_u32_t)(size_t)a; }

struct K2Ctx {
    char* smem; char* ws;
    __amdgpu_buffer_rsrc_t rs_phi, rs_sv, rs_out;
    int tid, lane, wv, h, n0, nn, hh, c2;
    unsigned lo;
};

__device__ __forceinline__ void put_record(char* smem, int p, u32x4 f, unsigned mm) {
    const float fc = bflo(f.z);
    *(f32x4*)(smem + L2_U + p * 32) = (f32x4){bfhi(f.z), bflo(f.w), bfhi(f.w), __uint_as_float(mm)};
    *(u32x2*)(smem + L2_U + p * 32 + 16) = (u32x2){pack2(bflo(f.x) * fc, bfhi(f.x) * fc), pack2(bflo(f.y) * fc, bfhi(f.y) * fc)};
}
__device__ __forceinline__ void put_record_from(char* smem, const char* ws, int p, unsigned gi) {
    const char* rp = ws + WS_REC + (size_t)gi * 20;
    put_record(smem, p, *(const u32x4a4*)rp, *(const unsigned*)(rp + 16));
}
__device__ __forceinline__ unsigned sum2(unsigned x, unsigned c) { return (x & 0xffff) + (x >> 16) + c; }

__device__ __forceinline__ void build_slots(const K2Ctx& c, int m) {
    unsigned short* cnt16 = (unsigned short*)(c.smem + L2_CNT);
    u32x2* slots = (u32x2*)(c.smem + L2_SLOT);
    unsigned mm[NCH / W2], rk[NCH / W2];
#pragma unroll
    for (int i = 0; i < NCH / W2; ++i) {
        const int p = 64 * (c.wv + W2 * i) + c.lane;
        if (64 * (c.wv + W2 * i) < m) {
            const bool valid = p < m;
            mm[i] = lds_rd32(L2_U + min(p, CAP) * 32 + 12);
            const unsigned nl = mm[i] >> 16;
            unsigned long long mk = ballot(valid);
#pragma unroll
            for (int bit = 0; bit < 5; ++bit) { const bool on = (nl >> bit) & 1; const unsigned long long bb = ballot(on); mk &= on ? bb : ~bb; }
            rk[i] = lanes_below(mk);
            if (valid && rk[i] == 0) cnt16[nl * NCH + c.wv + W2 * i] = __popcll(mk);
        }
    }
    raw_barrier();
    if (c.wv == 0 || 64 * c.wv < m) {
        const u32x4 cv = *(const u32x4*)(cnt16 + (c.lane & 31) * NCH);
        const unsigned cnt = sum2(cv[0], sum2(cv[1], sum2(cv[2], sum2(cv[3], 0))));
        const unsigned its = c.lane < c.nn ? max(1u, (cnt + 1) >> 1) : 0u;
        const unsigned inc = wave_incl_scan(its), st = inc - its;
#pragma unroll
        for (int i = 0; i < NCH / W2; ++i) {
            const int ch = c.wv + W2 * i;
            if (64 * ch < m) {
                unsigned below = 0;
#pragma unroll
                for (int d = 0; d < 4; ++d) {
                    const unsigned msk = ch >= 2 * d + 2 ? 0xffffffffu : (ch == 2 * d + 1 ? 0x0000ffffu : 0u);
                    below = sum2(cv[d] & msk, below);
                }
                const unsigned nl = mm[i] >> 16;
                const unsigned a = __shfl((2 * st + below) | (below << 12) | (its << 22), nl);
                const unsigned rank = ((a >> 12) & 1023) + rk[i];
                const unsigned e = ((rank >> 1) + 1 == (a >> 22)) ? nl + 1 : 0u;
                const int p = 64 * ch + c.lane;
                if (p < m) slots[(a & 4095) + rk[i]] = (u32x2){(unsigned)p * 32 | e, (mm[i] & 0xffff) << 10};
            }
        }
        if (c.wv == 0) {
            if (c.lane < c.nn) {
                const u32x2 pe = {PAD_A | (c.lane + 1), PAD_B};
                if ((cnt & 1) | (cnt == 0)) slots[2 * inc - 1] = pe;
                if (cnt == 0) slots[2 * st] = pe;
            } else if (c.lane >= 62) slots[2 * NITMAX + c.lane - 62] = (u32x2){PAD_A, PAD_B};
            const unsigned I = __builtin_amdgcn_readlane(inc, 63);
            unsigned bound = I;
#pragma unroll
            for (int w = 0; w < W2; ++w) {
                const unsigned long long mw = ballot((st >= w * I / W2) & (c.lane < c.nn));
                const unsigned bw = mw ? __builtin_amdgcn_readlane(st, __builtin_ctzll(mw)) : I;
                bound = c.lane == w ? bw : bound;
            }
            if (c.lane <= W2) ((unsigned*)(c.smem + L2_MISC))[1 + c.lane] = bound;
        }
    }
}
__device__ __forceinline__ void read_span(const K2Ctx& c, int& ta, int& tb) {
    const u32x2 x = {lds_rd32(L2_MISC + 4 + 4 * c.wv), lds_rd32(L2_MISC + 8 + 4 * c.wv)};
    ta = __builtin_amdgcn_readfirstlane(x[0]); tb = __builtin_amdgcn_readfirstlane(x[1]);
}

__device__ __forceinline__ float vmul(float a, float b) { float d = a * b; asm("" : "+v"(d)); return d; }
__device__ __forceinline__ float vfma(float a, float b, float c) { float d = __builtin_fmaf(a, b, c); asm("" : "+v"(d)); return d; }
__device__ __forceinline__ f32x2 unpack2(unsigned u) { return (f32x2){bflo(u), bfhi(u)}; }
__device__ __forceinline__ void edge_fma(f32x2 (&acc)[4], const float (&W)[3][2], const unsigned (&ld)[6], f32x4 hat) {
    const f32x2 mv = {vmul(bflo(ld[1]), W[1][0]), vmul(bfhi(ld[1]), W[1][1])};
    const f32x2 mr = {vmul(bflo(ld[2]), W[2][0]), vmul(bfhi(ld[2]), W[2][1])};
    acc[0] = (f32x2){vfma(bflo(ld[0]), W[0][0], acc[0][0]), vfma(bfhi(ld[0]), W[0][1], acc[0][1])};
#pragma unroll
    for (int a = 0; a < 3; ++a) {
        acc[1 + a] = __builtin_elementwise_fma(mv, unpack2(ld[3 + a]), acc[1 + a]);
        acc[1 + a] = __builtin_elementwise_fma(mr, (f32x2){hat[a], hat[a]}, acc[1 + a]);
    }
}
__device__ __forceinline__ f32x4 filter4(u32x2 g, u32x2 wb) {
    return __builtin_amdgcn_mfma_f32_4x4x4bf16_1k(__builtin_bit_cast(s16x4, g), __builtin_bit_cast(s16x4, wb), (f32x4){0.f, 0.f, 0.f, 0.f}, 0, 0, 0);
}
__device__ __forceinline__ void half_reduce(const f32x2 (&acc)[4], f32x2 (&tot)[2]) {
#pragma unroll
    for (int q = 0; q < 2; ++q) {
        const auto s0 = __builtin_amdgcn_permlane32_swap(__float_as_uint(acc[q][0]), __float_as_uint(acc[q + 2][0]), false, false);
        const auto s1 = __builtin_amdgcn_permlane32_swap(__float_as_uint(acc[q][1]), __float_as_uint(acc[q + 2][1]), false, false);
        tot[q] = (f32x2){__uint_as_float(s0[0]), __uint_as_float(s1[0])} + (f32x2){__uint_as_float(s0[1]), __uint_as_float(s1[1])};
    }
}
__device__ __forceinline__ void store_rows(const K2Ctx& c, int nl, const f32x2 (&tot)[2]) {
    const unsigned ra = L2_RES + nl * 512 + c.hh * 256 + c.c2 * 4;
#pragma unroll
    for (int q = 0; q < 2; ++q) {
        const f32x2 o = unpack2(lds_rd32(ra + q * 128)) + tot[q];
        __builtin_amdgcn_raw_buffer_store_b32(pack2(o[0], o[1]), c.rs_out, c.hh * 512 + c.lo + q * 256, (c.n0 + nl) * 1024,   2);
    }
}

struct Batch { unsigned ld[4][6]; unsigned sa[4]; unsigned ga; };
__device__ __forceinline__ void batch_load(Batch& B, const K2Ctx& c, int t4, int tb) {
    unsigned off[4];
#pragma unroll
    for (int j = 0; j < 4; ++j) {
        const int t = (t4 + j < tb) ? t4 + j : NITMAX;
        const u32x2 x = lds_rd64(L2_SLOT + t * 16 + c.hh * 8);
        B.sa[j] = x[0]; off[j] = x[1] | c.lo;
    }
    { const int t = t4 + (c.lane & 3); B.ga = lds_rd32(L2_SLOT + (t < tb ? t : NITMAX) * 16 + c.hh * 8); }
#pragma unroll
    for (int j = 0; j < 4; ++j) {
        B.ld[j][0] = __builtin_amdgcn_raw_buffer_load_b32(c.rs_phi, off[j], 0, 0);
        B.ld[j][1] = __builtin_amdgcn_raw_buffer_load_b32(c.rs_phi, off[j] + 256, 0, 0);
        B.ld[j][2] = __builtin_amdgcn_raw_buffer_load_b32(c.rs_phi, off[j] + 512, 0, 0);
        B.ld[j][3] = __builtin_amdgcn_raw_buffer_load_b32(c.rs_sv, off[j] + 256, 0, 0);
        B.ld[j][4] = __builtin_amdgcn_raw_buffer_load_b32(c.rs_sv, off[j] + 512, 0, 0);
        B.ld[j][5] = __builtin_amdgcn_raw_buffer_load_b32(c.rs_sv, off[j] + 768, 0, 0);
    }
}
__device__ __forceinline__ void batch_compute(const Batch& B, const K2Ctx& c, f32x2 (&acc)[4], const u32x2 (&wb)[3][2], bool first) {
    const u32x2 g = lds_rd64((B.ga & 0x7fe0) + 16);
    f32x4 hat[4];
#pragma unroll
    for (int j = 0; j < 4; ++j) hat[j] = lds_rd128(B.sa[j] & 0x7fe0);
    f32x4 W4[3][2];
#pragma unroll
    for (int q = 0; q < 3; ++q) { W4[q][0] = filter4(g, wb[q][0]); W4[q][1] = filter4(g, wb[q][1]); }
#pragma unroll
    for (int j = 0; j < 4; ++j) {
        const float W[3][2] = {{W4[0][0][j], W4[0][1][j]}, {W4[1][0][j], W4[1][1][j]}, {W4[2][0][j], W4[2][1][j]}};
        edge_fma(acc, W, B.ld[j], hat[j]);
        if (j == 0 && first) asm volatile("s_barrier" : "+v"(acc[0]), "+v"(acc[1]), "+v"(acc[2]), "+v"(acc[3]) :: "memory");
        const int en = __builtin_amdgcn_readfirstlane(B.sa[j]) & 31;
        if (en) {
            f32x2 tot[2];
            half_reduce(acc, tot);
            store_rows(c, en - 1, tot);
#pragma unroll
            for (int a = 0; a < 4; ++a) acc[a] = (f32x2){0.f, 0.f};
        }
    }
}

__global__ __launch_bounds__(T2, 4) void k2_gather(const unsigned short* __restrict__ sv, const unsigned short* __restrict__ wr,
                                                   char* __restrict__ ws, char* __restrict__ out) {
    extern __shared__ __attribute__((aligned(16))) char smem[];
    K2Ctx c;
    c.smem = smem; c.ws = ws;
    c.tid = threadIdx.x; c.lane = c.tid & 63; c.wv = __builtin_amdgcn_readfirstlane(c.tid >> 6);
    c.h = blockIdx.x & 1; const int r = ((blockIdx.x >> 1) & 3) * NRQ + (blockIdx.x >> 3);
    c.n0 = r * RN; c.nn = min(RN, NN - c.n0);
    c.hh = c.lane >> 5; c.c2 = c.lane & 31;
    c.lo = c.h * 128 + c.c2 * 4;
    c.rs_phi = __builtin_amdgcn_make_buffer_rsrc((void*)(ws + WS_PHI), 0, NN * 1024, 0x00020000);
    c.rs_sv = __builtin_amdgcn_make_buffer_rsrc((void*)sv, 0, NN * 1024, 0x00020000);
    c.rs_out = __builtin_amdgcn_make_buffer_rsrc((void*)out, 0, NN * 1024, 0x00020000);
    const int lane = c.lane, tid = c.tid, wv = c.wv, h = c.h, hh = c.hh, c2 = c.c2;
    int* tot_lds = (int*)(smem + L2_MISC);
    unsigned myce = 0, mycb = 0;
    const unsigned* cellp = (const unsigned*)(ws + WS_CELL) + r * 256 + (lane >> 4) * 32 + (lane & 15);
    unsigned ce0 = cellp[0], ce1 = cellp[128];
    u32x2 wb[3][2];
    {
        unsigned wraw[3][4];
#pragma unroll
        for (int p = 0; p < 3; ++p)
#pragma unroll
            for (int k = 0; k < 4; ++k) wraw[p][k] = *(const unsigned*)(wr + k * 384 + p * 128 + h * 64 + 2 * c2);
        if (wv >= 2) {
            ((unsigned*)(smem + L2_CNT))[tid - 128] = 0;
            if (tid < 130) ((f32x4*)(smem + L2_U))[2 * CAP + tid - 128] = (f32x4){0.f, 0.f, 0.f, 0.f};
            static_assert(32 * NCH * 2 == 128 * 4, "");
        }
#pragma unroll
        for (int p = 0; p < 3; ++p)
#pragma unroll
            for (int k = 0; k < 2; ++k) { wb[p][0][k] = __builtin_amdgcn_perm(wraw[p][2 * k + 1], wraw[p][2 * k], 0x05040100u); wb[p][1][k] = __builtin_amdgcn_perm(wraw[p][2 * k + 1], wraw[p][2 * k], 0x07060302u); }
        asm volatile("" : "+v"(wb[0][0]), "+v"(wb[0][1]), "+v"(wb[1][0]), "+v"(wb[1][1]), "+v"(wb[2][0]), "+v"(wb[2][1]));
    }
    if (wv < 2) {
        if (8 * (lane & 15) + (lane >> 4) >= NB_E) ce0 = 0;
        if (8 * (lane & 15) + (lane >> 4) + 4 >= NB_E) ce1 = 0;
        const unsigned cc0 = ce0 >> 16, cc1 = ce1 >> 16;
        const unsigned in0 = wave_incl_scan(cc0), t0 = __builtin_amdgcn_readlane(in0, 63);
        const unsigned in1 = wave_incl_scan(cc1) + t0;
        myce = wv == 0 ? ce0 : ce1;
        mycb = wv == 0 ? in0 - cc0 : in1 - cc1;
        if (tid == 63) *tot_lds = in1;
    }
    const unsigned mycc = myce >> 16;
    const unsigned gi0 = (8 * (tid & 15) + (tid >> 4)) * EPB + (myce & 0xffff);
    u32x4 rf[6]; unsigned rm[6];
    if (wv < 2) {
        const __amdgpu_buffer_rsrc_t rs_rec = __builtin_amdgcn_make_buffer_rsrc((void*)(ws + WS_REC), 0, NB_E * EPB * 20, 0x00020000);
#pragma unroll
        for (int k = 0; k < 6; ++k) {
            const unsigned go = ((unsigned)k < mycc && mycb + k < CAP) ? (gi0 + k) * 20 : 0x80000000u;
            asm volatile("buffer_load_dwordx4 %0, %2, %3, 0 offen\n\tbuffer_load_dword %1, %2, %3, 0 offen offset:16" : "=&v"(rf[k]), "=&v"(rm[k]) : "v"(go), "s"(rs_rec) : "memory");
        }
    }
    asm volatile("s_barrier" ::: "memory");
    if (wv >= 2) {
#pragma unroll
        for (int i4 = 0; i4 < (RN + 3) / 4; ++i4) {
            const int i = min(wv - 2 + 2 * i4, (RN + 1) / 2 - 1);
            const int node = min(c.n0 + 2 * i + hh, NN - 1);
            dma16_asm((const char*)sv + (size_t)node * 1024 + ((lane >> 3) & 3) * 256 + h * 128 + (lane & 7) * 16, L2_RES + i * 1024);
        }
    } else {
        asm volatile("s_waitcnt vmcnt(0)" : "+v"(rf[0]), "+v"(rf[1]), "+v"(rf[2]), "+v"(rf[3]), "+v"(rf[4]), "+v"(rf[5]),
                     "+v"(rm[0]), "+v"(rm[1]), "+v"(rm[2]), "+v"(rm[3]), "+v"(rm[4]), "+v"(rm[5]) :: "memory");
#pragma unroll
        for (int k = 0; k < 6; ++k) if ((unsigned)k < mycc && mycb + k < CAP) put_record(smem, mycb + k, rf[k], rm[k]);
        for (unsigned q = 6; q < mycc && mycb + q < CAP; ++q) put_record_from(smem, ws, mycb + q, gi0 + q);
    }
    __builtin_amdgcn_s_waitcnt(0xc07f);
    asm volatile("s_barrier" ::: "memory");
    const int total = __builtin_amdgcn_readfirstlane(*tot_lds);
    int ta, tb;
    build_slots(c, min(total, CAP));
    raw_barrier();
    read_span(c, ta, tb);
    f32x2 acc[4];
#pragma unroll
    for (int a = 0; a < 4; ++a) acc[a] = (f32x2){0.f, 0.f};

    if (total <= CAP) {
        Batch A;
        __builtin_amdgcn_s_setprio(3);
        const int q1 = ta + (tb - ta) / 4, q2 = ta + (tb - ta) / 2, q3 = tb - (tb - ta) / 4;
        for (int t4 = ta; t4 == ta || t4 < tb; t4 += 4) {
            if (t4 >= q3) __builtin_amdgcn_s_setprio(0); else if (t4 >= q2) __builtin_amdgcn_s_setprio(1); else if (t4 >= q1) __builtin_amdgcn_s_setprio(2);
            batch_load(A, c, t4, tb);
            batch_compute(A, c, acc, wb, t4 == ta);
        }
    } else {
        float* accw = (float*)(ws + WS_ACC);
        asm volatile("s_waitcnt vmcnt(0)\n\ts_barrier" ::: "memory");
        for (int w0 = 0;;) {
            const bool firstw = (w0 == 0), lastw = (w0 + CAP >= total);
#pragma unroll 1
            for (int t = ta; t < tb; ++t) {
                const u32x2 sl = lds_rd64(L2_SLOT + t * 16 + hh * 8);
                const unsigned off = sl[1] | c.lo;
                unsigned ld[6];
#pragma unroll
                for (int a = 0; a < 3; ++a) { ld[a] = __builtin_amdgcn_raw_buffer_load_b32(c.rs_phi, off + a * 256, 0, 0); ld[3 + a] = __builtin_amdgcn_raw_buffer_load_b32(c.rs_sv, off + 256 + a * 256, 0, 0); }
                const u32x2 g = lds_rd64((sl[0] & 0x7fe0) + 16);
                float W[3][2];
#pragma unroll
                for (int q = 0; q < 3; ++q) { W[q][0] = filter4(g, wb[q][0])[0]; W[q][1] = filter4(g, wb[q][1])[0]; }
                edge_fma(acc, W, ld, lds_rd128(sl[0] & 0x7fe0));
                const int en = __builtin_amdgcn_readfirstlane(sl[0]) & 31;
                if (en) {
                    f32x2 tot[2];
                    half_reduce(acc, tot);
                    float* aw = accw + (((size_t)(c.n0 + en - 1) * 2 + h) * 4 + 2 * hh) * 64 + 2 * c2;
                    if (!firstw) {
#pragma unroll
                        for (int q = 0; q < 2; ++q) { tot[q][0] += aw[q * 64]; tot[q][1] += aw[q * 64 + 1]; }
                    }
                    if (lastw) store_rows(c, en - 1, tot);
                    else {
#pragma unroll
                        for (int q = 0; q < 2; ++q) { aw[q * 64] = tot[q][0]; aw[q * 64 + 1] = tot[q][1]; }
                    }
#pragma unroll
                    for (int a = 0; a < 4; ++a) acc[a] = (f32x2){0.f, 0.f};
                }
            }
            w0 += CAP;
            if (w0 >= total) break;
            __syncthreads();
            if (tid < 128) ((unsigned*)(smem + L2_CNT))[tid] = 0;
            for (unsigned q = 0; q < mycc; ++q) {
                const int p = (int)(mycb + q) - w0;
                if (p >= 0 && p < CAP) put_record_from(smem, ws, p, gi0 + q);
            }
            __syncthreads();
            build_slots(c, min(CAP, total - w0));
            __syncthreads();
            read_span(c, ta, tb);
        }
    }
}

extern "C" void kernel_launch(void* const* d_in, const int* in_sizes, int n_in, void* d_out, int out_size, void* d_ws, size_t ws_size,
                              hipStream_t stream) {
    const unsigned short* sv = (const unsigned short*)d_in[0];
    const unsigned short* pos = (const unsigned short*)d_in[1];
    const int* src = (const int*)d_in[2];
    const int* dst = (const int*)d_in[3];
    const unsigned short* freqs = (const unsigned short*)d_in[4];
    const unsigned short* w1 = (const unsigned short*)d_in[5];
    const unsigned short* w2 = (const unsigned short*)d_in[6];
    const unsigned short* wr = (const unsigned short*)d_in[7];
    (void)hipFuncSetAttribute((const void*)k1_prepare, hipFuncAttributeMaxDynamicSharedMemorySize, K1_LDS);
    (void)hipFuncSetAttribute((const void*)k2_gather, hipFuncAttributeMaxDynamicSharedMemorySize, K2_LDS);
    k1_prepare<<<NB_E + NB_G, T1, K1_LDS, stream>>>(sv, pos, src, dst, freqs, w1, w2, (char*)d_ws);
    k2_gather<<<NR * 2, T2, K2_LDS, stream>>>(sv, wr, (char*)d_ws, (char*)d_out);
}
```

```cpp
#include <hip/hip_runtime.h>
#include <hip/hip_bf16.h>
#include <stdint.h>

typedef __attribute__((ext_vector_type(8))) short bf16x8;
typedef __attribute__((ext_vector_type(4))) short s16x4;
typedef __attribute__((ext_vector_type(4))) float f32x4;
typedef __attribute__((ext_vector_type(4))) unsigned u32x4;
typedef __attribute__((ext_vector_type(4), aligned(4))) unsigned u32x4a4;
typedef __attribute__((ext_vector_type(2))) unsigned u32x2;
typedef __attribute__((ext_vector_type(4))) int i32x4;
typedef __attribute__((address_space(3))) s16x4* lds_s16x4_t;

constexpr int NN   = 12500;
constexpr int NE   = 400000;
constexpr int T1   = 1024;
constexpr int EPB  = 3200;
constexpr int NB_E = NE / EPB;
constexpr int GTL  = 6;
constexpr int GT   = 16 * GTL;
constexpr int NB_G = (NN + GT - 1) / GT;
constexpr int RN   = 25;
constexpr int NR   = (NN + RN - 1) / RN;
constexpr int NRQ  = NR / 4;
constexpr int OFS  = 512;
constexpr int T2   = 256;
constexpr int W2   = T2 / 64;
constexpr int CAP  = 512;
static_assert(NB_E <= 128 && NB_E * EPB == NE && T2 == 256 && NR % 4 == 0 && EPB % 4 == 0 && EPB <= 4 * T1, "");
static_assert(OFS >= NR + 1 && RN <= 62 && OFS <= T1, "");

constexpr size_t WS_PHI  = 0;
constexpr size_t WS_REC  = 12800000;
constexpr size_t WS_CELL = WS_REC + (size_t)NB_E * EPB * 20;
constexpr size_t WS_ACC  = WS_CELL + (size_t)NR * 1024;
constexpr size_t WS_END  = WS_ACC + (size_t)NN * 512 * 4;
static_assert(WS_CELL % 128 == 0 && WS_ACC % 128 == 0, "");

__device__ __forceinline__ float bflo(unsigned u) { return __uint_as_float(u << 16); }
__device__ __forceinline__ float bfhi(unsigned u) { return __uint_as_float(u & 0xffff0000u); }
typedef __attribute__((ext_vector_type(2))) __bf16 bf16x2_t;
typedef __attribute__((ext_vector_type(2))) float f32x2_t;
__device__ __forceinline__ unsigned pack2(float lo, float hi) { f32x2_t f = {lo, hi}; bf16x2_t b = __builtin_convertvector(f, bf16x2_t); return __builtin_bit_cast(unsigned, b); }
__device__ __forceinline__ float rbf(float x) { return bflo(pack2(x, 0.f)); }

__device__ __forceinline__ unsigned wave_incl_scan(unsigned x) {
    x += __builtin_amdgcn_update_dpp(0u, x, 0x111, 0xf, 0xf, false);
    x += __builtin_amdgcn_update_dpp(0u, x, 0x112, 0xf, 0xf, false);
    x += __builtin_amdgcn_update_dpp(0u, x, 0x114, 0xf, 0xf, false);
    x += __builtin_amdgcn_update_dpp(0u, x, 0x118, 0xf, 0xf, false);
    x += __builtin_amdgcn_update_dpp(0u, x, 0x142, 0xa, 0xf, false);
    x += __builtin_amdgcn_update_dpp(0u, x, 0x143, 0xc, 0xf, false);
    return x;
}
__device__ __forceinline__ unsigned lanes_below(unsigned long long m) {
    return __builtin_amdgcn_mbcnt_hi((unsigned)(m >> 32), __builtin_amdgcn_mbcnt_lo((unsigned)m, 0u));
}
__device__ __forceinline__ unsigned long long ballot(bool p) { return __builtin_amdgcn_ballot_w64(p); }
__device__ __forceinline__ unsigned lds_add_rtn(unsigned lds_addr, unsigned v) {
    unsigned old;
    asm volatile("ds_add_rtn_u32 %0, %1, %2\n\ts_waitcnt lgkmcnt(0)" : "=v"(old) : "v"(lds_addr), "v"(v) : "memory");
    return old;
}
__device__ __forceinline__ void dma16_asm(const void* g, unsigned lds_addr) {
    unsigned keep;
    asm volatile("s_mov_b32 %0, m0\n\ts_mov_b32 m0, %2\n\ts_nop 0\n\tglobal_load_lds_dwordx4 %1, off\n\ts_mov_b32 m0, %0" : "=&s"(keep) : "v"(g), "s"(lds_addr) : "memory");
}
__device__ __forceinline__ void bdma16(__amdgpu_buffer_rsrc_t rs, unsigned voff, unsigned soff, unsigned lds_addr) {
    unsigned keep;
    asm volatile("s_mov_b32 %0, m0\n\ts_mov_b32 m0, %4\n\ts_nop 0\n\tbuffer_load_dwordx4 %1, %2, %3 offen lds\n\ts_mov_b32 m0, %0" : "=&s"(keep) : "v"(voff), "s"(rs), "s"(soff), "s"(lds_addr) : "memory");
}
__device__ __forceinline__ void raw_barrier() { asm volatile("s_waitcnt lgkmcnt(0)\n\ts_barrier" ::: "memory"); }

__device__ __forceinline__ float rev_frac(float x) {
    const float C_HI = 0.15915494f, C_LO = (float)(0.15915494309189535 - (double)0.15915494f);
    const float r = x * C_HI;
    const float e = __builtin_fmaf(x, C_HI, -r) + x * C_LO;
    return __builtin_amdgcn_fractf(r) + e;
}
__device__ __forceinline__ float fsin(float x) { return __builtin_amdgcn_sinf(rev_frac(x)); }
__device__ __forceinline__ float fcos(float x) { return __builtin_amdgcn_cosf(rev_frac(x)); }

#define CODE_ADDR(sym) ({ unsigned long long pc_; int d_; asm volatile("s_getpc_b64 %0\n.Lpf%=:\n\ts_mov_b32 %1, " #sym "-.Lpf%=" : "=s"(pc_), "=s"(d_)); (const char*)(pc_ + (long long)d_); })
__device__ __forceinline__ void touch_12k(const char* code, unsigned t192) {
    u32x4 sink[4];
    const unsigned vo = t192 * 16;
    asm volatile("global_load_dwordx4 %0, %4, %5\n\tglobal_load_dwordx4 %1, %4, %5 offset:3072\n\tglobal_load_dwordx4 %2, %6, %5\n\tglobal_load_dwordx4 %3, %6, %5 offset:3072\n\ts_waitcnt vmcnt(0)"
                 : "=&v"(sink[0]), "=&v"(sink[1]), "=&v"(sink[2]), "=&v"(sink[3]) : "v"(vo), "s"(code), "v"(vo + 6144) : "memory");
}

struct Pos3 { float x, y, z; };
typedef __attribute__((address_space(3))) const unsigned* lds_cu32_t;
typedef __attribute__((address_space(3))) u32x2* lds_u32x2w_t;
__device__ __forceinline__ u32x2 pos_words(unsigned j6) { lds_cu32_t p = (lds_cu32_t)(size_t)(j6 & ~3u); return (u32x2){p[0], p[1]}; }
__device__ __forceinline__ Pos3 unpack_pos(u32x2 d, unsigned j6) {
    const unsigned xy = __builtin_amdgcn_alignbyte(d[1], d[0], j6), zz = __builtin_amdgcn_alignbyte(d[1], d[1], j6);
    Pos3 p;
    p.x = bflo(xy); p.y = bfhi(xy); p.z = bflo(zz);
    return p;
}
__device__ __forceinline__ Pos3 lds_pos(unsigned j) { const unsigned j6 = __umul24(j, 6); return unpack_pos(pos_words(j6), j6); }

struct Rec { u32x4 f; bool act; };
__device__ __forceinline__ Rec edge_record(unsigned s, unsigned d, float f0, float f1, float f2, float f3) {
    const Pos3 ps = lds_pos(s), pd = lds_pos(d);
    const unsigned rxy = pack2(ps.x - pd.x, ps.y - pd.y);
    const float rx = bflo(rxy), ry = bfhi(rxy), rz = rbf(ps.z - pd.z);
    const unsigned sxy = pack2(rx * rx, ry * ry);
    const float ss = rbf(bflo(sxy) + bfhi(sxy) + rbf(rz * rz));
    const float dd = rbf(__builtin_amdgcn_sqrtf(rbf(ss + 1e-8f)));
    float fc = rbf(0.5f * rbf(rbf(fcos(rbf(rbf(3.14159265358979f * dd) * 0.2f))) + 1.0f));
    fc = (dd < 5.0f) ? fc : 0.0f * dd;
    const float inv = __builtin_amdgcn_rcpf(dd);
    const unsigned a01 = pack2(dd * f0, dd * f1), a23 = pack2(dd * f2, dd * f3);
    const unsigned s01 = pack2(fsin(bflo(a01)), fsin(bfhi(a01))), s23 = pack2(fsin(bflo(a23)), fsin(bfhi(a23)));
    const float b0 = bflo(s01) * inv, b1 = bfhi(s01) * inv, b2 = bflo(s23) * inv, b3 = bfhi(s23) * inv;
    Rec r;
    r.f = (u32x4){pack2(b0, b1), pack2(b2, b3), pack2(fc, rx * inv), pack2(ry * inv, rz * inv)};
    r.act = !(fc == 0.0f);
    return r;
}
__device__ __forceinline__ void store_record(char* ws, unsigned gi, u32x4 f, unsigned m) {
    char* p = ws + WS_REC + (size_t)gi * 20;
    *(u32x4a4*)p = f;
    *(unsigned*)(p + 16) = m;
}

constexpr int L1_HIST = 75008;
constexpr int L1_LIST = L1_HIST + OFS * 4 + 16;
constexpr int L1_CARR = L1_LIST + EPB * 8;
constexpr int L1_EDGE_END = L1_CARR + EPB * 2;

__device__ __forceinline__ void edge_block(int b, const unsigned short* __restrict__ pos, const int* __restrict__ src, const int* __restrict__ dst,
                                           const unsigned short* __restrict__ freqs, char* __restrict__ ws, char* smem) {
    unsigned* hist = (unsigned*)(smem + L1_HIST);
    unsigned* ncand = hist + OFS;
    u32x2* list = (u32x2*)(smem + L1_LIST);
    unsigned short* carr = (unsigned short*)(smem + L1_CARR);
    const int tid = threadIdx.x, lane = tid & 63, wv = __builtin_amdgcn_readfirstlane(tid >> 6);
    const int e0 = b * EPB;
    const int q4 = min(tid, EPB / 4 - 1);
    const i32x4 sv4 = __builtin_nontemporal_load((const i32x4*)(src + e0) + q4);
    const i32x4 dv4 = __builtin_nontemporal_load((const i32x4*)(dst + e0) + q4);
    u32x2 fq;
    asm volatile("global_load_dwordx2 %0, %1, %2" : "=&v"(fq) : "v"(0), "s"(freqs) : "memory");
    {
        const __amdgpu_buffer_rsrc_t rs_pos = __builtin_amdgcn_make_buffer_rsrc((void*)pos, 0, NN * 6, 0x00020000);
#pragma unroll
        for (int i = 0; i < 5; ++i) { const int p = wv + 16 * i; if (p < 74) bdma16(rs_pos, p * 1024 + lane * 16, 0, p * 1024); }
    }
    if (tid < OFS) hist[tid] = 0;
    if (tid == 0) *ncand = 0;
    if (wv >= 13) touch_12k(CODE_ADDR(_Z10k1_preparePKtS0_PKiS2_S0_S0_S0_Pc), tid - 832);
    asm volatile("s_waitcnt vmcnt(0) lgkmcnt(0)\n\ts_barrier" : "+v"(fq) :: "memory");
    if (4 * tid < EPB) {
        u32x2 pw[4][2]; unsigned j6[4][2];
#pragma unroll
        for (int j = 0; j < 4; ++j) {
            j6[j][0] = __umul24(sv4[j], 6); j6[j][1] = __umul24(dv4[j], 6);
            pw[j][0] = pos_words(j6[j][0]); pw[j][1] = pos_words(j6[j][1]);
        }
        asm volatile("" :: "v"(pw[0][0]), "v"(pw[0][1]), "v"(pw[1][0]), "v"(pw[1][1]), "v"(pw[2][0]), "v"(pw[2][1]), "v"(pw[3][0]), "v"(pw[3][1]));
        bool act[4];
        unsigned below = 0, total = 0;
#pragma unroll
        for (int j = 0; j < 4; ++j) {
            const Pos3 ps = unpack_pos(pw[j][0], j6[j][0]), pd = unpack_pos(pw[j][1], j6[j][1]);
            const float rx = ps.x - pd.x, ry = ps.y - pd.y, rz = ps.z - pd.z;
            const float ss = rx * rx + ry * ry + rz * rz;
            act[j] = __float_as_uint(ss) - 0x41d00000u > 0x7f7fffffu - 0x41d00000u;
            const unsigned long long m = ballot(act[j]);
            below = __builtin_amdgcn_mbcnt_hi((unsigned)(m >> 32), __builtin_amdgcn_mbcnt_lo((unsigned)m, below));
            total += __popcll(m);
        }
        unsigned run = 0;
        if (lane == 0) run = lds_add_rtn(L1_HIST + OFS * 4, total);
        unsigned la = L1_LIST + (__builtin_amdgcn_readfirstlane(run) + below) * 8;
#pragma unroll
        for (int j = 0; j < 4; ++j)
            if (act[j]) { *(lds_u32x2w_t)(size_t)la = (u32x2){(unsigned)sv4[j] | ((unsigned)dv4[j] << 16), (unsigned)(4 * tid + j)}; la += 8; }
    } else if (wv >= 13) {
        const char* k2 = CODE_ADDR(_Z9k2_gatherPKtS0_PcS1_);
        touch_12k(k2, tid - 832);
    }
    __syncthreads();
    const int n = *ncand;
    const float f0 = bflo(fq[0]), f1 = bfhi(fq[0]), f2 = bflo(fq[1]), f3 = bfhi(fq[1]);
    u32x2 en = {0u, 0u};
    Rec rc; rc.act = false;
    unsigned rg = 0, slot = 0;
    if ((tid & ~63) < n) {
        en = list[tid];
        const bool valid = tid < n;
        en.x = valid ? en.x : 0u;
        rc = edge_record(en.x & 0xffff, en.x >> 16, f0, f1, f2, f3);
        rc.act &= valid;
        rg = (en.x >> 16) / RN;
        if (rc.act) slot = atomicAdd(&hist[rg], 1u);
    }
    for (int i = tid + T1; i < n; i += T1) {
        const u32x2 e = list[i];
        const Rec r = edge_record(e.x & 0xffff, e.x >> 16, f0, f1, f2, f3);
        list[i].y = e.y | ((r.act ? atomicAdd(&hist[(e.x >> 16) / RN], 1u) : 0xffffu) << 16);
    }
    __syncthreads();
    if (wv == 0) {
        constexpr int PER = OFS / 64;
        const u32x4 ca = *(const u32x4*)(hist + lane * PER), cb = *(const u32x4*)(hist + lane * PER + 4);
        unsigned c[PER] = {ca[0], ca[1], ca[2], ca[3], cb[0], cb[1], cb[2], cb[3]}, sum = 0;
#pragma unroll
        for (int k = 0; k < PER; ++k) sum += c[k];
        unsigned run = wave_incl_scan(sum) - sum;
        unsigned o[PER];
#pragma unroll
        for (int k = 0; k < PER; ++k) { o[k] = run; run += c[k]; }
        *(u32x4*)(hist + lane * PER) = (u32x4){o[0], o[1], o[2], o[3]};
        *(u32x4*)(hist + lane * PER + 4) = (u32x4){o[4], o[5], o[6], o[7]};
    }
    __syncthreads();
    unsigned c0 = 0, c1 = 0;
    if ((tid & ~63) < n) {
        c0 = hist[rg]; c1 = hist[rg + 1];
        if (rc.act) carr[c0 + slot] = (unsigned short)en.y;
    }
    for (int i = tid + T1; i < n; i += T1) {
        const u32x2 e = list[i];
        if ((e.y >> 16) != 0xffff) carr[hist[(e.x >> 16) / RN] + (e.y >> 16)] = (unsigned short)(e.y & 0xffff);
    }
    if (tid >= T1 - OFS && tid < T1 - OFS + NR) {
        const int r = tid - (T1 - OFS);
        const unsigned a0 = hist[r], a1 = hist[r + 1];
        ((unsigned*)(ws + WS_CELL))[r * 256 + (b & 7) * 32 + (b >> 3)] = a0 | ((a1 - a0) << 16);
    }
    __syncthreads();
    const bool staged = n <= T1;
    if ((tid & ~63) < n) {
        unsigned rank = 0;
        unsigned cv[4];
#pragma unroll
        for (int q = 0; q < 4; ++q) cv[q] = carr[min(c0 + q, (unsigned)EPB - 1)];
#pragma unroll
        for (int q = 0; q < 4; ++q) rank += (c0 + q < c1) & (cv[q] < en.y);
        if (rc.act) {
            for (unsigned q = c0 + 4; q < c1; ++q) rank += (carr[q] < en.y);
            const unsigned d = en.x >> 16, m = (en.x & 0xffff) | ((d - rg * RN) << 16);
            if (staged) {
                unsigned* p = (unsigned*)(smem + (c0 + rank) * 20);
                p[0] = rc.f[0]; p[1] = rc.f[1]; p[2] = rc.f[2]; p[3] = rc.f[3]; p[4] = m;
            } else store_record(ws, e0 + c0 + rank, rc.f, m);
        }
    }
    if (staged) {
        __syncthreads();
        const unsigned bytes = hist[NR] * 20;
        const __amdgpu_buffer_rsrc_t rs_rec = __builtin_amdgcn_make_buffer_rsrc((void*)(ws + WS_REC + (size_t)e0 * 20), 0, EPB * 20, 0x00020000);
        for (unsigned o = tid * 16; o < ((bytes + 127) & ~127u); o += T1 * 16)
            __builtin_amdgcn_raw_buffer_store_b128(*(const u32x4*)(smem + o), rs_rec, o, 0, 16);
    }
    for (int i = tid + T1; i < n; i += T1) {
        const u32x2 e = list[i];
        if ((e.y >> 16) == 0xffff) continue;
        const unsigned s = e.x & 0xffff, d = e.x >> 16, el = e.y & 0xffff, r = d / RN;
        const unsigned a0 = hist[r], a1 = hist[r + 1];
        unsigned rank = 0;
        for (unsigned q = a0; q < a1; ++q) rank += (carr[q] < el);
        const Rec rr = edge_record(s, d, f0, f1, f2, f3);
        store_record(ws, e0 + a0 + rank, rr.f, s | ((d - r * RN) << 16));
    }
}

constexpr int L1_W1 = 0;
constexpr int L1_W2 = 32768;
constexpr int L1_X  = L1_W2 + 98304;
constexpr int L1_H0 = L1_X + GTL * 4096;
constexpr int OPITCH = 784;
constexpr int L1_GEMM_END = L1_H0 + 8192;
constexpr int K1_LDS = L1_GEMM_END > L1_EDGE_END ? L1_GEMM_END : L1_EDGE_END;
static_assert(32 * OPITCH <= 32768 && GTL == 6 && K1_LDS <= 163840, "");
__host__ __device__ constexpr int l1_h(int s) { return s == 0 ? L1_H0 : L1_X + (s - 1) * 8192; }
__host__ __device__ constexpr int l1_o(int s) { return s == 0 ? L1_W1 : L1_W2 + (s - 1) * 32768; }

__device__ __forceinline__ s16x4 tr_read(unsigned a) { return __builtin_amdgcn_ds_read_tr16_b64_v4i16((lds_s16x4_t)(size_t)a); }
__device__ __forceinline__ bf16x8 cat8(s16x4 a, s16x4 b) { return __builtin_shufflevector(a, b, 0, 1, 2, 3, 4, 5, 6, 7); }
typedef __attribute__((address_space(3))) u32x4* lds_u32x4_t;
__device__ __forceinline__ u32x4 lds_ld16(unsigned a) { return *(lds_u32x4_t)(size_t)a; }

#define PIN8(x) asm volatile("" :: "v"(x[0][0]), "v"(x[0][1]), "v"(x[0][2]), "v"(x[0][3]), "v"(x[1][0]), "v"(x[1][1]), "v"(x[1][2]), "v"(x[1][3]))
__device__ __forceinline__ void gemm_block(int g, const unsigned short* __restrict__ sv, const unsigned short* __restrict__ w1,
                                           const unsigned short* __restrict__ w2, char* __restrict__ ws) {
    const int tid = threadIdx.x;
    const int lane = tid & 63, wv = __builtin_amdgcn_readfirstlane(tid >> 6), slot = lane & 15, q = lane >> 4;
    const int w = wv & 7;
    const int n0 = g * GT;
    {
        const __amdgpu_buffer_rsrc_t rs_x = __builtin_amdgcn_make_buffer_rsrc((void*)sv, 0, NN * 1024, 0x00020000);
        const __amdgpu_buffer_rsrc_t rs_w1 = __builtin_amdgcn_make_buffer_rsrc((void*)w1, 0, 128 * 256, 0x00020000);
        const __amdgpu_buffer_rsrc_t rs_w2 = __builtin_amdgcn_make_buffer_rsrc((void*)w2, 0, 128 * 768, 0x00020000);
        const unsigned o1 = q * 256 + ((((slot >> 1) ^ (q | ((wv & 2) << 1))) << 1) | (slot & 1)) * 16;
        bdma16(rs_w1, o1, wv * 1024, L1_W1 + wv * 1024);
        bdma16(rs_w1, o1, (wv + 16) * 1024, L1_W1 + (wv + 16) * 1024);
        const unsigned xo = slot * 1024 + q * 16 + (n0 + 16 * (wv >> 2)) * 1024 + (wv & 3) * 64;
        bdma16(rs_x, xo, 0, L1_X + wv * 1024);
        if (wv < 8) bdma16(rs_x, xo + 64 * 1024, 0, L1_X + (16 + wv) * 1024);
        asm volatile("s_barrier" ::: "memory");
#pragma unroll
        for (int s3 = 0; s3 < 3; ++s3) {
            const int c = 64 * s3 + lane, row = c / 48, cp = c - 48 * row, P = cp >> 1;
            const unsigned o2 = row * 768 + ((((P & ~7) | ((P & 7) ^ ((4 * (wv & 1) + row) & 7))) << 1) | (cp & 1)) * 16;
            bdma16(rs_w2, o2, wv * 3072, L1_W2 + wv * 3072 + s3 * 1024);
            bdma16(rs_w2, o2, (wv + 16) * 3072, L1_W2 + (wv + 16) * 3072 + s3 * 1024);
        }
    }
    const int qq = slot >> 2, p4 = slot & 3;
    asm volatile("s_waitcnt vmcnt(6)\n\ts_barrier" ::: "memory");
    constexpr int NP = GTL / 2;
    const __amdgpu_buffer_rsrc_t rs_phi = __builtin_amdgcn_make_buffer_rsrc((void*)(ws + WS_PHI), 0, NN * 1024, 0x00020000);
    auto ship = [&](int s) {
#pragma unroll
        for (int k = 0; k < 3; ++k) {
            const int c = 192 * w + 64 * k + lane, r = c / 48, cp = c - 48 * r;
            __builtin_amdgcn_raw_buffer_store_b128(lds_ld16(l1_o(s) + r * OPITCH + cp * 16), rs_phi, (n0 + 32 * s + r) * 1024 + cp * 16, 0, 16);
        }
    };
    if (wv < 8) {
        bf16x8 a1[4];
        {
            const unsigned base = L1_W1 + (8 * q + qq) * 256 + (((w ^ (qq | ((q & 1) << 2))) << 1) | (p4 >> 1)) * 16 + 8 * (p4 & 1);
#pragma unroll
            for (int ks = 0; ks < 4; ++ks) a1[ks] = cat8(tr_read(base + ks * 8192), tr_read(base + ks * 8192 + 1024));
        }
        const unsigned hw = (w >> 1) * 1024 + lane * 16 + (w & 1) * 8;
#pragma unroll
        for (int s = 0; s < NP + 1; ++s) {
            if (s >= 2) ship(s - 2);
            if (s < NP) {
                u32x4 xb[2][4];
#pragma unroll
                for (int i = 0; i < 2; ++i)
#pragma unroll
                    for (int ks = 0; ks < 4; ++ks) xb[i][ks] = lds_ld16(L1_X + ((2 * s + i) * 4 + ks) * 1024 + lane * 16);
                PIN8(xb);
                f32x4 acc[2] = {{0, 0, 0, 0}, {0, 0, 0, 0}};
#pragma unroll
                for (int ks = 0; ks < 4; ++ks)
#pragma unroll
                    for (int i = 0; i < 2; ++i) acc[i] = __builtin_amdgcn_mfma_f32_16x16x32_bf16(a1[ks], __builtin_bit_cast(bf16x8, xb[i][ks]), acc[i], 0, 0, 0);
#pragma unroll
                for (int i = 0; i < 2; ++i) {
                    const unsigned x01 = pack2(acc[i][0], acc[i][1]), x23 = pack2(acc[i][2], acc[i][3]);
                    const float x[4] = {bflo(x01), bfhi(x01), bflo(x23), bfhi(x23)};
                    float v[4];
#pragma unroll
                    for (int r = 0; r < 4; ++r) v[r] = x[r] * __builtin_amdgcn_rcpf(1.0f + __builtin_amdgcn_exp2f(-1.44269504f * x[r]));
                    *(lds_u32x2w_t)(size_t)(l1_h(s) + i * 4096 + hw) = (u32x2){pack2(v[0], v[1]), pack2(v[2], v[3])};
                }
            }
            if (s == 0) asm volatile("s_waitcnt vmcnt(0)" ::: "memory");
            raw_barrier();
        }
        ship(NP - 1);
    } else {
        asm volatile("s_waitcnt vmcnt(0)" ::: "memory");
        raw_barrier();
        bf16x8 a2[3][4];
#pragma unroll
        for (int t = 0; t < 3; ++t) {
            const int P = 3 * w + t, row0 = 4 * q + qq;
            const unsigned base = L1_W2 + row0 * 768 + ((P & ~7) | ((P & 7) ^ (row0 & 7))) * 32 + (p4 >> 1) * 16 + 8 * (p4 & 1);
#pragma unroll
            for (int ks = 0; ks < 4; ++ks) a2[t][ks] = cat8(tr_read(base + ks * 24576), tr_read(base + ks * 24576 + 12288));
        }
        const unsigned obase = slot * OPITCH + (48 * w + 4 * q) * 2;
#pragma unroll
        for (int s = 1; s < NP + 1; ++s) {
            u32x4 hb[2][4];
#pragma unroll
            for (int i = 0; i < 2; ++i)
#pragma unroll
                for (int ks = 0; ks < 4; ++ks) hb[i][ks] = lds_ld16(l1_h(s - 1) + i * 4096 + ks * 1024 + lane * 16);
            PIN8(hb);
            f32x4 acc[2][3];
#pragma unroll
            for (int i = 0; i < 2; ++i)
#pragma unroll
                for (int t = 0; t < 3; ++t) acc[i][t] = (f32x4){0, 0, 0, 0};
#pragma unroll
            for (int i = 0; i < 2; ++i)
#pragma unroll
                for (int ks = 0; ks < 4; ++ks)
#pragma unroll
                    for (int t = 0; t < 3; ++t) acc[i][t] = __builtin_amdgcn_mfma_f32_16x16x32_bf16(a2[t][ks], __builtin_bit_cast(bf16x8, hb[i][ks]), acc[i][t], 0, 0, 0);
#pragma unroll
            for (int i = 0; i < 2; ++i)
#pragma unroll
                for (int t = 0; t < 3; ++t)
                    *(lds_u32x2w_t)(size_t)(l1_o(s - 1) + obase + i * 16 * OPITCH + t * 32) = (u32x2){pack2(acc[i][t][0], acc[i][t][1]), pack2(acc[i][t][2], acc[i][t][3])};
            raw_barrier();
        }
    }
}

__global__ __launch_bounds__(T1) void k1_prepare(const unsigned short* __restrict__ sv, const unsigned short* __restrict__ pos,
                                                 const int* __restrict__ src, const int* __restrict__ dst,
                                                 const unsigned short* __restrict__ freqs, const unsigned short* __restrict__ w1,
                                                 const unsigned short* __restrict__ w2, char* __restrict__ ws) {
    extern __shared__ __attribute__((aligned(16))) char smem[];
    const int b = blockIdx.x;
    if (b < NB_E) edge_block(b, pos, src, dst, freqs, ws, smem);
    else gemm_block(b - NB_E, sv, w1, w2, ws);
}

constexpr int NCH     = CAP / 64;
constexpr int NITMAX  = CAP / 2 + RN;
constexpr int L2_U    = 0;
constexpr int L2_RES  = L2_U + (CAP + 1) * 32;
constexpr int L2_CNT  = L2_RES + (RN + 1) * 512;
constexpr int L2_SLOT = L2_CNT + 32 * NCH * 2;
constexpr int L2_MISC = L2_SLOT + (NITMAX + 1) * 16;
constexpr int K2_LDS  = L2_MISC + 32;
static_assert(L2_U == 0 && L2_RES % 16 == 0 && L2_CNT % 16 == 0 && L2_SLOT % 16 == 0 && NCH == 8 && RN <= 31 && CAP * 32 <= 0x4000, "");
constexpr unsigned PAD_A = CAP * 32, PAD_B = 0x3fffu << 10;
typedef __attribute__((ext_vector_type(2))) float f32x2;
typedef __attribute__((address_space(3))) const u32x2* lds_u32x2_t;
typedef __attribute__((address_space(3))) const f32x4* lds_f32x4_t;
typedef __attribute__((address_space(3))) const unsigned* lds_u32_t;
__device__ __forceinline__ u32x2 lds_rd64(unsigned a) { return *(lds_u32x2_t)(size_t)a; }
__device__ __forceinline__ f32x4 lds_rd128(unsigned a) { return *(lds_f32x4_t)(size_t)a; }
__device__ __forceinline__ unsigned lds_rd32(unsigned a) { return *(lds_u32_t)(size_t)a; }

struct K2Ctx {
    char* smem; char* ws;
    __amdgpu_buffer_rsrc_t rs_phi, rs_sv, rs_out;
    int tid, lane, wv, h, n0, nn, hh, c2;
    unsigned lo;
};

__device__ __forceinline__ void put_record(char* smem, int p, u32x4 f, unsigned mm) {
    const float fc = bflo(f.z);
    *(f32x4*)(smem + L2_U + p * 32) = (f32x4){bfhi(f.z), bflo(f.w), bfhi(f.w), __uint_as_float(mm)};
    *(u32x2*)(smem + L2_U + p * 32 + 16) = (u32x2){pack2(bflo(f.x) * fc, bfhi(f.x) * fc), pack2(bflo(f.y) * fc, bfhi(f.y) * fc)};
}
__device__ __forceinline__ void put_record_from(char* smem, const char* ws, int p, unsigned gi) {
    const char* rp = ws + WS_REC + (size_t)gi * 20;
    put_record(smem, p, *(const u32x4a4*)rp, *(const unsigned*)(rp + 16));
}
__device__ __forceinline__ unsigned sum2(unsigned x, unsigned c) { return (x & 0xffff) + (x >> 16) + c; }

__device__ __forceinline__ void build_slots(const K2Ctx& c, int m) {
    unsigned short* cnt16 = (unsigned short*)(c.smem + L2_CNT);
    u32x2* slots = (u32x2*)(c.smem + L2_SLOT);
    unsigned mm[NCH / W2], rk[NCH / W2];
#pragma unroll
    for (int i = 0; i < NCH / W2; ++i) {
        const int p = 64 * (c.wv + W2 * i) + c.lane;
        if (64 * (c.wv + W2 * i) < m) {
            const bool valid = p < m;
            mm[i] = lds_rd32(L2_U + min(p, CAP) * 32 + 12);
            const unsigned nl = mm[i] >> 16;
            unsigned long long mk = ballot(valid);
#pragma unroll
            for (int bit = 0; bit < 5; ++bit) { const bool on = (nl >> bit) & 1; const unsigned long long bb = ballot(on); mk &= on ? bb : ~bb; }
            rk[i] = lanes_below(mk);
            if (valid && rk[i] == 0) cnt16[nl * NCH + c.wv + W2 * i] = __popcll(mk);
        }
    }
    raw_barrier();
    if (c.wv == 0 || 64 * c.wv < m) {
        const u32x4 cv = *(const u32x4*)(cnt16 + (c.lane & 31) * NCH);
        const unsigned cnt = sum2(cv[0], sum2(cv[1], sum2(cv[2], sum2(cv[3], 0))));
        const unsigned its = c.lane < c.nn ? max(1u, (cnt + 1) >> 1) : 0u;
        const unsigned inc = wave_incl_scan(its), st = inc - its;
#pragma unroll
        for (int i = 0; i < NCH / W2; ++i) {
            const int ch = c.wv + W2 * i;
            if (64 * ch < m) {
                unsigned below = 0;
#pragma unroll
                for (int d = 0; d < 4; ++d) {
                    const unsigned msk = ch >= 2 * d + 2 ? 0xffffffffu : (ch == 2 * d + 1 ? 0x0000ffffu : 0u);
                    below = sum2(cv[d] & msk, below);
                }
                const unsigned nl = mm[i] >> 16;
                const unsigned a = __shfl((2 * st + below) | (below << 12) | (its << 22), nl);
                const unsigned rank = ((a >> 12) & 1023) + rk[i];
                const unsigned e = ((rank >> 1) + 1 == (a >> 22)) ? nl + 1 : 0u;
                const int p = 64 * ch + c.lane;
                if (p < m) slots[(a & 4095) + rk[i]] = (u32x2){(unsigned)p * 32 | e, (mm[i] & 0xffff) << 10};
            }
        }
        if (c.wv == 0) {
            if (c.lane < c.nn) {
                const u32x2 pe = {PAD_A | (c.lane + 1), PAD_B};
                if ((cnt & 1) | (cnt == 0)) slots[2 * inc - 1] = pe;
                if (cnt == 0) slots[2 * st] = pe;
            } else if (c.lane >= 62) slots[2 * NITMAX + c.lane - 62] = (u32x2){PAD_A, PAD_B};
            const unsigned I = __builtin_amdgcn_readlane(inc, 63);
            unsigned bound = I;
#pragma unroll
            for (int w = 0; w < W2; ++w) {
                const unsigned long long mw = ballot((st >= w * I / W2) & (c.lane < c.nn));
                const unsigned bw = mw ? __builtin_amdgcn_readlane(st, __builtin_ctzll(mw)) : I;
                bound = c.lane == w ? bw : bound;
            }
            if (c.lane <= W2) ((unsigned*)(c.smem + L2_MISC))[1 + c.lane] = bound;
        }
    }
}
__device__ __forceinline__ void read_span(const K2Ctx& c, int& ta, int& tb) {
    const u32x2 x = {lds_rd32(L2_MISC + 4 + 4 * c.wv), lds_rd32(L2_MISC + 8 + 4 * c.wv)};
    ta = __builtin_amdgcn_readfirstlane(x[0]); tb = __builtin_amdgcn_readfirstlane(x[1]);
}

__device__ __forceinline__ float vmul(float a, float b) { float d = a * b; asm("" : "+v"(d)); return d; }
__device__ __forceinline__ float vfma(float a, float b, float c) { float d = __builtin_fmaf(a, b, c); asm("" : "+v"(d)); return d; }
__device__ __forceinline__ f32x2 unpack2(unsigned u) { return (f32x2){bflo(u), bfhi(u)}; }
__device__ __forceinline__ void edge_fma(f32x2 (&acc)[4], const float (&W)[3][2], const unsigned (&ld)[6], f32x4 hat) {
    const f32x2 mv = {vmul(bflo(ld[1]), W[1][0]), vmul(bfhi(ld[1]), W[1][1])};
    const f32x2 mr = {vmul(bflo(ld[2]), W[2][0]), vmul(bfhi(ld[2]), W[2][1])};
    acc[0] = (f32x2){vfma(bflo(ld[0]), W[0][0], acc[0][0]), vfma(bfhi(ld[0]), W[0][1], acc[0][1])};
#pragma unroll
    for (int a = 0; a < 3; ++a) {
        acc[1 + a] = __builtin_elementwise_fma(mv, unpack2(ld[3 + a]), acc[1 + a]);
        acc[1 + a] = __builtin_elementwise_fma(mr, (f32x2){hat[a], hat[a]}, acc[1 + a]);
    }
}
__device__ __forceinline__ f32x4 filter4(u32x2 g, u32x2 wb) {
    return __builtin_amdgcn_mfma_f32_4x4x4bf16_1k(__builtin_bit_cast(s16x4, g), __builtin_bit_cast(s16x4, wb), (f32x4){0.f, 0.f, 0.f, 0.f}, 0, 0, 0);
}
__device__ __forceinline__ void half_reduce(const f32x2 (&acc)[4], f32x2 (&tot)[2]) {
#pragma unroll
    for (int q = 0; q < 2; ++q) {
        const auto s0 = __builtin_amdgcn_permlane32_swap(__float_as_uint(acc[q][0]), __float_as_uint(acc[q + 2][0]), false, false);
        const auto s1 = __builtin_amdgcn_permlane32_swap(__float_as_uint(acc[q][1]), __float_as_uint(acc[q + 2][1]), false, false);
        tot[q] = (f32x2){__uint_as_float(s0[0]), __uint_as_float(s1[0])} + (f32x2){__uint_as_float(s0[1]), __uint_as_float(s1[1])};
    }
}
__device__ __forceinline__ void store_rows(const K2Ctx& c, int nl, const f32x2 (&tot)[2]) {
    const unsigned ra = L2_RES + nl * 512 + c.hh * 256 + c.c2 * 4;
#pragma unroll
    for (int q = 0; q < 2; ++q) {
        const f32x2 o = unpack2(lds_rd32(ra + q * 128)) + tot[q];
        __builtin_amdgcn_raw_buffer_store_b32(pack2(o[0], o[1]), c.rs_out, c.hh * 512 + c.lo + q * 256, (c.n0 + nl) * 1024,   2);
    }
}

struct Batch { unsigned ld[4][6]; unsigned sa[4]; unsigned ga; };
__device__ __forceinline__ void batch_load(Batch& B, const K2Ctx& c, int t4, int tb) {
    unsigned off[4];
#pragma unroll
    for (int j = 0; j < 4; ++j) {
        const int t = (t4 + j < tb) ? t4 + j : NITMAX;
        const u32x2 x = lds_rd64(L2_SLOT + t * 16 + c.hh * 8);
        B.sa[j] = x[0]; off[j] = x[1] | c.lo;
    }
    { const int t = t4 + (c.lane & 3); B.ga = lds_rd32(L2_SLOT + (t < tb ? t : NITMAX) * 16 + c.hh * 8); }
#pragma unroll
    for (int j = 0; j < 4; ++j) {
        B.ld[j][0] = __builtin_amdgcn_raw_buffer_load_b32(c.rs_phi, off[j], 0, 0);
        B.ld[j][1] = __builtin_amdgcn_raw_buffer_load_b32(c.rs_phi, off[j] + 256, 0, 0);
        B.ld[j][2] = __builtin_amdgcn_raw_buffer_load_b32(c.rs_phi, off[j] + 512, 0, 0);
        B.ld[j][3] = __builtin_amdgcn_raw_buffer_load_b32(c.rs_sv, off[j] + 256, 0, 0);
        B.ld[j][4] = __builtin_amdgcn_raw_buffer_load_b32(c.rs_sv, off[j] + 512, 0, 0);
        B.ld[j][5] = __builtin_amdgcn_raw_buffer_load_b32(c.rs_sv, off[j] + 768, 0, 0);
    }
}
__device__ __forceinline__ void batch_compute(const Batch& B, const K2Ctx& c, f32x2 (&acc)[4], const u32x2 (&wb)[3][2], bool first) {
    const u32x2 g = lds_rd64((B.ga & 0x7fe0) + 16);
    f32x4 hat[4];
#pragma unroll
    for (int j = 0; j < 4; ++j) hat[j] = lds_rd128(B.sa[j] & 0x7fe0);
    f32x4 W4[3][2];
#pragma unroll
    for (int q = 0; q < 3; ++q) { W4[q][0] = filter4(g, wb[q][0]); W4[q][1] = filter4(g, wb[q][1]); }
#pragma unroll
    for (int j = 0; j < 4; ++j) {
        const float W[3][2] = {{W4[0][0][j], W4[0][1][j]}, {W4[1][0][j], W4[1][1][j]}, {W4[2][0][j], W4[2][1][j]}};
        edge_fma(acc, W, B.ld[j], hat[j]);
        if (j == 0 && first) asm volatile("s_barrier" : "+v"(acc[0]), "+v"(acc[1]), "+v"(acc[2]), "+v"(acc[3]) :: "memory");
        const int en = __builtin_amdgcn_readfirstlane(B.sa[j]) & 31;
        if (en) {
            f32x2 tot[2];
            half_reduce(acc, tot);
            store_rows(c, en - 1, tot);
#pragma unroll
            for (int a = 0; a < 4; ++a) acc[a] = (f32x2){0.f, 0.f};
        }
    }
}

__global__ __launch_bounds__(T2, 4) void k2_gather(const unsigned short* __restrict__ sv, const unsigned short* __restrict__ wr,
                                                   char* __restrict__ ws, char* __restrict__ out) {
    extern __shared__ __attribute__((aligned(16))) char smem[];
    K2Ctx c;
    c.smem = smem; c.ws = ws;
    c.tid = threadIdx.x; c.lane = c.tid & 63; c.wv = __builtin_amdgcn_readfirstlane(c.tid >> 6);
    c.h = blockIdx.x & 1; const int r = ((blockIdx.x >> 1) & 3) * NRQ + (blockIdx.x >> 3);
    c.n0 = r * RN; c.nn = min(RN, NN - c.n0);
    c.hh = c.lane >> 5; c.c2 = c.lane & 31;
    c.lo = c.h * 128 + c.c2 * 4;
    c.rs_phi = __builtin_amdgcn_make_buffer_rsrc((void*)(ws + WS_PHI), 0, NN * 1024, 0x00020000);
    c.rs_sv = __builtin_amdgcn_make_buffer_rsrc((void*)sv, 0, NN * 1024, 0x00020000);
    c.rs_out = __builtin_amdgcn_make_buffer_rsrc((void*)out, 0, NN * 1024, 0x00020000);
    const int lane = c.lane, tid = c.tid, wv = c.wv, h = c.h, hh = c.hh, c2 = c.c2;
    int* tot_lds = (int*)(smem + L2_MISC);
    unsigned myce = 0, mycb = 0;
    const unsigned* cellp = (const unsigned*)(ws + WS_CELL) + r * 256 + (lane >> 4) * 32 + (lane & 15);
    unsigned ce0 = cellp[0], ce1 = cellp[128];
    u32x2 wb[3][2];
    {
        unsigned wraw[3][4];
#pragma unroll
        for (int p = 0; p < 3; ++p)
#pragma unroll
            for (int k = 0; k < 4; ++k) wraw[p][k] = *(const unsigned*)(wr + k * 384 + p * 128 + h * 64 + 2 * c2);
        if (wv >= 2) {
            ((unsigned*)(smem + L2_CNT))[tid - 128] = 0;
            if (tid < 130) ((f32x4*)(smem + L2_U))[2 * CAP + tid - 128] = (f32x4){0.f, 0.f, 0.f, 0.f};
            static_assert(32 * NCH * 2 == 128 * 4, "");
        }
#pragma unroll
        for (int p = 0; p < 3; ++p)
#pragma unroll
            for (int k = 0; k < 2; ++k) { wb[p][0][k] = __builtin_amdgcn_perm(wraw[p][2 * k + 1], wraw[p][2 * k], 0x05040100u); wb[p][1][k] = __builtin_amdgcn_perm(wraw[p][2 * k + 1], wraw[p][2 * k], 0x07060302u); }
        asm volatile("" : "+v"(wb[0][0]), "+v"(wb[0][1]), "+v"(wb[1][0]), "+v"(wb[1][1]), "+v"(wb[2][0]), "+v"(wb[2][1]));
    }
    if (wv < 2) {
        if (8 * (lane & 15) + (lane >> 4) >= NB_E) ce0 = 0;
        if (8 * (lane & 15) + (lane >> 4) + 4 >= NB_E) ce1 = 0;
        const unsigned cc0 = ce0 >> 16, cc1 = ce1 >> 16;
        const unsigned in0 = wave_incl_scan(cc0), t0 = __builtin_amdgcn_readlane(in0, 63);
        const unsigned in1 = wave_incl_scan(cc1) + t0;
        myce = wv == 0 ? ce0 : ce1;
        mycb = wv == 0 ? in0 - cc0 : in1 - cc1;
        if (tid == 63) *tot_lds = in1;
    }
    const unsigned mycc = myce >> 16;
    const unsigned gi0 = (8 * (tid & 15) + (tid >> 4)) * EPB + (myce & 0xffff);
    u32x4 rf[6]; unsigned rm[6];
    if (wv < 2) {
        const __amdgpu_buffer_rsrc_t rs_rec = __builtin_amdgcn_make_buffer_rsrc((void*)(ws + WS_REC), 0, NB_E * EPB * 20, 0x00020000);
#pragma unroll
        for (int k = 0; k < 6; ++k) {
            const unsigned go = ((unsigned)k < mycc && mycb + k < CAP) ? (gi0 + k) * 20 : 0x80000000u;
            asm volatile("buffer_load_dwordx4 %0, %2, %3, 0 offen\n\tbuffer_load_dword %1, %2, %3, 0 offen offset:16" : "=&v"(rf[k]), "=&v"(rm[k]) : "v"(go), "s"(rs_rec) : "memory");
        }
    }
    asm volatile("s_barrier" ::: "memory");
    if (wv >= 2) {
#pragma unroll
        for (int i4 = 0; i4 < (RN + 3) / 4; ++i4) {
            const int i = min(wv - 2 + 2 * i4, (RN + 1) / 2 - 1);
            const int node = min(c.n0 + 2 * i + hh, NN - 1);
            dma16_asm((const char*)sv + (size_t)node * 1024 + ((lane >> 3) & 3) * 256 + h * 128 + (lane & 7) * 16, L2_RES + i * 1024);
        }
    } else {
        asm volatile("s_waitcnt vmcnt(0)" : "+v"(rf[0]), "+v"(rf[1]), "+v"(rf[2]), "+v"(rf[3]), "+v"(rf[4]), "+v"(rf[5]),
                     "+v"(rm[0]), "+v"(rm[1]), "+v"(rm[2]), "+v"(rm[3]), "+v"(rm[4]), "+v"(rm[5]) :: "memory");
#pragma unroll
        for (int k = 0; k < 6; ++k) if ((unsigned)k < mycc && mycb + k < CAP) put_record(smem, mycb + k, rf[k], rm[k]);
        for (unsigned q = 6; q < mycc && mycb + q < CAP; ++q) put_record_from(smem, ws, mycb + q, gi0 + q);
    }
    __builtin_amdgcn_s_waitcnt(0xc07f);
    asm volatile("s_barrier" ::: "memory");
    const int total = __builtin_amdgcn_readfirstlane(*tot_lds);
    int ta, tb;
    build_slots(c, min(total, CAP));
    raw_barrier();
    read_span(c, ta, tb);
    f32x2 acc[4];
#pragma unroll
    for (int a = 0; a < 4; ++a) acc[a] = (f32x2){0.f, 0.f};

    if (total <= CAP) {
        Batch A;
        __builtin_amdgcn_s_setprio(3);
        const int q1 = ta + (tb - ta) / 4, q2 = ta + (tb - ta) / 2, q3 = tb - (tb - ta) / 4;
        for (int t4 = ta; t4 == ta || t4 < tb; t4 += 4) {
            if (t4 >= q3) __builtin_amdgcn_s_setprio(0); else if (t4 >= q2) __builtin_amdgcn_s_setprio(1); else if (t4 >= q1) __builtin_amdgcn_s_setprio(2);
            batch_load(A, c, t4, tb);
            batch_compute(A, c, acc, wb, t4 == ta);
        }
    } else {
        float* accw = (float*)(ws + WS_ACC);
        asm volatile("s_waitcnt vmcnt(0)\n\ts_barrier" ::: "memory");
        for (int w0 = 0;;) {
            const bool firstw = (w0 == 0), lastw = (w0 + CAP >= total);
#pragma unroll 1
            for (int t = ta; t < tb; ++t) {
                const u32x2 sl = lds_rd64(L2_SLOT + t * 16 + hh * 8);
                const unsigned off = sl[1] | c.lo;
                unsigned ld[6];
#pragma unroll
                for (int a = 0; a < 3; ++a) { ld[a] = __builtin_amdgcn_raw_buffer_load_b32(c.rs_phi, off + a * 256, 0, 0); ld[3 + a] = __builtin_amdgcn_raw_buffer_load_b32(c.rs_sv, off + 256 + a * 256, 0, 0); }
                const u32x2 g = lds_rd64((sl[0] & 0x7fe0) + 16);
                float W[3][2];
#pragma unroll
                for (int q = 0; q < 3; ++q) { W[q][0] = filter4(g, wb[q][0])[0]; W[q][1] = filter4(g, wb[q][1])[0]; }
                edge_fma(acc, W, ld, lds_rd128(sl[0] & 0x7fe0));
                const int en = __builtin_amdgcn_readfirstlane(sl[0]) & 31;
                if (en) {
                    f32x2 tot[2];
                    half_reduce(acc, tot);
                    float* aw = accw + (((size_t)(c.n0 + en - 1) * 2 + h) * 4 + 2 * hh) * 64 + 2 * c2;
                    if (!firstw) {
#pragma unroll
                        for (int q = 0; q < 2; ++q) { tot[q][0] += aw[q * 64]; tot[q][1] += aw[q * 64 + 1]; }
                    }
                    if (lastw) store_rows(c, en - 1, tot);
                    else {
#pragma unroll
                        for (int q = 0; q < 2; ++q) { aw[q * 64] = tot[q][0]; aw[q * 64 + 1] = tot[q][1]; }
                    }
#pragma unroll
                    for (int a = 0; a < 4; ++a) acc[a] = (f32x2){0.f, 0.f};
                }
            }
            w0 += CAP;
            if (w0 >= total) break;
            __syncthreads();
            if (tid < 128) ((unsigned*)(smem + L2_CNT))[tid] = 0;
            for (unsigned q = 0; q < mycc; ++q) {
                const int p = (int)(mycb + q) - w0;
                if (p >= 0 && p < CAP) put_record_from(smem, ws, p, gi0 + q);
            }
            __syncthreads();
            build_slots(c, min(CAP, total - w0));
            __syncthreads();
            read_span(c, ta, tb);
        }
    }
}

extern "C" void kernel_launch(void* const* d_in, const int* in_sizes, int n_in, void* d_out, int out_size, void* d_ws, size_t ws_size,
                              hipStream_t stream) {
    const unsigned short* sv = (const unsigned short*)d_in[0];
    const unsigned short* pos = (const unsigned short*)d_in[1];
    const int* src = (const int*)d_in[2];
    const int* dst = (const int*)d_in[3];
    const unsigned short* freqs = (const unsigned short*)d_in[4];
    const unsigned short* w1 = (const unsigned short*)d_in[5];
    const unsigned short* w2 = (const unsigned short*)d_in[6];
    const unsigned short* wr = (const unsigned short*)d_in[7];
    (void)hipFuncSetAttribute((const void*)k1_prepare, hipFuncAttributeMaxDynamicSharedMemorySize, K1_LDS);
    (void)hipFuncSetAttribute((const void*)k2_gather, hipFuncAttributeMaxDynamicSharedMemorySize, K2_LDS);
    k1_prepare<<<NB_E + NB_G, T1, K1_LDS, stream>>>(sv, pos, src, dst, freqs, w1, w2, (char*)d_ws);
    k2_gather<<<NR * 2, T2, K2_LDS, stream>>>(sv, wr, (char*)d_ws, (char*)d_out);
}
```
